```python
import torch

def _softmax(x, axis=-1):
    return torch.softmax(x, dim=axis)
def _gelu(x):
    return torch.nn.functional.gelu(x, approximate="tanh")
def _silu(x):
    return torch.nn.functional.silu(x)
def _sigmoid(x):
    return torch.sigmoid(x)
def _layernorm(x, axis=-1, eps=1e-5):
    m = x.mean(dim=axis, keepdim=True)
    v = x.var(dim=axis, keepdim=True, unbiased=False)
    return (x - m) / torch.sqrt(v + eps)
def _scatter_add(tgt, idx, src, axis=0):
    return tgt.clone().index_add_(axis, idx, src)
def _segment_sum(src, seg, n):
    out = torch.zeros((n,) + src.shape[1:], dtype=src.dtype)
    return out.index_add_(0, seg, src)
def _pad(x, pw):
    flat = tuple(v for pair in reversed(pw) for v in pair)
    return torch.nn.functional.pad(x, flat)

kernel_name = 'recurrent_scan__compressed_kv_cla__geglu_ffn__prompt'

def setup_inputs():

    _w = (lambda a: (lambda t: t.to(torch.bfloat16) if t.is_floating_point() else t)(torch.as_tensor(a)))
    def _A_setup():
        B, H, Sq, Sk, D, M = 1, 32, 2048, 2048, 128, 512
        Dc = 4 * D
        return {
            "x": _w((lambda *s: torch.randn(*s, dtype=torch.bfloat16) * 0.05)(B, Sq, M)),
            "c_kv": _w((lambda *s: torch.randn(*s, dtype=torch.bfloat16) * 0.2)(B, Sk, Dc)),
            "w_q": _w((lambda *s: torch.randn(*s, dtype=torch.bfloat16) * 3.2)(M, H, D) * (M ** -0.5)),
            "w_uk": _w((lambda *s: torch.randn(*s, dtype=torch.bfloat16) * 3.2)(Dc, H, D) * (Dc ** -0.5)),
            "w_uv": _w((lambda *s: torch.randn(*s, dtype=torch.bfloat16) * 0.2)(Dc, H, D) * (Dc ** -0.5)),
            "w_o": _w((lambda *s: torch.randn(*s, dtype=torch.bfloat16) * 0.2)(H, D, M) * ((H * D) ** -0.5)),
        }
    _A_in = _A_setup()

    def _B_setup():
        B, S, D, F = 1, 2048, 512, 1376
        return {
            "x": _w((lambda *s: torch.randn(*s, dtype=torch.bfloat16) * 0.05)(B, S, D)),
            "w_gate": _w((lambda *s: torch.randn(*s, dtype=torch.bfloat16) * 0.1)(D, F)),
            "w_up": _w((lambda *s: torch.randn(*s, dtype=torch.bfloat16) * 0.1)(D, F)),
            "w_down": _w((lambda *s: torch.randn(*s, dtype=torch.bfloat16) * 0.1)(F, D)),
        }
    _B_in = _B_setup()

    return {
        "A_x": _A_in["x"],
        "A_c_kv": _A_in["c_kv"],
        "A_w_q": _A_in["w_q"],
        "A_w_uk": _A_in["w_uk"],
        "A_w_uv": _A_in["w_uv"],
        "A_w_o": _A_in["w_o"],
        "B_x": _B_in["x"],
        "B_w_gate": _B_in["w_gate"],
        "B_w_up": _B_in["w_up"],
        "B_w_down": _B_in["w_down"],
    }

def reference(A_x, A_c_kv, A_w_q, A_w_uk, A_w_uv, A_w_o, B_x, B_w_gate, B_w_up, B_w_down):
    def _A_ref(x, c_kv, w_q, w_uk, w_uv, w_o):
        B, Sq, M = x.shape[0], x.shape[1], x.shape[2]
        Sk = c_kv.shape[1]
        H, D = w_q.shape[1], w_q.shape[2]
        q = torch.einsum("bqm,mhd->bhqd", x, w_q)
        k = torch.einsum("bkc,chd->bhkd", c_kv, w_uk)
        v = torch.einsum("bkc,chd->bhkd", c_kv, w_uv)
        scores = torch.einsum("bhqd,bhkd->bhqk", q, k) * (D ** -0.5)
        off = Sk - Sq
        qi = torch.arange(Sq)[:, None]
        kj = torch.arange(Sk)[None, :]
        cmask = kj <= (qi + off)
        scores = torch.where(cmask[None, None], scores, -1e9)
        p = _softmax(scores, axis=-1)
        ctx = torch.einsum("bhqk,bhkd->bhqd", p, v)
        return torch.einsum("bhqd,hdm->bqm", ctx, w_o)

    def _B_ref(x, w_gate, w_up, w_down):
        return (_gelu(x @ w_gate) * (x @ w_up)) @ w_down

    x = A_x
    xn = _layernorm(x, axis=-1)
    s = _A_ref(xn, A_c_kv, A_w_q, A_w_uk, A_w_uv, A_w_o)
    g = _silu(_B_ref(xn, B_w_gate, B_w_up, B_w_down))
    return x + s * g
```

```cpp
#include <hip/hip_runtime.h>
#include <stdint.h>

typedef short bf16x8 __attribute__((ext_vector_type(8)));
typedef short s16x4 __attribute__((ext_vector_type(4)));
typedef float f32x16 __attribute__((ext_vector_type(16)));
typedef float f32x4 __attribute__((ext_vector_type(4)));
typedef unsigned u32x4 __attribute__((ext_vector_type(4)));
typedef unsigned short u16;
#define LAS __attribute__((address_space(3)))
typedef LAS const char* ldsp;
#define DI __device__ __forceinline__
#define SBAR() __builtin_amdgcn_sched_barrier(0)
#define MFMA(a, b, c) __builtin_amdgcn_mfma_f32_32x32x16_bf16((a), (b), (c), 0, 0, 0)

constexpr int SEQ = 2048, DM = 512, NH = 32, HD = 128, FF = 1376, NFU = FF / 16;
constexpr size_t WS_XN = 0;
constexpr size_t WS_K = WS_XN + (size_t)SEQ * DM * 2;
constexpr size_t WS_V = WS_K + (size_t)SEQ * NH * HD * 2;
constexpr size_t WS_CTX = WS_V + (size_t)SEQ * NH * HD * 2;
constexpr size_t WS_H = WS_CTX + (size_t)SEQ * NH * HD * 2;
constexpr size_t WS_END = WS_H + (size_t)SEQ * FF * 2;

typedef float f32x2 __attribute__((ext_vector_type(2)));
typedef __bf16 bf16x2 __attribute__((ext_vector_type(2)));
DI unsigned cvtpk(float lo, float hi) { f32x2 v = {lo, hi}; bf16x2 b = __builtin_convertvector(v, bf16x2); return __builtin_bit_cast(unsigned, b); }
DI float bf_lo(unsigned w) { return __uint_as_float(w << 16); }
DI float bf_hi(unsigned w) { return __uint_as_float(w & 0xffff0000u); }
DI void dma16(unsigned voff, const void* sbase, unsigned ldsdst) {
    asm volatile("s_mov_b32 m0, %2\n\ts_nop 0\n\tglobal_load_lds_dwordx4 %0, %1" :: "v"(voff), "s"(sbase), "s"(ldsdst) : "memory");
}
DI bf16x8 lds128(ldsp p) { return *(const LAS bf16x8*)p; }
typedef short v4i16_t __attribute__((ext_vector_type(4)));
DI s16x4 ldstr(ldsp p) { return __builtin_bit_cast(s16x4, __builtin_amdgcn_ds_read_tr16_b64_v4i16((LAS v4i16_t*)p)); }
DI bf16x8 cat8(s16x4 a, s16x4 b) { return __builtin_shufflevector(a, b, 0, 1, 2, 3, 4, 5, 6, 7); }
DI u32x4 pack8(const f32x16& x, int s) {
    u32x4 w; w[0] = cvtpk(x[8 * s], x[8 * s + 1]); w[1] = cvtpk(x[8 * s + 2], x[8 * s + 3]); w[2] = cvtpk(x[8 * s + 4], x[8 * s + 5]); w[3] = cvtpk(x[8 * s + 6], x[8 * s + 7]); return w;
}
template <int N> DI void wait_vm_bar() { asm volatile("s_waitcnt vmcnt(%0) lgkmcnt(0)\n\ts_barrier" :: "i"(N) : "memory"); }

template <bool ISV>
DI void kv_loop(f32x16 (&acc)[4][2], ldsp xr0, ldsp xr1, ldsp wr, unsigned xoff0, unsigned xoff1, unsigned woff0, unsigned woff1,
                const char* xs, const char* wsrc, unsigned ldsx, unsigned ldsw) {
    bf16x8 xa[4], wa[2], xb[4], wb[2];
#define RD(X, W, slot, ks) do { _Pragma("unroll") for (int n = 0; n < 2; ++n) W[n] = cat8(ldstr(wr + (slot) * 32768 + (ks) * 8192 + n * 512), ldstr(wr + (slot) * 32768 + (ks) * 8192 + n * 512 + 256)); \
        _Pragma("unroll") for (int i = 0; i < 4; ++i) X[i] = lds128(((ks) ? xr1 : xr0) + (slot) * 32768 + i * 2048); } while (0)
#define MM(X, W) do { _Pragma("unroll") for (int i = 0; i < 4; ++i) _Pragma("unroll") for (int n = 0; n < 2; ++n) acc[i][n] = ISV ? MFMA(X[i], W[n], acc[i][n]) : MFMA(W[n], X[i], acc[i][n]); } while (0)
#define ILV() do { _Pragma("unroll") for (int g = 0; g < 8; ++g) { __builtin_amdgcn_sched_group_barrier(0x8, 1, 0); __builtin_amdgcn_sched_group_barrier(0x100, 1, 0); } } while (0)
    RD(xa, wa, 0, 0);
    SBAR();
#pragma unroll
    for (int s = 0; s < 16; ++s) {
        RD(xb, wb, s & 3, 1); MM(xa, wa); ILV(); SBAR();
        if (s < 15) {
            constexpr int dummy = 0; (void)dummy;
            if (s <= 2) wait_vm_bar<9>(); else if (s <= 12) wait_vm_bar<8>(); else if (s == 13) wait_vm_bar<4>(); else wait_vm_bar<0>();
            if (s + 4 < 16) {
                const char* xs2 = xs + (s + 4) * 64; const char* ws2 = wsrc + (size_t)(s + 4) * 32 * 8192;
                dma16(xoff0, xs2, ldsx + (s & 3) * 32768); dma16(xoff1, xs2, ldsx + (s & 3) * 32768 + 1024);
                dma16(woff0, ws2, ldsw + (s & 3) * 32768); dma16(woff1, ws2, ldsw + (s & 3) * 32768 + 1024);
            }
            SBAR();
            RD(xa, wa, (s + 1) & 3, 0); MM(xb, wb); ILV(); SBAR();
        } else { MM(xb, wb); }
    }
#undef RD
#undef MM
#undef ILV
}

__global__ __launch_bounds__(512, 2) void k0_kv(const u16* __restrict__ x, const u16* __restrict__ ckv, const u16* __restrict__ wuk, const u16* __restrict__ wuv, char* __restrict__ ws) {
    extern __shared__ __attribute__((aligned(16))) char lds[];
    const int tid = threadIdx.x, lane = tid & 63, wid = __builtin_amdgcn_readfirstlane(tid >> 6);
    const int b = blockIdx.x, h = (b & 7) * 4 + ((b >> 3) & 3), j = b >> 5;
    const int r = lane & 31, hh = lane >> 5;
    const unsigned lds0 = (unsigned)(uintptr_t)lds;
    const int lrow = b * 8 + wid;
    u32x4 xv;
    { const char* xp = (const char*)x + (size_t)lrow * 1024; unsigned vo = lane * 16;
      asm volatile("global_load_dwordx4 %0, %1, %2" : "=&v"(xv) : "v"(vo), "s"(xp) : "memory"); }
    unsigned xoff[2], woff[2];
#pragma unroll
    for (int i = 0; i < 2; ++i) { const int p = 2 * wid + i, row = 16 * p + (lane >> 2), c = (lane & 3) ^ ((row >> 2) & 3);
        xoff[i] = row * 1024 + c * 16;
        woff[i] = (8 * (p >> 2) + ((lane >> 2) & 7)) * 8192 + ((p & 1) * 64 + hh * 32 + (lane & 3) * 8) * 2; }
    const char* xs = (const char*)ckv + (size_t)j * 256 * 1024;
    const char* wsrc = (const char*)((wid & 1) ? wuv : wuk) + h * 256;
    const unsigned ldsx = lds0 + wid * 2048, ldsw = lds0 + 16384 + wid * 2048;
#pragma unroll
    for (int s = 0; s < 4; ++s) {
        dma16(xoff[0], xs + s * 64, ldsx + s * 32768); dma16(xoff[1], xs + s * 64, ldsx + s * 32768 + 1024);
        dma16(woff[0], wsrc + (size_t)s * 32 * 8192, ldsw + s * 32768); dma16(woff[1], wsrc + (size_t)s * 32 * 8192, ldsw + s * 32768 + 1024);
    }
    asm volatile("s_waitcnt vmcnt(16)" : "+v"(xv) :: "memory");
    {
        float v[8];
#pragma unroll
        for (int i = 0; i < 4; ++i) { v[2 * i] = bf_lo(xv[i]); v[2 * i + 1] = bf_hi(xv[i]); }
        float s = ((v[0] + v[1]) + (v[2] + v[3])) + ((v[4] + v[5]) + (v[6] + v[7]));
#pragma unroll
        for (int o = 1; o < 64; o <<= 1) s += __shfl_xor(s, o);
        const float mean = s * (1.0f / DM);
        float q = 0.f;
#pragma unroll
        for (int i = 0; i < 8; ++i) { v[i] -= mean; q += v[i] * v[i]; }
#pragma unroll
        for (int o = 1; o < 64; o <<= 1) q += __shfl_xor(q, o);
        const float rstd = 1.0f / sqrtf(q * (1.0f / DM) + 1e-5f);
        u32x4 w;
#pragma unroll
        for (int i = 0; i < 4; ++i) w[i] = cvtpk(v[2 * i] * rstd, v[2 * i + 1] * rstd);
        *(u32x4*)(ws + WS_XN + ((size_t)((lrow >> 5) * 32 + (lane >> 1)) * 64 + (lane & 1) * 32 + (lrow & 31)) * 16) = w;
    }
    SBAR();
    const int wk = wid & 1, wc = wid >> 1;
    const int xrow = 128 * wk + r;
    ldsp xr0 = (ldsp)lds + xrow * 64 + ((hh ^ ((r >> 2) & 3)) << 4);
    ldsp xr1 = (ldsp)lds + xrow * 64 + (((2 + hh) ^ ((r >> 2) & 3)) << 4);
    ldsp wr = (ldsp)lds + 16384 + hh * 4096 + ((lane >> 2) & 3) * 64 + ((lane >> 4) & 1) * 32 + (lane & 3) * 8 + wc * 1024;
    f32x16 acc[4][2];
#pragma unroll
    for (int i = 0; i < 4; ++i) { acc[i][0] = f32x16{}; acc[i][1] = f32x16{}; }
    wait_vm_bar<13>();
    if (wc >= 2) {
        kv_loop<true>(acc, xr0, xr1, wr, xoff[0], xoff[1], woff[0], woff[1], xs, wsrc, ldsx, ldsw);
        char* vb = ws + WS_V + (size_t)h * (SEQ * HD * 2) + lane * 16;
#pragma unroll
        for (int i = 0; i < 4; ++i)
#pragma unroll
            for (int n = 0; n < 2; ++n)
#pragma unroll
                for (int s = 0; s < 2; ++s) *(u32x4*)(vb + (size_t)(((8 * j + 4 * wk + i) * 2 + s) * 4 + 2 * (wc - 2) + n) * 1024) = pack8(acc[i][n], s);
    } else {
        kv_loop<false>(acc, xr0, xr1, wr, xoff[0], xoff[1], woff[0], woff[1], xs, wsrc, ldsx, ldsw);
        char* kb = ws + WS_K + (size_t)h * (SEQ * HD * 2) + lane * 16;
#pragma unroll
        for (int i = 0; i < 4; ++i)
#pragma unroll
            for (int n = 0; n < 2; ++n)
#pragma unroll
                for (int s = 0; s < 2; ++s) *(u32x4*)(kb + (size_t)((8 * j + 4 * wk + i) * 8 + 2 * (2 * wc + n) + s) * 1024) = pack8(acc[i][n], s);
    }
}
constexpr float QSCALE = 0.08838834764831845f * 1.4426950408889634f;
constexpr float THR = 8.f;
constexpr int PJ_WQ = 16384, PJ_G = 24576, PJ_U = PJ_G + 3072 + 128, PJ_DUMMY = PJ_U + 3072;
constexpr int LDS_QB = 131072, KA_LDS = 163840;
#define PIN(x) asm volatile("" : "+v"(x))
DI unsigned opq(unsigned x) { asm volatile("" : "+v"(x)); return x; }
DI void st16(char* sbase, unsigned voff, u32x4 v) { *(u32x4*)(sbase + voff) = v; }
DI void dma16x2(unsigned voff, const void* sbase, unsigned ldsdst) {
    asm volatile("s_mov_b32 m0, %2\n\ts_nop 0\n\tglobal_load_lds_dwordx4 %0, %1\n\tglobal_load_lds_dwordx4 %0, %1 offset:1024" :: "v"(voff), "s"(sbase), "s"(ldsdst) : "memory");
}
DI void dma16x4(unsigned voff, const void* sbase, unsigned ldsdst) {
    asm volatile("s_mov_b32 m0, %2\n\ts_nop 0\n\tglobal_load_lds_dwordx4 %0, %1\n\tglobal_load_lds_dwordx4 %0, %1 offset:1024\n\t"
                 "global_load_lds_dwordx4 %0, %1 offset:2048\n\tglobal_load_lds_dwordx4 %0, %1 offset:3072" :: "v"(voff), "s"(sbase), "s"(ldsdst) : "memory");
}
#define MX3(a, b, c) __builtin_fmaxf(__builtin_fmaxf((a), (b)), (c))
DI float half_max(float m) { auto rr = __builtin_amdgcn_permlane32_swap(__float_as_uint(m), __float_as_uint(m), false, false); return __builtin_fmaxf(__uint_as_float(rr[0]), __uint_as_float(rr[1])); }
DI float half_sum(float m) { auto rr = __builtin_amdgcn_permlane32_swap(__float_as_uint(m), __float_as_uint(m), false, false); return __uint_as_float(rr[0]) + __uint_as_float(rr[1]); }
DI void cmask(f32x16& p0, f32x16& p1, int d) {
#pragma unroll
    for (int g = 0; g < 4; ++g) {
#pragma unroll
        for (int e = 0; e < 4; ++e) { const int r = 4 * g + e, c = e + 8 * g; if (c > d) p0[r] = -__builtin_inff(); if (c + 32 > d) p1[r] = -__builtin_inff(); }
        PIN(p0); PIN(p1); asm volatile("" : "+v"(d)); SBAR();
    }
}
DI float gelu_mul(float g, float u) {
    const float z = g * (1.0f + 0.044715f * g * g) * (-2.0f * 0.7978845608028654f * 1.4426950408889634f);
    return g * u * __builtin_amdgcn_rcpf(1.0f + __builtin_amdgcn_exp2f(z));
}

__global__ __launch_bounds__(512, 2) void ka_attn(const u16* __restrict__ wq, const u16* __restrict__ wg, const u16* __restrict__ wu, char* __restrict__ ws) {
    extern __shared__ __attribute__((aligned(16))) char lds[];
    const int tid = threadIdx.x, lane = tid & 63, wid = __builtin_amdgcn_readfirstlane(tid >> 6);
    const int b = blockIdx.x, h = (b & 7) * 4 + ((b >> 3) & 3), j = b >> 5;
    const int r = lane & 31, hh = lane >> 5;
    const bool isA = wid < 4;
    const int rbA = 4 * j + (wid & 3), rbB = 60 - 4 * j + (wid & 3), rb = isA ? rbA : rbB;
    const unsigned lds0 = (unsigned)(uintptr_t)lds;
    const unsigned l16 = lane * 16;
    const char* Kh = ws + WS_K + (size_t)h * (SEQ * HD * 2);
    const char* Vh = ws + WS_V + (size_t)h * (SEQ * HD * 2);
    bf16x8 qr[8];
    {
        const int fcol = (48 * h < FF - 48) ? 48 * h : FF - 48;
        const char* xsrc = ws + WS_XN + (size_t)rb * 32768;
        const char* wsb[2]; unsigned wvo[2], wdst[2], wstep[2];
#pragma unroll
        for (int i = 0; i < 2; ++i) {
            const int p = 2 * wid + i;
            if (p < 8) { wsb[i] = (const char*)wq + h * 256; wvo[i] = (8 * (p >> 1) + ((lane >> 2) & 7)) * 8192 + ((p & 1) * 64 + hh * 32 + (lane & 3) * 8) * 2; wdst[i] = PJ_WQ + p * 1024; wstep[i] = 32 * 8192; }
            else if (p < 14) { const int q = (p - 8) % 3, isu = (p - 8) / 3, idx = 64 * q + lane, kk = idx / 6, c = idx - kk * 6;
                wsb[i] = (const char*)(isu ? wu : wg) + fcol * 2; wvo[i] = kk * (FF * 2) + c * 16; wdst[i] = (isu ? PJ_U : PJ_G) + q * 1024; wstep[i] = 32 * FF * 2; }
            else { wsb[i] = xsrc; wvo[i] = l16; wdst[i] = PJ_DUMMY; wstep[i] = 2048; }
        }
#define PJ_STAGE(s) do { dma16x2(l16, xsrc + (s) * 2048, lds0 + ((s) & 3) * 32768 + wid * 2048); \
            dma16(wvo[0], wsb[0] + (size_t)(s) * wstep[0], lds0 + ((s) & 3) * 32768 + wdst[0]); dma16(wvo[1], wsb[1] + (size_t)(s) * wstep[1], lds0 + ((s) & 3) * 32768 + wdst[1]); } while (0)
        PJ_STAGE(0); PJ_STAGE(1); PJ_STAGE(2); PJ_STAGE(3);
        const int q4 = (lane >> 2) & 3, blk = (lane >> 4) & 1, p4 = lane & 3;
        ldsp xr[2], wr[2], gr[2];
        xr[0] = (ldsp)lds + wid * 2048 + l16;
        wr[0] = (ldsp)lds + PJ_WQ + hh * 2048 + q4 * 64 + blk * 32 + p4 * 8;
        gr[0] = (ldsp)lds + (blk ? PJ_U : PJ_G) + (8 * hh + q4) * 96 + p4 * 8;
        xr[1] = xr[0] + 65536; wr[1] = wr[0] + 65536; gr[1] = gr[0] + 65536; PIN(xr[1]); PIN(wr[1]); PIN(gr[1]);
        f32x16 acc[7];
#pragma unroll
        for (int i = 0; i < 7; ++i) acc[i] = f32x16{};
        bf16x8 xa, xb, wa[7], wb[7];
#define PJ_RD(X, W, slot, ks) do { X = lds128(xr[(slot) >> 1] + ((slot) & 1) * 32768 + (ks) * 1024); \
            _Pragma("unroll") for (int n = 0; n < 4; ++n) W[n] = cat8(ldstr(wr[(slot) >> 1] + ((slot) & 1) * 32768 + (ks) * 4096 + n * 512), ldstr(wr[(slot) >> 1] + ((slot) & 1) * 32768 + (ks) * 4096 + n * 512 + 256)); \
            _Pragma("unroll") for (int n = 0; n < 3; ++n) W[4 + n] = cat8(ldstr(gr[(slot) >> 1] + ((slot) & 1) * 32768 + (ks) * 1536 + n * 32), ldstr(gr[(slot) >> 1] + ((slot) & 1) * 32768 + (ks) * 1536 + n * 32 + 384)); } while (0)
#define PJ_MM(X, W) do { _Pragma("unroll") for (int n = 0; n < 7; ++n) acc[n] = MFMA(W[n], X, acc[n]); } while (0)
#define PJ_ILV() do { _Pragma("unroll") for (int g = 0; g < 5; ++g) { __builtin_amdgcn_sched_group_barrier(0x8, 1, 0); __builtin_amdgcn_sched_group_barrier(0x100, 3, 0); } __builtin_amdgcn_sched_group_barrier(0x8, 2, 0); } while (0)
        wait_vm_bar<12>();
        PJ_RD(xa, wa, 0, 0);
        SBAR();
#pragma unroll
        for (int s = 0; s < 16; ++s) {
            PJ_RD(xb, wb, s & 3, 1); PJ_MM(xa, wa); PJ_ILV(); SBAR();
            if (s < 15) {
                if (s <= 12) wait_vm_bar<8>(); else if (s == 13) wait_vm_bar<6>(); else wait_vm_bar<4>();
                if (s + 4 < 16) PJ_STAGE(s + 4);
                else if (s == 12) dma16x2(l16, Kh + wid * 2048, lds0 + wid * 2048);
                else if (s == 13) dma16x2(l16, Kh + 16384 + wid * 2048, lds0 + 32768 + wid * 2048);
                else dma16x2(l16, Vh + wid * 2048, lds0 + 65536 + wid * 2048);
                SBAR();
                PJ_RD(xa, wa, (s + 1) & 3, 0); PJ_MM(xb, wb); PJ_ILV(); SBAR();
            } else { PJ_MM(xb, wb); }
        }
#undef PJ_STAGE
#undef PJ_RD
#undef PJ_MM
#undef PJ_ILV
#pragma unroll
        for (int n = 0; n < 4; ++n) { acc[n] *= QSCALE; qr[2 * n] = __builtin_bit_cast(bf16x8, pack8(acc[n], 0)); qr[2 * n + 1] = __builtin_bit_cast(bf16x8, pack8(acc[n], 1)); }
        if (!isA) {
#pragma unroll
            for (int s = 0; s < 8; ++s) *(LAS bf16x8*)((LAS char*)lds + LDS_QB + (wid - 4) * 8192 + s * 1024 + l16) = qr[s];
        }
#pragma unroll
        for (int n = 0; n < 3; ++n) {
            const int u = fcol / 16 + n;
            u32x4 w;
#pragma unroll
            for (int i = 0; i < 4; ++i) w[i] = cvtpk(gelu_mul(acc[4 + n][2 * i], acc[4 + n][8 + 2 * i]), gelu_mul(acc[4 + n][2 * i + 1], acc[4 + n][8 + 2 * i + 1]));
            if (u >= 3 * h) st16(ws + WS_H + ((size_t)rb * NFU + u) * 1024, l16, w);
        }
    }
    const int sw = 2 * j + 2;
    const int swA = isA ? sw : -100;
    f32x16 o[4], negm = f32x16{}, sA0, sA1, sB0, sB1;
#pragma unroll
    for (int d = 0; d < 4; ++d) o[d] = f32x16{};
    float m = 0.f, l0 = 0.f, l1 = 0.f, fsc = 1.f; bool pend = false;
    u32x4 pk0, pk1, pk2, pk3;
    bf16x8 kf[16], vf[16];
    ldsp kbase = (ldsp)lds + l16, vbase = (ldsp)lds + 65536 + l16;
    char* ctxA = ws + WS_CTX + (size_t)h * 8192 + (size_t)rbA * 262144;
    char* ctxB = ws + WS_CTX + (size_t)h * 8192 + (size_t)rbB * 262144;
#define EX2(v) __builtin_amdgcn_exp2f(v)
#define ISSUE_KV(t) do {   \
        if ((t) + 2 <= 16) { if ((t) + 2 >= sw) dma16x4(l16, Kh + (size_t)(2 * ((t) + 2) - sw) * 16384 + wid * 4096, lds0 + ((t) & 1) * 32768 + wid * 4096); \
                             else dma16x2(l16, Kh + (size_t)((t) + 2) * 16384 + wid * 2048, lds0 + ((t) & 1) * 32768 + wid * 2048); } \
        if ((t) + 1 <= 16) { if ((t) + 1 >= sw) dma16x4(l16, Vh + (size_t)(2 * ((t) + 1) - sw) * 16384 + wid * 4096, lds0 + 65536 + (((t) + 1) & 1) * 32768 + wid * 4096); \
                             else dma16x2(l16, Vh + (size_t)((t) + 1) * 16384 + wid * 2048, lds0 + 65536 + (((t) + 1) & 1) * 32768 + wid * 2048); } } while (0)
#define ROWMAX(C0, C1) ({ float a_ = MX3(C0[0], C0[1], C1[0]), b_ = MX3(C0[2], C0[3], C1[1]); a_ = MX3(a_, C1[2], C1[3]); \
        _Pragma("unroll") for (int e = 4; e < 16; e += 4) { a_ = MX3(a_, C0[e], C0[e + 1]); b_ = MX3(b_, C0[e + 2], C0[e + 3]); a_ = MX3(a_, C1[e], C1[e + 1]); b_ = MX3(b_, C1[e + 2], C1[e + 3]); } \
        half_max(__builtin_fmaxf(a_, b_)); })
    wait_vm_bar<7>();
    {
#pragma unroll
        for (int i = 0; i < 16; ++i) kf[i] = lds128(kbase + ((i & 1) * 8 + (i >> 1)) * 1024);
        sA0 = MFMA(kf[0], qr[0], negm); sA1 = MFMA(kf[1], qr[0], negm);
#pragma unroll
        for (int s = 1; s < 8; ++s) { sA0 = MFMA(kf[2 * s], qr[s], sA0); sA1 = MFMA(kf[2 * s + 1], qr[s], sA1); }
        if (isA && j == 0) cmask(sA0, sA1, rb * 32 + r - 4 * hh);
        m = __builtin_fmaxf(ROWMAX(sA0, sA1), -1e30f);
#pragma unroll
        for (int e = 0; e < 16; ++e) { sA0[e] = EX2(sA0[e] - m); sA1[e] -= m; negm[e] = -m; }
#pragma unroll
        for (int e = 0; e < 16; e += 2) { l0 += sA0[e]; l1 += sA0[e + 1]; }
        SBAR();
        wait_vm_bar<0>();
        ISSUE_KV(0);
#pragma unroll
        for (int i = 0; i < 4; ++i) kf[i] = lds128(kbase + 32768 + ((i & 1) * 8 + (i >> 1)) * 1024);
        SBAR();
    }
#define GA(i, C, P0, P1, KP, VP, PAR) do { \
        if ((i) < 12) kf[(i) + 4] = lds128(KP + (PAR) * 32768 + ((((i) + 4) & 1) * 8 + (((i) + 4) >> 1)) * 1024); else vf[(i) - 12] = lds128(VP + (1 - (PAR)) * 32768 + ((i) - 12) * 1024); \
        C = MFMA(kf[i], qr[(i) >> 1], ((i) < 2) ? negm : C); PIN(C); \
        if ((i) < 8) { P1[2 * (i)] = EX2(P1[2 * (i)]); P1[2 * (i) + 1] = EX2(P1[2 * (i) + 1]); \
            if ((i) < 4) { pk0[(i)] = cvtpk(P0[2 * (i)], P0[2 * (i) + 1]); PIN(pk0); } else { pk1[(i) - 4] = cvtpk(P0[2 * (i)], P0[2 * (i) + 1]); PIN(pk1); } PIN(P1); } \
        else { const int e_ = 2 * ((i) - 8); l0 += P1[e_]; l1 += P1[e_ + 1]; PIN(l0); PIN(l1); \
            if ((i) < 12) { pk2[(i) - 8] = cvtpk(P1[e_], P1[e_ + 1]); PIN(pk2); } else { pk3[(i) - 12] = cvtpk(P1[e_], P1[e_ + 1]); PIN(pk3); } } \
        SBAR(); } while (0)
#define PKF(i) __builtin_bit_cast(bf16x8, ((i) < 4) ? pk0 : ((i) < 8) ? pk1 : ((i) < 12) ? pk2 : pk3)
#define GB_MM(i, VP, PAR) do { if ((i) < 12) vf[(i) + 4] = lds128(VP + (1 - (PAR)) * 32768 + ((i) + 4) * 1024); o[(i) & 3] = MFMA(vf[i], PKF(i), o[(i) & 3]); PIN(o[(i) & 3]); } while (0)
#define GB(i, C0, VP, PAR, e0, e1) do { GB_MM(i, VP, PAR); \
        _Pragma("unroll") for (int e = (e0); e < (e1); ++e) C0[e] = EX2(C0[e]); PIN(C0); \
        _Pragma("unroll") for (int e = (e0) - 2; e < (e0); ++e) if (e >= 0 && (e1) > (e0)) { if (e & 1) l1 += C0[e]; else l0 += C0[e]; } PIN(l0); PIN(l1); SBAR(); } while (0)
#define STEP(C0, C1, P0, P1, t, PAR) do { \
        const bool dosw = (t) == swA; \
        if ((PAR) == 0 && dosw) { _Pragma("unroll") for (int s = 0; s < 8; ++s) qr[s] = lds128((ldsp)lds + LDS_QB + wid * 8192 + s * 1024 + opq(l16)); negm = f32x16{}; } \
        vbase += ((t) == swA + 1) ? 16384 : 0; PIN(vbase); \
        SBAR(); \
        GA(0, C0, P0, P1, kbase, vbase, PAR); GA(1, C1, P0, P1, kbase, vbase, PAR); GA(2, C0, P0, P1, kbase, vbase, PAR); GA(3, C1, P0, P1, kbase, vbase, PAR); \
        GA(4, C0, P0, P1, kbase, vbase, PAR); GA(5, C1, P0, P1, kbase, vbase, PAR); GA(6, C0, P0, P1, kbase, vbase, PAR); GA(7, C1, P0, P1, kbase, vbase, PAR); \
        GA(8, C0, P0, P1, kbase, vbase, PAR); GA(9, C1, P0, P1, kbase, vbase, PAR); GA(10, C0, P0, P1, kbase, vbase, PAR); GA(11, C1, P0, P1, kbase, vbase, PAR); \
        GA(12, C0, P0, P1, kbase, vbase, PAR); GA(13, C1, P0, P1, kbase, vbase, PAR); GA(14, C0, P0, P1, kbase, vbase, PAR); GA(15, C1, P0, P1, kbase, vbase, PAR); \
        GB_MM(0, vbase, PAR); GB_MM(1, vbase, PAR); SBAR(); \
        if ((t) == swA - 2 || (t) == swA - 1 || (t) == 16) { \
            const int tile_ = ((t) >= sw) ? 2 * (t) - sw + (isA ? 1 : 0) : (t); const int ln_ = (int)(opq(l16) >> 4); cmask(C0, C1, (((t) >= sw) ? rbB : rb) * 32 - 64 * tile_ + (ln_ & 31) - 4 * (ln_ >> 5)); } \
        GB_MM(2, vbase, PAR); GB_MM(3, vbase, PAR); \
        float rm_ = ROWMAX(C0, C1); float lfin_ = 0.f; \
        if ((PAR) == 0 && dosw) { lfin_ = l0 + l1; l0 = 0.f; l1 = 0.f; m = __builtin_fmaxf(rm_, -1e30f); \
            _Pragma("unroll") for (int e = 0; e < 16; ++e) { C0[e] -= m; C1[e] -= m; negm[e] = -m; } } \
        else if (__builtin_expect(__any(rm_ > THR), 0)) { const float dl_ = __builtin_fmaxf(rm_, 0.f); m += dl_; fsc = EX2(-dl_); l0 *= fsc; l1 *= fsc; pend = true; \
            _Pragma("unroll") for (int e = 0; e < 16; ++e) { C0[e] -= dl_; C1[e] -= dl_; negm[e] = -m; } } \
        SBAR(); \
        GB(4, C0, vbase, PAR, 0, 2); GB(5, C0, vbase, PAR, 2, 4); GB(6, C0, vbase, PAR, 4, 6); GB(7, C0, vbase, PAR, 6, 8); \
        GB(8, C0, vbase, PAR, 8, 10); GB(9, C0, vbase, PAR, 10, 12); GB(10, C0, vbase, PAR, 12, 14); GB(11, C0, vbase, PAR, 14, 16); \
        GB_MM(12, vbase, PAR); l0 += C0[14]; l1 += C0[15]; PIN(l0); PIN(l1); SBAR(); \
        GB_MM(13, vbase, PAR); SBAR(); \
        wait_vm_bar<0>(); \
        ISSUE_KV(t); \
        kbase += ((t) + 1 == swA) ? 16384 : 0; PIN(kbase); \
        if ((t) < 16) { _Pragma("unroll") for (int i = 0; i < 4; ++i) kf[i] = lds128(kbase + (1 - (PAR)) * 32768 + ((i & 1) * 8 + (i >> 1)) * 1024); } \
        SBAR(); \
        GB_MM(14, vbase, PAR); GB_MM(15, vbase, PAR); SBAR(); \
        if ((PAR) == 0 && dosw) { const float il_ = __builtin_amdgcn_rcpf(half_sum(lfin_)); const unsigned lo_ = opq(l16); \
            _Pragma("unroll") for (int d = 0; d < 4; ++d) { o[d] *= il_; \
                st16(ctxA, lo_ + (2 * d) * 1024, pack8(o[d], 0)); st16(ctxA, lo_ + (2 * d + 1) * 1024, pack8(o[d], 1)); o[d] = f32x16{}; } } \
        else if (__builtin_expect(pend, 0)) { _Pragma("unroll") for (int d = 0; d < 4; ++d) o[d] *= fsc; pend = false; } \
        SBAR(); } while (0)
    for (int t = 1; t < 16; t += 2) {
        STEP(sB0, sB1, sA0, sA1, t, 1);
        STEP(sA0, sA1, sB0, sB1, t + 1, 0);
    }
    {
        ldsp vp = vbase + ((17 == swA + 1) ? 16384 : 0);
#pragma unroll
        for (int i = 0; i < 16; ++i) vf[i] = lds128(vp + i * 1024);
#pragma unroll
        for (int e = 0; e < 16; ++e) sA1[e] = EX2(sA1[e]);
#pragma unroll
        for (int e = 0; e < 16; e += 2) { l0 += sA1[e]; l1 += sA1[e + 1]; }
        pk0 = pack8(sA0, 0); pk1 = pack8(sA0, 1); pk2 = pack8(sA1, 0); pk3 = pack8(sA1, 1);
#pragma unroll
        for (int i = 0; i < 16; ++i) o[i & 3] = MFMA(vf[i], PKF(i), o[i & 3]);
    }
    {
        const float l = half_sum(l0 + l1);
        LAS char* xo = (LAS char*)lds + wid * 8192 + l16;
        LAS float* xs = (LAS float*)((LAS char*)lds + LDS_QB) + wid * 128;
        const int give = isA ? 0 : 2;
#pragma unroll
        for (int d = 0; d < 2; ++d)
#pragma unroll
            for (int g = 0; g < 4; ++g) { const f32x16& s = isA ? o[d] : o[2 + d]; *(LAS f32x4*)(xo + (d * 4 + g) * 1024) = (f32x4){s[4 * g], s[4 * g + 1], s[4 * g + 2], s[4 * g + 3]}; }
        xs[lane] = m; xs[64 + lane] = l;
        asm volatile("s_waitcnt lgkmcnt(0)\n\ts_barrier" ::: "memory");
        const int pw = wid ^ 4;
        LAS const char* po = (LAS const char*)lds + pw * 8192 + l16;
        LAS const float* ps = (LAS const float*)((LAS char*)lds + LDS_QB) + pw * 128;
        const float m2 = ps[lane], l2 = ps[64 + lane];
        const float mm = __builtin_fmaxf(m, m2), f1 = EX2(m - mm), f2 = EX2(m2 - mm);
        const float il = __builtin_amdgcn_rcpf(l * f1 + l2 * f2), g1 = f1 * il, g2 = f2 * il;
        const int keep = 2 - give;
#pragma unroll
        for (int d = 0; d < 2; ++d) {
            f32x16 s = isA ? o[2 + d] : o[d];
#pragma unroll
            for (int g = 0; g < 4; ++g) { const f32x4 v = *(LAS const f32x4*)(po + (d * 4 + g) * 1024);
#pragma unroll
                for (int e = 0; e < 4; ++e) s[4 * g + e] = s[4 * g + e] * g1 + v[e] * g2; }
            st16(ctxB + keep * 2048, l16 + (2 * d) * 1024, pack8(s, 0));
            st16(ctxB + keep * 2048, l16 + (2 * d + 1) * 1024, pack8(s, 1));
        }
    }
#undef EX2
#undef ISSUE_KV
#undef ROWMAX
#undef GA
#undef PKF
#undef GB_MM
#undef GB
#undef STEP
}
__global__ __launch_bounds__(512, 2) void kb_out(const u16* __restrict__ x, const u16* __restrict__ wo, const u16* __restrict__ wd, const char* __restrict__ ws, u16* __restrict__ out) {
    extern __shared__ __attribute__((aligned(16))) char lds[];
    const int tid = threadIdx.x, lane = tid & 63, wid = __builtin_amdgcn_readfirstlane(tid >> 6);
    const int b = blockIdx.x, rb0 = (b & 7) * 8 + ((b >> 3) & 3) * 2, col0 = (b >> 5) * 64;
    const int hh = lane >> 5;
    const unsigned lds0 = (unsigned)(uintptr_t)lds + wid * 16384;
    const unsigned l16 = lane * 16;
    const bool isg = wid >= 6;
    const int k0 = isg ? (wid - 6) * 43 : (wid < 4 ? wid * 43 : 172 + (wid - 4) * 42);
    const int n = (wid == 4 || wid == 5) ? 42 : 43;
    const size_t astride = isg ? (size_t)NFU * 1024 : (size_t)262144;
    const char* a0 = ws + (isg ? WS_H : WS_CTX) + (size_t)rb0 * astride + (size_t)k0 * 1024;
    const char* a1 = a0 + astride;
    const char* w0 = (const char*)(isg ? wd : wo) + (size_t)k0 * 16 * 1024 + col0 * 2;
    const unsigned wvo = ((lane >> 2) & 7) * 1024 + hh * 64 + (lane & 3) * 16;
#define KB_ISSUE(i, slot) do { const int ii_ = (i) < n ? (i) : n - 1; const unsigned d_ = lds0 + (slot) * 4096; \
        dma16(l16, a0 + (size_t)ii_ * 1024, d_); dma16(l16, a1 + (size_t)ii_ * 1024, d_ + 1024); \
        dma16(wvo, w0 + (size_t)ii_ * 16384, d_ + 2048); dma16(wvo, w0 + (size_t)ii_ * 16384 + 8192, d_ + 3072); } while (0)
    KB_ISSUE(0, 0); KB_ISSUE(1, 1); KB_ISSUE(2, 2); KB_ISSUE(3, 3);
    ldsp ar = (ldsp)lds + wid * 16384 + l16;
    ldsp wr = (ldsp)lds + wid * 16384 + 2048 + (4 * hh + ((lane >> 2) & 3)) * 64 + ((lane >> 4) & 1) * 32 + (lane & 3) * 8;
    f32x16 acc[2][2];
    acc[0][0] = f32x16{}; acc[0][1] = f32x16{}; acc[1][0] = f32x16{}; acc[1][1] = f32x16{};
    for (int i = 0; i < n; ++i) {
        const int so = (i & 3) * 4096;
        asm volatile("s_waitcnt vmcnt(12)" ::: "memory");
        const bf16x8 q0 = lds128(ar + so), q1 = lds128(ar + so + 1024);
        const bf16x8 m0 = cat8(ldstr(wr + so), ldstr(wr + so + 1024)), m1 = cat8(ldstr(wr + so + 512), ldstr(wr + so + 1536));
        acc[0][0] = MFMA(m0, q0, acc[0][0]); acc[1][0] = MFMA(m0, q1, acc[1][0]);
        acc[0][1] = MFMA(m1, q0, acc[0][1]); acc[1][1] = MFMA(m1, q1, acc[1][1]);
        asm volatile("s_waitcnt lgkmcnt(0)" ::: "memory");
        KB_ISSUE(i + 4, i & 3);
    }
#undef KB_ISSUE
    asm volatile("s_waitcnt vmcnt(0)" ::: "memory");
    {
        LAS char* xo = (LAS char*)lds + wid * 16384 + l16;
#pragma unroll
        for (int qi = 0; qi < 2; ++qi)
#pragma unroll
            for (int mi = 0; mi < 2; ++mi)
#pragma unroll
                for (int g = 0; g < 4; ++g) { const f32x16& s = acc[qi][mi]; *(LAS f32x4*)(xo + ((qi * 2 + mi) * 4 + g) * 1024) = (f32x4){s[4 * g], s[4 * g + 1], s[4 * g + 2], s[4 * g + 3]}; }
    }
    const int qi = wid & 1, cg = wid >> 1, g = 2 * (cg & 1) + hh, mi = cg >> 1;
    const size_t ooff = (size_t)((rb0 + qi) * 32 + (lane & 31)) * DM + col0 + 32 * mi + 8 * g;
    const u32x4 xv = *(const u32x4*)(x + ooff);
    asm volatile("s_waitcnt lgkmcnt(0)\n\ts_barrier" ::: "memory");
    {
        LAS const char* p = (LAS const char*)lds + ((qi * 2 + mi) * 4 + g) * 1024 + (lane & 31) * 16;
        f32x4 s0 = {}, s1 = {}, g0, g1;
#pragma unroll
        for (int w = 0; w < 6; ++w) { s0 += *(LAS const f32x4*)(p + w * 16384); s1 += *(LAS const f32x4*)(p + w * 16384 + 512); }
        g0 = *(LAS const f32x4*)(p + 6 * 16384) + *(LAS const f32x4*)(p + 7 * 16384);
        g1 = *(LAS const f32x4*)(p + 6 * 16384 + 512) + *(LAS const f32x4*)(p + 7 * 16384 + 512);
        float ov[8];
#pragma unroll
        for (int e = 0; e < 8; ++e) {
            const float gg = e < 4 ? g0[e & 3] : g1[e & 3], ss = e < 4 ? s0[e & 3] : s1[e & 3];
            const float sl = gg * __builtin_amdgcn_rcpf(1.0f + __builtin_amdgcn_exp2f(gg * -1.4426950408889634f));
            const float xx = (e & 1) ? bf_hi(xv[e >> 1]) : bf_lo(xv[e >> 1]);
            ov[e] = xx + ss * sl;
        }
        u32x4 w; w[0] = cvtpk(ov[0], ov[1]); w[1] = cvtpk(ov[2], ov[3]); w[2] = cvtpk(ov[4], ov[5]); w[3] = cvtpk(ov[6], ov[7]);
        *(u32x4*)(out + ooff) = w;
    }
}
extern "C" void kernel_launch(void* const* d_in, const int* in_sizes, int n_in, void* d_out, int out_size, void* d_ws, size_t ws_size, hipStream_t stream) {
    (void)hipFuncSetAttribute((const void*)k0_kv, hipFuncAttributeMaxDynamicSharedMemorySize, 131072);
    (void)hipFuncSetAttribute((const void*)ka_attn, hipFuncAttributeMaxDynamicSharedMemorySize, KA_LDS);
    (void)hipFuncSetAttribute((const void*)kb_out, hipFuncAttributeMaxDynamicSharedMemorySize, 131072);
    k0_kv<<<256, 512, 131072, stream>>>((const u16*)d_in[0], (const u16*)d_in[1], (const u16*)d_in[3], (const u16*)d_in[4], (char*)d_ws);
    ka_attn<<<256, 512, KA_LDS, stream>>>((const u16*)d_in[2], (const u16*)d_in[7], (const u16*)d_in[8], (char*)d_ws);
    kb_out<<<256, 512, 131072, stream>>>((const u16*)d_in[0], (const u16*)d_in[5], (const u16*)d_in[9], (const char*)d_ws, (u16*)d_out);
}
```

```cpp
#include <hip/hip_runtime.h>
#include <stdint.h>


typedef short bf16x8 __attribute__((ext_vector_type(8)));
typedef short s16x4 __attribute__((ext_vector_type(4)));
typedef float f32x16 __attribute__((ext_vector_type(16)));
typedef float f32x4 __attribute__((ext_vector_type(4)));
typedef float f32x2 __attribute__((ext_vector_type(2)));
typedef __bf16 bf16x2 __attribute__((ext_vector_type(2)));
typedef unsigned u32x4 __attribute__((ext_vector_type(4)));
typedef unsigned u32x2 __attribute__((ext_vector_type(2)));
typedef unsigned short u16;
typedef unsigned long long u64;
#define LAS __attribute__((address_space(3)))
typedef LAS const char* ldsp;
#define DI __device__ __forceinline__
#define SBAR() __builtin_amdgcn_sched_barrier(0)
#define PIN(x) asm volatile("" : "+v"(x))
#define MFMA16(a, b, c) __builtin_amdgcn_mfma_f32_16x16x32_bf16((a), (b), (c), 0, 0, 0)
#define MFMA(a, b, c) __builtin_amdgcn_mfma_f32_32x32x16_bf16((a), (b), (c), 0, 0, 0)

#define MFMAG(z, a, b, c) MFMA(a, b, c)
constexpr int SEQ = 2048, DM = 512, NH = 32, HD = 128, FF = 1376, NFU = FF / 16;
constexpr unsigned WS_XN = 0;
constexpr unsigned WS_K = WS_XN + SEQ * DM * 2;
#define HPAD 0
constexpr unsigned HS = SEQ * HD * 2 + HPAD;
constexpr unsigned WS_V = WS_K + NH * HS;
constexpr unsigned WS_CTX = WS_V + NH * HS;
constexpr unsigned WS_H = WS_CTX + SEQ * NH * HD * 2;
constexpr unsigned WS_EX = WS_H + SEQ * FF * 2;
constexpr unsigned WS_FLAG = WS_EX + 64 * 262144;
constexpr unsigned WS_GEN = WS_FLAG + 6 * 256 * 128;
constexpr unsigned WS_END = WS_GEN + 128;
enum { F_LN = 0, F_KV = 1, F_H = 2, F_CA = 3, F_CB = 4, F_EX = 5 };
constexpr float QSCALE = 0.08838834764831845f * 1.4426950408889634f;
constexpr float THR = 8.f;
constexpr int PJ_WQ = 16384, PJ_G = 24576;
constexpr int LDS_QB = 131072, LDS_BYTES = 163840;

DI unsigned cvtpk(float lo, float hi) { f32x2 v = {lo, hi}; bf16x2 b = __builtin_convertvector(v, bf16x2); return __builtin_bit_cast(unsigned, b); }
DI float bf_lo(unsigned w) { return __uint_as_float(w << 16); }
DI float bf_hi(unsigned w) { return __uint_as_float(w & 0xffff0000u); }
DI unsigned opq(unsigned x) { asm volatile("" : "+v"(x)); return x; }
DI void dma16(unsigned voff, const void* sbase, unsigned ldsdst) {
    asm volatile("s_mov_b32 m0, %2\n\ts_nop 0\n\tglobal_load_lds_dwordx4 %0, %1" :: "v"(voff), "s"(sbase), "s"(ldsdst) : "memory");
}
DI void dma16c(unsigned voff, const void* sbase, unsigned ldsdst) {
    asm volatile("s_mov_b32 m0, %2\n\ts_nop 0\n\tglobal_load_lds_dwordx4 %0, %1 sc1" :: "v"(voff), "s"(sbase), "s"(ldsdst) : "memory");
}
template <int OFF> DI void dmab(unsigned voff, u32x4 srd, unsigned ldsdst) {
    asm volatile("s_mov_b32 m0, %2\n\ts_nop 0\n\tbuffer_load_dwordx4 %0, %1, 0 offen offset:%3 sc1 lds" :: "v"(voff), "s"(srd), "s"(ldsdst), "i"(OFF) : "memory");
}
template <int OFF> DI void dmab_m(unsigned voff, u32x4 srd) {
    asm volatile("buffer_load_dwordx4 %0, %1, 0 offen offset:%2 sc1 lds" :: "v"(voff), "s"(srd), "i"(OFF) : "memory");
}
template <int OFF> DI void dmaw(unsigned voff, u32x4 srd, unsigned ldsdst) {
    asm volatile("s_mov_b32 m0, %2\n\ts_nop 0\n\tbuffer_load_dwordx4 %0, %1, 0 offen offset:%3 lds" :: "v"(voff), "s"(srd), "s"(ldsdst), "i"(OFF) : "memory");
}
DI bf16x8 lds128(ldsp p) { return *(const LAS bf16x8*)p; }
typedef short v4i16_t __attribute__((ext_vector_type(4)));
DI s16x4 ldstr(ldsp p) { return __builtin_bit_cast(s16x4, __builtin_amdgcn_ds_read_tr16_b64_v4i16((LAS v4i16_t*)p)); }
DI bf16x8 cat8(s16x4 a, s16x4 b) { return __builtin_shufflevector(a, b, 0, 1, 2, 3, 4, 5, 6, 7); }
DI u32x4 pack8(const f32x16& x, int s) {
    u32x4 w; w[0] = cvtpk(x[8 * s], x[8 * s + 1]); w[1] = cvtpk(x[8 * s + 2], x[8 * s + 3]); w[2] = cvtpk(x[8 * s + 4], x[8 * s + 5]); w[3] = cvtpk(x[8 * s + 6], x[8 * s + 7]); return w;
}
DI void mm16(f32x4& c, bf16x8 a, bf16x8 b) { asm volatile("v_mfma_f32_16x16x32_bf16 %0, %1, %2, %0" : "+v"(c) : "v"(a), "v"(b)); }
DI void mm16z(f32x4& c, bf16x8 a, bf16x8 b) { asm volatile("v_mfma_f32_16x16x32_bf16 %0, %1, %2, 0" : "=&v"(c) : "v"(a), "v"(b)); }
DI u32x4 pack2(f32x4 a, f32x4 b) { return (u32x4){cvtpk(a[0], a[1]), cvtpk(a[2], a[3]), cvtpk(b[0], b[1]), cvtpk(b[2], b[3])}; }
DI u32x4 swapT(u32x4 w) { auto r0 = __builtin_amdgcn_permlane32_swap(w[0], w[2], false, false); auto r1 = __builtin_amdgcn_permlane32_swap(w[1], w[3], false, false);
    return (u32x4){r0[0], r1[0], r0[1], r1[1]}; }
DI u32x4 packT(f32x4 a, f32x4 b) { return swapT(pack2(a, b)); }
template <int N> DI void wait_vm_bar() { asm volatile("s_waitcnt vmcnt(%0) lgkmcnt(0)\n\ts_barrier" :: "i"(N) : "memory"); }
template <int N> DI void wait_vm() { asm volatile("s_waitcnt vmcnt(%0)" :: "i"(N) : "memory"); }
DI void st16(u32x4 r, unsigned voff, unsigned soff, u32x4 v) {
    asm volatile("s_nop 4\n\tbuffer_store_dwordx4 %0, %1, %2, %3 offen sc1\n\ts_nop 1" :: "v"(v), "v"(voff), "s"(r), "s"(soff) : "memory");
}
DI void st16kv(u32x4 r, unsigned voff, unsigned soff, u32x4 v) {
    st16(r, voff, soff, v);
}
DI void st_flag(u32x4 r, unsigned soff, u64 tag) { const u32x2 v = {(unsigned)tag, (unsigned)(tag >> 32)};
    asm volatile("s_nop 4\n\tbuffer_store_dwordx2 %0, off, %1, %2 sc1" :: "v"(v), "s"(r), "s"(soff) : "memory");
}
DI u32x2 poll_issue(const char* sbase, unsigned voff) { u32x2 v; asm volatile("s_nop 4\n\tglobal_load_dwordx2 %0, %1, %2 sc1" : "=&v"(v) : "v"(voff), "s"(sbase) : "memory"); return v; }
DI void poll_reissue(u32x2& v, const char* sbase, unsigned voff) { asm volatile("s_nop 4\n\tglobal_load_dwordx2 %0, %1, %2 sc1" : "+v"(v) : "v"(voff), "s"(sbase) : "memory"); }
DI void poll_check(u32x2 v, const char* sbase, unsigned voff, u64 tag) {
    asm volatile("" : "+v"(v));
    const unsigned tl = (unsigned)tag, th = (unsigned)(tag >> 32);
    if (__builtin_expect(!__all(v[0] == tl && v[1] == th), 0)) {
        for (int it = 0; it < 400000; ++it) {
            __builtin_amdgcn_s_sleep(1);
            asm volatile("s_nop 4\n\tglobal_load_dwordx2 %0, %1, %2 sc1\n\ts_waitcnt vmcnt(0)" : "=&v"(v) : "v"(voff), "s"(sbase) : "memory");
            if (__all(v[0] == tl && v[1] == th)) break;
        }
    }
    asm volatile("" ::: "memory");
}
DI void bcast16(f32x16& t, float v, unsigned l16) { const float one = opq(l16) < 512u ? 1.f : 0.f;
    asm volatile("s_nop 1\n\tv_mfma_f32_32x32x2_f32 %0, %1, %2, 0\n\ts_nop 1" : "+v"(t) : "v"(one), "v"(v)); }
#define MX3(a, b, c) __builtin_fmaxf(__builtin_fmaxf((a), (b)), (c))
DI float half_max(float m) { auto rr = __builtin_amdgcn_permlane32_swap(__float_as_uint(m), __float_as_uint(m), false, false); return __builtin_fmaxf(__uint_as_float(rr[0]), __uint_as_float(rr[1])); }
DI float dot2(unsigned p, float acc) { return __builtin_amdgcn_fdot2_f32_bf16(__builtin_bit_cast(bf16x2, p), __builtin_bit_cast(bf16x2, 0x3f803f80u), acc, false); }
DI f32x2 pkadd(f32x2 a, f32x2 b) { f32x2 r; asm("v_pk_add_f32 %0, %1, %2" : "=v"(r) : "v"(a), "v"(b)); return r; }
DI float max3(float a, float b, float c) { float r; asm("v_max3_f32 %0, %1, %2, %3" : "=v"(r) : "v"(a), "v"(b), "v"(c)); return r; }
DI float half_max2(float a, float b) {
    float r, s; asm("v_max_f32 %0, %2, %3\n\tv_max_f32 %1, %2, %3\n\ts_nop 1\n\tv_permlane32_swap_b32 %0, %1\n\ts_nop 0\n\tv_max_f32 %0, %0, %1" : "=&v"(r), "=&v"(s) : "v"(a), "v"(b)); return r; }
template <int CTRL> DI float dpp_add(float v) { return v + __uint_as_float(__builtin_amdgcn_update_dpp(0u, __float_as_uint(v), CTRL, 0xf, 0xf, true)); }
DI float wave_sum(float v) {
    v = dpp_add<0xB1>(v); v = dpp_add<0x4E>(v); v = dpp_add<0x141>(v); v = dpp_add<0x140>(v);
    { auto r = __builtin_amdgcn_permlane16_swap(__float_as_uint(v), __float_as_uint(v), false, false); v = __uint_as_float(r[0]) + __uint_as_float(r[1]); }
    { auto r = __builtin_amdgcn_permlane32_swap(__float_as_uint(v), __float_as_uint(v), false, false); v = __uint_as_float(r[0]) + __uint_as_float(r[1]); }
    return v;
}
DI float half_sum(float m) { auto rr = __builtin_amdgcn_permlane32_swap(__float_as_uint(m), __float_as_uint(m), false, false); return __uint_as_float(rr[0]) + __uint_as_float(rr[1]); }
DI void tri(f32x16& p, int d) {
    const float ninf = -__builtin_inff();
#pragma unroll
    for (int g = 0; g < 4; ++g) {
        u64 m0_, m1_, m2_, m3_;
        asm volatile("v_cmp_gt_i32_e64 %4, %10, %8\n\tv_cmp_gt_i32_e64 %5, %11, %8\n\tv_cmp_gt_i32_e64 %6, %12, %8\n\tv_cmp_gt_i32_e64 %7, %13, %8\n\t"
                     "v_cndmask_b32_e64 %0, %0, %9, %4\n\tv_cndmask_b32_e64 %1, %1, %9, %5\n\tv_cndmask_b32_e64 %2, %2, %9, %6\n\tv_cndmask_b32_e64 %3, %3, %9, %7"
                     : "+v"(p[4 * g]), "+v"(p[4 * g + 1]), "+v"(p[4 * g + 2]), "+v"(p[4 * g + 3]), "=&s"(m0_), "=&s"(m1_), "=&s"(m2_), "=&s"(m3_)
                     : "v"(d), "v"(ninf), "i"(8 * g), "i"(8 * g + 1), "i"(8 * g + 2), "i"(8 * g + 3));
    }
}
DI void cmask(f32x16& p0, f32x16& p1, int d0, unsigned l16) {
    const int ln = (int)(opq(l16) >> 4), d = (ln & 31) - 4 * (ln >> 5);
    asm volatile("s_nop 7\n\ts_nop 3");
    tri(p0, d0 == 0 ? d : (d0 > 0 ? 1000 : -1000));
    tri(p1, d0 == 32 ? d : (d0 > 32 ? 1000 : -1000));
}
#define MEMF() asm volatile("" ::: "memory")
DI float gelu_mul(float g, float u) {
    const float z = g * (1.0f + 0.044715f * g * g) * (-2.0f * 0.7978845608028654f * 1.4426950408889634f);
    return g * u * __builtin_amdgcn_rcpf(1.0f + __builtin_amdgcn_exp2f(z));
}
#define EX2(v) __builtin_amdgcn_exp2f(v)

DI void warm(unsigned voff, const char* sbase, unsigned ldsdst) {
    asm volatile("s_mov_b32 m0, %2\n\ts_nop 3\n\tglobal_load_lds_dword %0, %1" :: "v"(voff), "s"(sbase), "s"(ldsdst) : "memory");
}
DI void warmb(unsigned voff, const char* base, unsigned nbytes, unsigned ldsdst) {
    const u32x4 srd = {(unsigned)(uintptr_t)base, (unsigned)((u64)(uintptr_t)base >> 32), nbytes, 0x00020000u};
    asm volatile("s_mov_b32 m0, %2\n\ts_nop 3\n\tbuffer_load_dword %0, %1, 0 offen lds" :: "v"(voff), "s"(srd), "s"(ldsdst) : "memory");
}
DI void warmv(unsigned& junk, unsigned voff, const char* base, unsigned nbytes) {
    const u32x4 srd = {(unsigned)(uintptr_t)base, (unsigned)((u64)(uintptr_t)base >> 32), nbytes, 0x00020000u};
    asm volatile("s_nop 4\n\tbuffer_load_dword %0, %1, %2, 0 offen ; WARMV" : "+v"(junk) : "v"(voff), "s"(srd) : "memory");
}
DI bf16x8 gload(const char* sbase, unsigned voff) { bf16x8 v; asm volatile("s_nop 4\n\tglobal_load_dwordx4 %0, %1, %2 sc1" : "=&v"(v) : "v"(voff), "s"(sbase) : "memory"); return v; }

__global__ __launch_bounds__(512, 2) void fused_kernel(const u16* __restrict__ x, const u16* __restrict__ ckv, const u16* __restrict__ wq, const u16* __restrict__ wuk,
                                                       const u16* __restrict__ wuv, const u16* __restrict__ wo, const u16* __restrict__ wg, const u16* __restrict__ wu,
                                                       const u16* __restrict__ wd, char* ws, u16* __restrict__ out) {
    extern __shared__ __attribute__((aligned(16))) char lds[];
    const int tid = threadIdx.x, lane = tid & 63, wid = __builtin_amdgcn_readfirstlane(tid >> 6);
    const int b = blockIdx.x, h = (b & 7) * 4 + ((b >> 3) & 3), j = b >> 5;
    const int hh = lane >> 5;
    const unsigned lds0 = (unsigned)(uintptr_t)lds;
    const unsigned l16 = lane * 16;
    const bool isA = wid < 4;
    const int rbA = 4 * j + (wid & 3), rbB = 60 - 4 * j + (wid & 3), rb = isA ? rbA : rbB;
    const u32x4 wsr = {(unsigned)(uintptr_t)ws, (unsigned)((u64)(uintptr_t)ws >> 32), WS_END, 0x00020000u};
    const char* fbase = ws + WS_FLAG;
    asm volatile("" :: "s"(x), "s"(ckv), "s"(wq), "s"(wuk), "s"(wuv), "s"(wo), "s"(wg), "s"(wu), "s"(wd), "s"(ws), "s"(out));
#define KV_OFFS(lane) const int xrow_ = 64 * (wid & 3) + (lane >> 2); \
    const unsigned xoff = xrow_ * 1024 + ((lane & 3) ^ ((0 - (xrow_ >> 2)) & 3)) * 16; \
    const unsigned woff = (8 * (wid & 3) + ((lane >> 2) & 7)) * 8192 + (lane >> 5) * 64 + ((lane & 3) ^ ((wid & 1) * 2)) * 16
    const char* xs = (const char*)ckv + j * 256 * 1024;
    const char* wks = (const char*)wuk + h * 256; const char* wvs = (const char*)wuv + h * 256;
    const unsigned ldsk = lds0 + (wid & 3) * 4096;
    const int gw_ = b * 4 + (wid & 3), g2_ = gw_ & 255, sel_ = gw_ >> 8;
    u32x4 xv;
    asm volatile("global_load_dwordx4 %0, %1, %2" : "=&v"(xv) : "v"(l16), "s"((const char*)x + (b * 8 + wid) * 1024) : "memory");
    if (isA) {
        KV_OFFS(lane);
        dma16(xoff, xs + 0, ldsk + 0);
        dma16(xoff, xs + 16384, ldsk + 1024);
        dma16(xoff, xs + 32768, ldsk + 2048);
        dma16(xoff, xs + 49152, ldsk + 3072);
        dma16(woff, wks + 0, ldsk + 16384);
        dma16(woff, wks + 128, ldsk + 17408);
        dma16(woff, wvs + 0, ldsk + 18432);
        dma16(woff, wvs + 128, ldsk + 19456);
        dma16(xoff, xs + 64, ldsk + 32768);
        dma16(xoff, xs + 16448, ldsk + 33792);
        dma16(xoff, xs + 32832, ldsk + 34816);
        dma16(xoff, xs + 49216, ldsk + 35840);
        dma16(woff, wks + 262144, ldsk + 49152);
        dma16(woff, wks + 262272, ldsk + 50176);
        dma16(woff, wvs + 262144, ldsk + 51200);
        dma16(woff, wvs + 262272, ldsk + 52224);
        dma16(xoff, xs + 128, ldsk + 65536);
        dma16(xoff, xs + 16512, ldsk + 66560);
        dma16(xoff, xs + 32896, ldsk + 67584);
        dma16(xoff, xs + 49280, ldsk + 68608);
        dma16(woff, wks + 524288, ldsk + 81920);
        dma16(woff, wks + 524416, ldsk + 82944);
        dma16(woff, wvs + 524288, ldsk + 83968);
        dma16(woff, wvs + 524416, ldsk + 84992);
        dma16(xoff, xs + 192, ldsk + 98304);
        dma16(xoff, xs + 16576, ldsk + 99328);
        dma16(xoff, xs + 32960, ldsk + 100352);
        dma16(xoff, xs + 49344, ldsk + 101376);
        dma16(woff, wks + 786432, ldsk + 114688);
        dma16(woff, wks + 786560, ldsk + 115712);
        dma16(woff, wvs + 786432, ldsk + 116736);
        dma16(woff, wvs + 786560, ldsk + 117760);
    } else {
        {
            u64 pc_; const int csz_ = 73 * 1024;
            asm volatile("s_getpc_b64 %0" : "=s"(pc_));
            const int co_ = ((b >> 3) * 4 + (wid & 3)) * 8192, cl_ = csz_ - co_;
            warmb(lane * 128, (const char*)pc_ + co_, (unsigned)__builtin_amdgcn_readfirstlane(cl_ > 0 ? cl_ : 0), lds0 + LDS_QB + 16384);
        }
        if (gw_ >= 768) warm(lane * 128, (const char*)ckv + (gw_ - 768) * 8192, lds0 + LDS_QB + 16384);
    }
    SBAR();
    u64 gen;
    asm volatile("s_load_dwordx2 %0, %1, 0x0" : "=s"(gen) : "s"(ws + WS_GEN) : "memory");
    u32x2 pv = {0u, 0u};
    u32x4 held[16]; unsigned wjunk = 0;
    const unsigned ldsx = lds0 + wid * 2048;
    u64 tag;
    const int wk = wid & 1, wc = wid >> 1;
    const int fcol = (48 * h < FF - 48) ? 48 * h : FF - 48;
    const char* xsrc0 = ws + WS_XN + rbA * 32768; const char* xsrc1 = ws + WS_XN + rbB * 32768;
    unsigned kvoff = l16 + wid * 4096; const unsigned lds0w = lds0 + wid * 4096;
    u64 kad = (u64)(uintptr_t)(ws + WS_K + h * HS), vad = (u64)(uintptr_t)(ws + WS_V + h * HS);
    u32x4 ksrd = (u32x4){(unsigned)kad, (unsigned)(kad >> 32), 16384u, 0x00020000u}, vsrd = (u32x4){(unsigned)vad, (unsigned)(vad >> 32), 16384u, 0x00020000u};
    bf16x8 qr[8];
    f32x4 pacc[8][4];
    f32x16 sA0, sA1;
    bf16x8 kf[16];
    {
        const char* wp0 = gw_ < 768 ? (const char*)(gw_ < 384 ? wuk : wuv) + (128 + (gw_ < 384 ? gw_ : gw_ - 384)) * 8192 : (const char*)ckv + (gw_ - 768) * 8192;
        const int w0st = gw_ < 768 ? 4 + ((gw_ < 384 ? gw_ : gw_ - 384) >> 5) : 99;
        const char* wp1 = gw_ < 512 ? (const char*)wq + gw_ * 8192 : (const char*)(sel_ == 2 ? wg : wu) + (g2_ < 172 ? g2_ : 171) * 8192;
        const char* wp2 = gw_ < 512 ? (const char*)wo + gw_ * 8192 : (const char*)wd + ((gw_ & 511) < 172 ? (gw_ & 511) : 171) * 8192;
        if (!isA) { if (gw_ >= 768) wait_vm<2>(); else wait_vm<1>(); }
        asm volatile("s_waitcnt vmcnt(32)" : "+v"(xv) :: "memory");
        {
            float v[8];
#pragma unroll
            for (int i = 0; i < 4; ++i) { v[2 * i] = bf_lo(xv[i]); v[2 * i + 1] = bf_hi(xv[i]); }
            float s = ((v[0] + v[1]) + (v[2] + v[3])) + ((v[4] + v[5]) + (v[6] + v[7]));
            s = wave_sum(s);
            const float mean = s * (1.0f / DM);
            float q = 0.f;
#pragma unroll
            for (int i = 0; i < 8; ++i) { v[i] -= mean; q += v[i] * v[i]; }
            q = wave_sum(q);
            const float rstd = 1.0f / sqrtf(q * (1.0f / DM) + 1e-5f);
            u32x4 w;
#pragma unroll
            for (int i = 0; i < 4; ++i) w[i] = cvtpk(v[2 * i] * rstd, v[2 * i + 1] * rstd);
            *(LAS u32x4*)((LAS char*)lds + LDS_QB + wid * 1024 + l16) = w;
        }
        SBAR();
#define KV_SETUP() const int lane = (int)(opq(l16) >> 4), hh = lane >> 5, g4 = lane >> 4, q4 = (lane >> 2) & 3, p4 = lane & 3; (void)hh; \
        ldsp xr[2], wr0[2], wr1[2]; \
        xr[0] = (ldsp)lds + (128 * wk + (lane & 15)) * 64 + ((g4 ^ ((0 - q4) & 3)) << 4); \
        wr0[0] = (ldsp)lds + 16384 + g4 * 4096 + wc * 1024 + q4 * 64 + (g4 & 1) * 32 + p4 * 8; \
        wr1[0] = (ldsp)lds + 16384 + g4 * 4096 + wc * 1024 + q4 * 64 + (1 - (g4 & 1)) * 32 + p4 * 8; \
        xr[1] = xr[0] + 65536; wr0[1] = wr0[0] + 65536; wr1[1] = wr1[0] + 65536; PIN(xr[1]); PIN(wr0[1]); PIN(wr1[1]); \
        const unsigned kvo = (g4 >> 1) * 1024 + (g4 & 1) * 512 + (lane & 15) * 16; \
        LAS char* hand = (LAS char*)lds + LDS_QB + (wid & 3) * 8192 + lane * 16
        f32x4 acc[8][4];
        bf16x8 xf[4]; s16x4 wl[2][4], wh[2][4];
        unsigned kst = WS_K + (wc & 1) * 4096 + h * HS + (8 * j + 4 * wk) * 8192, vst = WS_V + (wc & 1) * 2048 + h * HS + (8 * j + 4 * wk) * 8192;
        ldsp pxr[2], pw0[2], pw1[2], pw2[2];
        bf16x8 px[4]; s16x4 pl[4], ph[4]; u32x4 hb[4];
#define PJ_SETUP(W0, W1, W2) do { pxr[0] = (ldsp)lds + (wid & 3) * 2048 + kvo; pw0[0] = (ldsp)lds + (W0); pw1[0] = (ldsp)lds + (W1); pw2[0] = (ldsp)lds + (W2); \
        pxr[1] = pxr[0] + 65536; pw0[1] = pw0[0] + 65536; pw1[1] = pw1[0] + 65536; pw2[1] = pw2[0] + 65536; PIN(pxr[1]); PIN(pw0[1]); PIN(pw1[1]); PIN(pw2[1]); } while (0)
        wait_vm_bar<24>();
        asm volatile("" : "+s"(gen));
        { u64 z = gen + 0x9E3779B97F4A7C15ull; z = (z ^ (z >> 30)) * 0xBF58476D1CE4E5B9ull; z = (z ^ (z >> 27)) * 0x94D049BB133111EBull; tag = z ^ (z >> 31); }
        if (isA) {
        KV_SETUP();
        KV_OFFS(lane);
    const char* psb[4]; unsigned pvo[4], pdst[4], pst[4];
#pragma unroll
    for (int i = 0; i < 4; ++i) {
        const int p = 4 * (wid & 3) + i;
        if (p < 8) { psb[i] = (const char*)wq + h * 256; pvo[i] = (8 * (p >> 1) + ((lane >> 2) & 7)) * 8192 + (p & 1) * 128 + hh * 64 + ((lane & 3) ^ (p & 2)) * 16; pst[i] = 32 * 8192; }
        else { const int row = 8 * (p & 3) + (lane >> 3), u = ((lane >> 1) & 3) ^ (((row >> 1) & 1) | ((row >> 2) & 2));
            psb[i] = (const char*)(p >= 12 ? wu : wg) + fcol * 2; pvo[i] = row * (FF * 2) + (u == 3 ? 0 : u) * 32 + (lane & 1) * 16; pst[i] = 32 * FF * 2; }
        pdst[i] = PJ_WQ + p * 1024;
    }
    const unsigned pvoff_ln = (unsigned)(4 * ((lane & 4) ? rbB : rbA) + (lane & 3)) * 128u;
    const unsigned pvoff_kv = (unsigned)((b & 31) + 32 * (lane & 7)) * 128u;
        wl[0][0] = ldstr(wr0[0] + 0); wh[0][0] = ldstr(wr0[0] + 0 + 256);
        xf[0] = lds128(xr[0] + 0);
        wl[0][1] = ldstr(wr1[0] + 0); wh[0][1] = ldstr(wr1[0] + 0 + 256);
        wl[0][2] = ldstr(wr0[0] + 512); wh[0][2] = ldstr(wr0[0] + 512 + 256);
        wl[0][3] = ldstr(wr1[0] + 512); wh[0][3] = ldstr(wr1[0] + 512 + 256);
        xf[1] = lds128(xr[0] + 1024);
        SBAR();
        xf[2] = lds128(xr[0] + 2048);
        mm16z(acc[0][0], cat8(wl[0][0], wh[0][0]), xf[0]);
        mm16z(acc[0][1], cat8(wl[0][1], wh[0][1]), xf[0]);
        SBAR();
        mm16z(acc[0][2], cat8(wl[0][2], wh[0][2]), xf[0]);
        mm16z(acc[0][3], cat8(wl[0][3], wh[0][3]), xf[0]);
        SBAR();
        xf[3] = lds128(xr[0] + 3072);
        mm16z(acc[1][0], cat8(wl[0][0], wh[0][0]), xf[1]);
        mm16z(acc[1][1], cat8(wl[0][1], wh[0][1]), xf[1]);
        SBAR();
        mm16z(acc[1][2], cat8(wl[0][2], wh[0][2]), xf[1]);
        mm16z(acc[1][3], cat8(wl[0][3], wh[0][3]), xf[1]);
        SBAR();
        xf[0] = lds128(xr[0] + 4096);
        mm16z(acc[2][0], cat8(wl[0][0], wh[0][0]), xf[2]);
        mm16z(acc[2][1], cat8(wl[0][1], wh[0][1]), xf[2]);
        SBAR();
        mm16z(acc[2][2], cat8(wl[0][2], wh[0][2]), xf[2]);
        mm16z(acc[2][3], cat8(wl[0][3], wh[0][3]), xf[2]);
        SBAR();
        xf[1] = lds128(xr[0] + 5120);
        mm16z(acc[3][0], cat8(wl[0][0], wh[0][0]), xf[3]);
        mm16z(acc[3][1], cat8(wl[0][1], wh[0][1]), xf[3]);
        SBAR();
        mm16z(acc[3][2], cat8(wl[0][2], wh[0][2]), xf[3]);
        mm16z(acc[3][3], cat8(wl[0][3], wh[0][3]), xf[3]);
        SBAR();
        xf[2] = lds128(xr[0] + 6144);
        mm16z(acc[4][0], cat8(wl[0][0], wh[0][0]), xf[0]);
        mm16z(acc[4][1], cat8(wl[0][1], wh[0][1]), xf[0]);
        SBAR();
        mm16z(acc[4][2], cat8(wl[0][2], wh[0][2]), xf[0]);
        mm16z(acc[4][3], cat8(wl[0][3], wh[0][3]), xf[0]);
        SBAR();
        xf[3] = lds128(xr[0] + 7168);
        mm16z(acc[5][0], cat8(wl[0][0], wh[0][0]), xf[1]);
        mm16z(acc[5][1], cat8(wl[0][1], wh[0][1]), xf[1]);
        SBAR();
        mm16z(acc[5][2], cat8(wl[0][2], wh[0][2]), xf[1]);
        mm16z(acc[5][3], cat8(wl[0][3], wh[0][3]), xf[1]);
        SBAR();
        mm16z(acc[6][0], cat8(wl[0][0], wh[0][0]), xf[2]);
        mm16z(acc[6][1], cat8(wl[0][1], wh[0][1]), xf[2]);
        SBAR();
        wait_vm_bar<16>();
        wl[1][0] = ldstr(wr0[0] + 32768); wh[1][0] = ldstr(wr0[0] + 32768 + 256); xf[0] = lds128(xr[0] + 32768); wl[1][1] = ldstr(wr1[0] + 32768); wh[1][1] = ldstr(wr1[0] + 32768 + 256);
        mm16z(acc[6][2], cat8(wl[0][2], wh[0][2]), xf[2]);
        mm16z(acc[6][3], cat8(wl[0][3], wh[0][3]), xf[2]);
        dma16(xoff, xs + 256, ldsk + 0);
        SBAR();
        wl[1][2] = ldstr(wr0[0] + 33280); wh[1][2] = ldstr(wr0[0] + 33280 + 256); wl[1][3] = ldstr(wr1[0] + 33280); wh[1][3] = ldstr(wr1[0] + 33280 + 256);
        mm16z(acc[7][0], cat8(wl[0][0], wh[0][0]), xf[3]);
        mm16z(acc[7][1], cat8(wl[0][1], wh[0][1]), xf[3]);
        dma16(xoff, xs + 16640, ldsk + 1024);
        SBAR();
        xf[1] = lds128(xr[0] + 33792);
        mm16z(acc[7][2], cat8(wl[0][2], wh[0][2]), xf[3]);
        mm16z(acc[7][3], cat8(wl[0][3], wh[0][3]), xf[3]);
        dma16(xoff, xs + 33024, ldsk + 2048);
        SBAR();
        xf[2] = lds128(xr[0] + 34816);
        mm16(acc[0][0], cat8(wl[1][0], wh[1][0]), xf[0]);
        mm16(acc[0][1], cat8(wl[1][1], wh[1][1]), xf[0]);
        dma16(xoff, xs + 49408, ldsk + 3072);
        SBAR();
        mm16(acc[0][2], cat8(wl[1][2], wh[1][2]), xf[0]);
        mm16(acc[0][3], cat8(wl[1][3], wh[1][3]), xf[0]);
        dma16(woff, wks + 1048576, ldsk + 16384);
        SBAR();
        xf[3] = lds128(xr[0] + 35840);
        mm16(acc[1][0], cat8(wl[1][0], wh[1][0]), xf[1]);
        mm16(acc[1][1], cat8(wl[1][1], wh[1][1]), xf[1]);
        dma16(woff, wks + 1048704, ldsk + 17408);
        SBAR();
        mm16(acc[1][2], cat8(wl[1][2], wh[1][2]), xf[1]);
        mm16(acc[1][3], cat8(wl[1][3], wh[1][3]), xf[1]);
        dma16(woff, wvs + 1048576, ldsk + 18432);
        SBAR();
        xf[0] = lds128(xr[0] + 36864);
        mm16(acc[2][0], cat8(wl[1][0], wh[1][0]), xf[2]);
        mm16(acc[2][1], cat8(wl[1][1], wh[1][1]), xf[2]);
        dma16(woff, wvs + 1048704, ldsk + 19456);
        SBAR();
        mm16(acc[2][2], cat8(wl[1][2], wh[1][2]), xf[2]);
        mm16(acc[2][3], cat8(wl[1][3], wh[1][3]), xf[2]);
        SBAR();
        xf[1] = lds128(xr[0] + 37888);
        mm16(acc[3][0], cat8(wl[1][0], wh[1][0]), xf[3]);
        mm16(acc[3][1], cat8(wl[1][1], wh[1][1]), xf[3]);
        SBAR();
        mm16(acc[3][2], cat8(wl[1][2], wh[1][2]), xf[3]);
        mm16(acc[3][3], cat8(wl[1][3], wh[1][3]), xf[3]);
        SBAR();
        xf[2] = lds128(xr[0] + 38912);
        mm16(acc[4][0], cat8(wl[1][0], wh[1][0]), xf[0]);
        mm16(acc[4][1], cat8(wl[1][1], wh[1][1]), xf[0]);
        SBAR();
        mm16(acc[4][2], cat8(wl[1][2], wh[1][2]), xf[0]);
        mm16(acc[4][3], cat8(wl[1][3], wh[1][3]), xf[0]);
        SBAR();
        xf[3] = lds128(xr[0] + 39936);
        mm16(acc[5][0], cat8(wl[1][0], wh[1][0]), xf[1]);
        mm16(acc[5][1], cat8(wl[1][1], wh[1][1]), xf[1]);
        SBAR();
        mm16(acc[5][2], cat8(wl[1][2], wh[1][2]), xf[1]);
        mm16(acc[5][3], cat8(wl[1][3], wh[1][3]), xf[1]);
        SBAR();
        mm16(acc[6][0], cat8(wl[1][0], wh[1][0]), xf[2]);
        mm16(acc[6][1], cat8(wl[1][1], wh[1][1]), xf[2]);
        SBAR();
        wait_vm_bar<16>();
        wl[0][0] = ldstr(wr0[1] + 0); wh[0][0] = ldstr(wr0[1] + 0 + 256); xf[0] = lds128(xr[1] + 0); wl[0][1] = ldstr(wr1[1] + 0); wh[0][1] = ldstr(wr1[1] + 0 + 256);
        mm16(acc[6][2], cat8(wl[1][2], wh[1][2]), xf[2]);
        mm16(acc[6][3], cat8(wl[1][3], wh[1][3]), xf[2]);
        dma16(xoff, xs + 320, ldsk + 32768);
        SBAR();
        wl[0][2] = ldstr(wr0[1] + 512); wh[0][2] = ldstr(wr0[1] + 512 + 256); wl[0][3] = ldstr(wr1[1] + 512); wh[0][3] = ldstr(wr1[1] + 512 + 256);
        mm16(acc[7][0], cat8(wl[1][0], wh[1][0]), xf[3]);
        mm16(acc[7][1], cat8(wl[1][1], wh[1][1]), xf[3]);
        dma16(xoff, xs + 16704, ldsk + 33792);
        SBAR();
        xf[1] = lds128(xr[1] + 1024);
        mm16(acc[7][2], cat8(wl[1][2], wh[1][2]), xf[3]);
        mm16(acc[7][3], cat8(wl[1][3], wh[1][3]), xf[3]);
        dma16(xoff, xs + 33088, ldsk + 34816);
        SBAR();
        xf[2] = lds128(xr[1] + 2048);
        mm16(acc[0][0], cat8(wl[0][0], wh[0][0]), xf[0]);
        mm16(acc[0][1], cat8(wl[0][1], wh[0][1]), xf[0]);
        dma16(xoff, xs + 49472, ldsk + 35840);
        SBAR();
        mm16(acc[0][2], cat8(wl[0][2], wh[0][2]), xf[0]);
        mm16(acc[0][3], cat8(wl[0][3], wh[0][3]), xf[0]);
        dma16(woff, wks + 1310720, ldsk + 49152);
        SBAR();
        xf[3] = lds128(xr[1] + 3072);
        mm16(acc[1][0], cat8(wl[0][0], wh[0][0]), xf[1]);
        mm16(acc[1][1], cat8(wl[0][1], wh[0][1]), xf[1]);
        dma16(woff, wks + 1310848, ldsk + 50176);
        SBAR();
        mm16(acc[1][2], cat8(wl[0][2], wh[0][2]), xf[1]);
        mm16(acc[1][3], cat8(wl[0][3], wh[0][3]), xf[1]);
        dma16(woff, wvs + 1310720, ldsk + 51200);
        SBAR();
        xf[0] = lds128(xr[1] + 4096);
        mm16(acc[2][0], cat8(wl[0][0], wh[0][0]), xf[2]);
        mm16(acc[2][1], cat8(wl[0][1], wh[0][1]), xf[2]);
        dma16(woff, wvs + 1310848, ldsk + 52224);
        SBAR();
        mm16(acc[2][2], cat8(wl[0][2], wh[0][2]), xf[2]);
        mm16(acc[2][3], cat8(wl[0][3], wh[0][3]), xf[2]);
        SBAR();
        xf[1] = lds128(xr[1] + 5120);
        mm16(acc[3][0], cat8(wl[0][0], wh[0][0]), xf[3]);
        mm16(acc[3][1], cat8(wl[0][1], wh[0][1]), xf[3]);
        SBAR();
        mm16(acc[3][2], cat8(wl[0][2], wh[0][2]), xf[3]);
        mm16(acc[3][3], cat8(wl[0][3], wh[0][3]), xf[3]);
        SBAR();
        xf[2] = lds128(xr[1] + 6144);
        mm16(acc[4][0], cat8(wl[0][0], wh[0][0]), xf[0]);
        mm16(acc[4][1], cat8(wl[0][1], wh[0][1]), xf[0]);
        SBAR();
        mm16(acc[4][2], cat8(wl[0][2], wh[0][2]), xf[0]);
        mm16(acc[4][3], cat8(wl[0][3], wh[0][3]), xf[0]);
        SBAR();
        xf[3] = lds128(xr[1] + 7168);
        mm16(acc[5][0], cat8(wl[0][0], wh[0][0]), xf[1]);
        mm16(acc[5][1], cat8(wl[0][1], wh[0][1]), xf[1]);
        SBAR();
        mm16(acc[5][2], cat8(wl[0][2], wh[0][2]), xf[1]);
        mm16(acc[5][3], cat8(wl[0][3], wh[0][3]), xf[1]);
        SBAR();
        mm16(acc[6][0], cat8(wl[0][0], wh[0][0]), xf[2]);
        mm16(acc[6][1], cat8(wl[0][1], wh[0][1]), xf[2]);
        SBAR();
        wait_vm_bar<16>();
        wl[1][0] = ldstr(wr0[1] + 32768); wh[1][0] = ldstr(wr0[1] + 32768 + 256); xf[0] = lds128(xr[1] + 32768); wl[1][1] = ldstr(wr1[1] + 32768); wh[1][1] = ldstr(wr1[1] + 32768 + 256);
        mm16(acc[6][2], cat8(wl[0][2], wh[0][2]), xf[2]);
        mm16(acc[6][3], cat8(wl[0][3], wh[0][3]), xf[2]);
        dma16(xoff, xs + 384, ldsk + 65536);
        SBAR();
        wl[1][2] = ldstr(wr0[1] + 33280); wh[1][2] = ldstr(wr0[1] + 33280 + 256); wl[1][3] = ldstr(wr1[1] + 33280); wh[1][3] = ldstr(wr1[1] + 33280 + 256);
        mm16(acc[7][0], cat8(wl[0][0], wh[0][0]), xf[3]);
        mm16(acc[7][1], cat8(wl[0][1], wh[0][1]), xf[3]);
        dma16(xoff, xs + 16768, ldsk + 66560);
        SBAR();
        xf[1] = lds128(xr[1] + 33792);
        mm16(acc[7][2], cat8(wl[0][2], wh[0][2]), xf[3]);
        mm16(acc[7][3], cat8(wl[0][3], wh[0][3]), xf[3]);
        dma16(xoff, xs + 33152, ldsk + 67584);
        SBAR();
        xf[2] = lds128(xr[1] + 34816);
        mm16(acc[0][0], cat8(wl[1][0], wh[1][0]), xf[0]);
        mm16(acc[0][1], cat8(wl[1][1], wh[1][1]), xf[0]);
        dma16(xoff, xs + 49536, ldsk + 68608);
        SBAR();
        mm16(acc[0][2], cat8(wl[1][2], wh[1][2]), xf[0]);
        mm16(acc[0][3], cat8(wl[1][3], wh[1][3]), xf[0]);
        dma16(woff, wks + 1572864, ldsk + 81920);
        SBAR();
        xf[3] = lds128(xr[1] + 35840);
        mm16(acc[1][0], cat8(wl[1][0], wh[1][0]), xf[1]);
        mm16(acc[1][1], cat8(wl[1][1], wh[1][1]), xf[1]);
        dma16(woff, wks + 1572992, ldsk + 82944);
        SBAR();
        mm16(acc[1][2], cat8(wl[1][2], wh[1][2]), xf[1]);
        mm16(acc[1][3], cat8(wl[1][3], wh[1][3]), xf[1]);
        dma16(woff, wvs + 1572864, ldsk + 83968);
        SBAR();
        xf[0] = lds128(xr[1] + 36864);
        mm16(acc[2][0], cat8(wl[1][0], wh[1][0]), xf[2]);
        mm16(acc[2][1], cat8(wl[1][1], wh[1][1]), xf[2]);
        dma16(woff, wvs + 1572992, ldsk + 84992);
        SBAR();
        mm16(acc[2][2], cat8(wl[1][2], wh[1][2]), xf[2]);
        mm16(acc[2][3], cat8(wl[1][3], wh[1][3]), xf[2]);
        SBAR();
        xf[1] = lds128(xr[1] + 37888);
        mm16(acc[3][0], cat8(wl[1][0], wh[1][0]), xf[3]);
        mm16(acc[3][1], cat8(wl[1][1], wh[1][1]), xf[3]);
        SBAR();
        mm16(acc[3][2], cat8(wl[1][2], wh[1][2]), xf[3]);
        mm16(acc[3][3], cat8(wl[1][3], wh[1][3]), xf[3]);
        SBAR();
        xf[2] = lds128(xr[1] + 38912);
        mm16(acc[4][0], cat8(wl[1][0], wh[1][0]), xf[0]);
        mm16(acc[4][1], cat8(wl[1][1], wh[1][1]), xf[0]);
        SBAR();
        mm16(acc[4][2], cat8(wl[1][2], wh[1][2]), xf[0]);
        mm16(acc[4][3], cat8(wl[1][3], wh[1][3]), xf[0]);
        SBAR();
        xf[3] = lds128(xr[1] + 39936);
        mm16(acc[5][0], cat8(wl[1][0], wh[1][0]), xf[1]);
        mm16(acc[5][1], cat8(wl[1][1], wh[1][1]), xf[1]);
        SBAR();
        mm16(acc[5][2], cat8(wl[1][2], wh[1][2]), xf[1]);
        mm16(acc[5][3], cat8(wl[1][3], wh[1][3]), xf[1]);
        SBAR();
        mm16(acc[6][0], cat8(wl[1][0], wh[1][0]), xf[2]);
        mm16(acc[6][1], cat8(wl[1][1], wh[1][1]), xf[2]);
        SBAR();
        wait_vm_bar<16>();
        wl[0][0] = ldstr(wr0[0] + 0); wh[0][0] = ldstr(wr0[0] + 0 + 256); xf[0] = lds128(xr[0] + 0); wl[0][1] = ldstr(wr1[0] + 0); wh[0][1] = ldstr(wr1[0] + 0 + 256);
        mm16(acc[6][2], cat8(wl[1][2], wh[1][2]), xf[2]);
        mm16(acc[6][3], cat8(wl[1][3], wh[1][3]), xf[2]);
        dma16(xoff, xs + 448, ldsk + 98304);
        SBAR();
        wl[0][2] = ldstr(wr0[0] + 512); wh[0][2] = ldstr(wr0[0] + 512 + 256); wl[0][3] = ldstr(wr1[0] + 512); wh[0][3] = ldstr(wr1[0] + 512 + 256);
        mm16(acc[7][0], cat8(wl[1][0], wh[1][0]), xf[3]);
        mm16(acc[7][1], cat8(wl[1][1], wh[1][1]), xf[3]);
        dma16(xoff, xs + 16832, ldsk + 99328);
        SBAR();
        xf[1] = lds128(xr[0] + 1024);
        mm16(acc[7][2], cat8(wl[1][2], wh[1][2]), xf[3]);
        mm16(acc[7][3], cat8(wl[1][3], wh[1][3]), xf[3]);
        dma16(xoff, xs + 33216, ldsk + 100352);
        SBAR();
        xf[2] = lds128(xr[0] + 2048);
        mm16(acc[0][0], cat8(wl[0][0], wh[0][0]), xf[0]);
        mm16(acc[0][1], cat8(wl[0][1], wh[0][1]), xf[0]);
        dma16(xoff, xs + 49600, ldsk + 101376);
        SBAR();
        mm16(acc[0][2], cat8(wl[0][2], wh[0][2]), xf[0]);
        mm16(acc[0][3], cat8(wl[0][3], wh[0][3]), xf[0]);
        dma16(woff, wks + 1835008, ldsk + 114688);
        SBAR();
        xf[3] = lds128(xr[0] + 3072);
        mm16(acc[1][0], cat8(wl[0][0], wh[0][0]), xf[1]);
        mm16(acc[1][1], cat8(wl[0][1], wh[0][1]), xf[1]);
        dma16(woff, wks + 1835136, ldsk + 115712);
        SBAR();
        mm16(acc[1][2], cat8(wl[0][2], wh[0][2]), xf[1]);
        mm16(acc[1][3], cat8(wl[0][3], wh[0][3]), xf[1]);
        dma16(woff, wvs + 1835008, ldsk + 116736);
        SBAR();
        xf[0] = lds128(xr[0] + 4096);
        mm16(acc[2][0], cat8(wl[0][0], wh[0][0]), xf[2]);
        mm16(acc[2][1], cat8(wl[0][1], wh[0][1]), xf[2]);
        dma16(woff, wvs + 1835136, ldsk + 117760);
        SBAR();
        mm16(acc[2][2], cat8(wl[0][2], wh[0][2]), xf[2]);
        mm16(acc[2][3], cat8(wl[0][3], wh[0][3]), xf[2]);
        SBAR();
        xf[1] = lds128(xr[0] + 5120);
        mm16(acc[3][0], cat8(wl[0][0], wh[0][0]), xf[3]);
        mm16(acc[3][1], cat8(wl[0][1], wh[0][1]), xf[3]);
        SBAR();
        mm16(acc[3][2], cat8(wl[0][2], wh[0][2]), xf[3]);
        mm16(acc[3][3], cat8(wl[0][3], wh[0][3]), xf[3]);
        SBAR();
        xf[2] = lds128(xr[0] + 6144);
        mm16(acc[4][0], cat8(wl[0][0], wh[0][0]), xf[0]);
        mm16(acc[4][1], cat8(wl[0][1], wh[0][1]), xf[0]);
        SBAR();
        mm16(acc[4][2], cat8(wl[0][2], wh[0][2]), xf[0]);
        mm16(acc[4][3], cat8(wl[0][3], wh[0][3]), xf[0]);
        SBAR();
        xf[3] = lds128(xr[0] + 7168);
        mm16(acc[5][0], cat8(wl[0][0], wh[0][0]), xf[1]);
        mm16(acc[5][1], cat8(wl[0][1], wh[0][1]), xf[1]);
        SBAR();
        mm16(acc[5][2], cat8(wl[0][2], wh[0][2]), xf[1]);
        mm16(acc[5][3], cat8(wl[0][3], wh[0][3]), xf[1]);
        SBAR();
        mm16(acc[6][0], cat8(wl[0][0], wh[0][0]), xf[2]);
        mm16(acc[6][1], cat8(wl[0][1], wh[0][1]), xf[2]);
        SBAR();
        wait_vm_bar<16>();
        wl[1][0] = ldstr(wr0[0] + 32768); wh[1][0] = ldstr(wr0[0] + 32768 + 256); xf[0] = lds128(xr[0] + 32768); wl[1][1] = ldstr(wr1[0] + 32768); wh[1][1] = ldstr(wr1[0] + 32768 + 256);
        mm16(acc[6][2], cat8(wl[0][2], wh[0][2]), xf[2]);
        mm16(acc[6][3], cat8(wl[0][3], wh[0][3]), xf[2]);
        dma16(xoff, xs + 512, ldsk + 0);
        SBAR();
        wl[1][2] = ldstr(wr0[0] + 33280); wh[1][2] = ldstr(wr0[0] + 33280 + 256); wl[1][3] = ldstr(wr1[0] + 33280); wh[1][3] = ldstr(wr1[0] + 33280 + 256);
        mm16(acc[7][0], cat8(wl[0][0], wh[0][0]), xf[3]);
        mm16(acc[7][1], cat8(wl[0][1], wh[0][1]), xf[3]);
        dma16(xoff, xs + 16896, ldsk + 1024);
        SBAR();
        xf[1] = lds128(xr[0] + 33792);
        mm16(acc[7][2], cat8(wl[0][2], wh[0][2]), xf[3]);
        mm16(acc[7][3], cat8(wl[0][3], wh[0][3]), xf[3]);
        dma16(xoff, xs + 33280, ldsk + 2048);
        SBAR();
        xf[2] = lds128(xr[0] + 34816);
        mm16(acc[0][0], cat8(wl[1][0], wh[1][0]), xf[0]);
        mm16(acc[0][1], cat8(wl[1][1], wh[1][1]), xf[0]);
        dma16(xoff, xs + 49664, ldsk + 3072);
        SBAR();
        mm16(acc[0][2], cat8(wl[1][2], wh[1][2]), xf[0]);
        mm16(acc[0][3], cat8(wl[1][3], wh[1][3]), xf[0]);
        dma16(woff, wks + 2097152, ldsk + 16384);
        SBAR();
        xf[3] = lds128(xr[0] + 35840);
        mm16(acc[1][0], cat8(wl[1][0], wh[1][0]), xf[1]);
        mm16(acc[1][1], cat8(wl[1][1], wh[1][1]), xf[1]);
        dma16(woff, wks + 2097280, ldsk + 17408);
        SBAR();
        mm16(acc[1][2], cat8(wl[1][2], wh[1][2]), xf[1]);
        mm16(acc[1][3], cat8(wl[1][3], wh[1][3]), xf[1]);
        dma16(woff, wvs + 2097152, ldsk + 18432);
        SBAR();
        xf[0] = lds128(xr[0] + 36864);
        mm16(acc[2][0], cat8(wl[1][0], wh[1][0]), xf[2]);
        mm16(acc[2][1], cat8(wl[1][1], wh[1][1]), xf[2]);
        dma16(woff, wvs + 2097280, ldsk + 19456);
        SBAR();
        mm16(acc[2][2], cat8(wl[1][2], wh[1][2]), xf[2]);
        mm16(acc[2][3], cat8(wl[1][3], wh[1][3]), xf[2]);
        SBAR();
        xf[1] = lds128(xr[0] + 37888);
        mm16(acc[3][0], cat8(wl[1][0], wh[1][0]), xf[3]);
        mm16(acc[3][1], cat8(wl[1][1], wh[1][1]), xf[3]);
        SBAR();
        mm16(acc[3][2], cat8(wl[1][2], wh[1][2]), xf[3]);
        mm16(acc[3][3], cat8(wl[1][3], wh[1][3]), xf[3]);
        SBAR();
        xf[2] = lds128(xr[0] + 38912);
        mm16(acc[4][0], cat8(wl[1][0], wh[1][0]), xf[0]);
        mm16(acc[4][1], cat8(wl[1][1], wh[1][1]), xf[0]);
        SBAR();
        mm16(acc[4][2], cat8(wl[1][2], wh[1][2]), xf[0]);
        mm16(acc[4][3], cat8(wl[1][3], wh[1][3]), xf[0]);
        SBAR();
        xf[3] = lds128(xr[0] + 39936);
        mm16(acc[5][0], cat8(wl[1][0], wh[1][0]), xf[1]);
        mm16(acc[5][1], cat8(wl[1][1], wh[1][1]), xf[1]);
        SBAR();
        mm16(acc[5][2], cat8(wl[1][2], wh[1][2]), xf[1]);
        mm16(acc[5][3], cat8(wl[1][3], wh[1][3]), xf[1]);
        SBAR();
        mm16(acc[6][0], cat8(wl[1][0], wh[1][0]), xf[2]);
        mm16(acc[6][1], cat8(wl[1][1], wh[1][1]), xf[2]);
        SBAR();
        wait_vm_bar<16>();
        wl[0][0] = ldstr(wr0[1] + 0); wh[0][0] = ldstr(wr0[1] + 0 + 256); xf[0] = lds128(xr[1] + 0); wl[0][1] = ldstr(wr1[1] + 0); wh[0][1] = ldstr(wr1[1] + 0 + 256);
        mm16(acc[6][2], cat8(wl[1][2], wh[1][2]), xf[2]);
        mm16(acc[6][3], cat8(wl[1][3], wh[1][3]), xf[2]);
        dma16(xoff, xs + 576, ldsk + 32768);
        SBAR();
        wl[0][2] = ldstr(wr0[1] + 512); wh[0][2] = ldstr(wr0[1] + 512 + 256); wl[0][3] = ldstr(wr1[1] + 512); wh[0][3] = ldstr(wr1[1] + 512 + 256);
        mm16(acc[7][0], cat8(wl[1][0], wh[1][0]), xf[3]);
        mm16(acc[7][1], cat8(wl[1][1], wh[1][1]), xf[3]);
        dma16(xoff, xs + 16960, ldsk + 33792);
        SBAR();
        xf[1] = lds128(xr[1] + 1024);
        mm16(acc[7][2], cat8(wl[1][2], wh[1][2]), xf[3]);
        mm16(acc[7][3], cat8(wl[1][3], wh[1][3]), xf[3]);
        dma16(xoff, xs + 33344, ldsk + 34816);
        SBAR();
        xf[2] = lds128(xr[1] + 2048);
        mm16(acc[0][0], cat8(wl[0][0], wh[0][0]), xf[0]);
        mm16(acc[0][1], cat8(wl[0][1], wh[0][1]), xf[0]);
        dma16(xoff, xs + 49728, ldsk + 35840);
        SBAR();
        mm16(acc[0][2], cat8(wl[0][2], wh[0][2]), xf[0]);
        mm16(acc[0][3], cat8(wl[0][3], wh[0][3]), xf[0]);
        dma16(woff, wks + 2359296, ldsk + 49152);
        SBAR();
        xf[3] = lds128(xr[1] + 3072);
        mm16(acc[1][0], cat8(wl[0][0], wh[0][0]), xf[1]);
        mm16(acc[1][1], cat8(wl[0][1], wh[0][1]), xf[1]);
        dma16(woff, wks + 2359424, ldsk + 50176);
        SBAR();
        mm16(acc[1][2], cat8(wl[0][2], wh[0][2]), xf[1]);
        mm16(acc[1][3], cat8(wl[0][3], wh[0][3]), xf[1]);
        dma16(woff, wvs + 2359296, ldsk + 51200);
        SBAR();
        xf[0] = lds128(xr[1] + 4096);
        mm16(acc[2][0], cat8(wl[0][0], wh[0][0]), xf[2]);
        mm16(acc[2][1], cat8(wl[0][1], wh[0][1]), xf[2]);
        dma16(woff, wvs + 2359424, ldsk + 52224);
        SBAR();
        mm16(acc[2][2], cat8(wl[0][2], wh[0][2]), xf[2]);
        mm16(acc[2][3], cat8(wl[0][3], wh[0][3]), xf[2]);
        SBAR();
        xf[1] = lds128(xr[1] + 5120);
        mm16(acc[3][0], cat8(wl[0][0], wh[0][0]), xf[3]);
        mm16(acc[3][1], cat8(wl[0][1], wh[0][1]), xf[3]);
        SBAR();
        mm16(acc[3][2], cat8(wl[0][2], wh[0][2]), xf[3]);
        mm16(acc[3][3], cat8(wl[0][3], wh[0][3]), xf[3]);
        SBAR();
        xf[2] = lds128(xr[1] + 6144);
        mm16(acc[4][0], cat8(wl[0][0], wh[0][0]), xf[0]);
        mm16(acc[4][1], cat8(wl[0][1], wh[0][1]), xf[0]);
        SBAR();
        mm16(acc[4][2], cat8(wl[0][2], wh[0][2]), xf[0]);
        mm16(acc[4][3], cat8(wl[0][3], wh[0][3]), xf[0]);
        SBAR();
        xf[3] = lds128(xr[1] + 7168);
        mm16(acc[5][0], cat8(wl[0][0], wh[0][0]), xf[1]);
        mm16(acc[5][1], cat8(wl[0][1], wh[0][1]), xf[1]);
        SBAR();
        mm16(acc[5][2], cat8(wl[0][2], wh[0][2]), xf[1]);
        mm16(acc[5][3], cat8(wl[0][3], wh[0][3]), xf[1]);
        SBAR();
        mm16(acc[6][0], cat8(wl[0][0], wh[0][0]), xf[2]);
        mm16(acc[6][1], cat8(wl[0][1], wh[0][1]), xf[2]);
        SBAR();
        wait_vm_bar<16>();
        wl[1][0] = ldstr(wr0[1] + 32768); wh[1][0] = ldstr(wr0[1] + 32768 + 256); xf[0] = lds128(xr[1] + 32768); wl[1][1] = ldstr(wr1[1] + 32768); wh[1][1] = ldstr(wr1[1] + 32768 + 256);
        mm16(acc[6][2], cat8(wl[0][2], wh[0][2]), xf[2]);
        mm16(acc[6][3], cat8(wl[0][3], wh[0][3]), xf[2]);
        dma16(xoff, xs + 640, ldsk + 65536);
        SBAR();
        wl[1][2] = ldstr(wr0[1] + 33280); wh[1][2] = ldstr(wr0[1] + 33280 + 256); wl[1][3] = ldstr(wr1[1] + 33280); wh[1][3] = ldstr(wr1[1] + 33280 + 256);
        mm16(acc[7][0], cat8(wl[0][0], wh[0][0]), xf[3]);
        mm16(acc[7][1], cat8(wl[0][1], wh[0][1]), xf[3]);
        dma16(xoff, xs + 17024, ldsk + 66560);
        SBAR();
        xf[1] = lds128(xr[1] + 33792);
        mm16(acc[7][2], cat8(wl[0][2], wh[0][2]), xf[3]);
        mm16(acc[7][3], cat8(wl[0][3], wh[0][3]), xf[3]);
        dma16(xoff, xs + 33408, ldsk + 67584);
        SBAR();
        xf[2] = lds128(xr[1] + 34816);
        mm16(acc[0][0], cat8(wl[1][0], wh[1][0]), xf[0]);
        mm16(acc[0][1], cat8(wl[1][1], wh[1][1]), xf[0]);
        dma16(xoff, xs + 49792, ldsk + 68608);
        SBAR();
        mm16(acc[0][2], cat8(wl[1][2], wh[1][2]), xf[0]);
        mm16(acc[0][3], cat8(wl[1][3], wh[1][3]), xf[0]);
        dma16(woff, wks + 2621440, ldsk + 81920);
        SBAR();
        xf[3] = lds128(xr[1] + 35840);
        mm16(acc[1][0], cat8(wl[1][0], wh[1][0]), xf[1]);
        mm16(acc[1][1], cat8(wl[1][1], wh[1][1]), xf[1]);
        dma16(woff, wks + 2621568, ldsk + 82944);
        SBAR();
        mm16(acc[1][2], cat8(wl[1][2], wh[1][2]), xf[1]);
        mm16(acc[1][3], cat8(wl[1][3], wh[1][3]), xf[1]);
        dma16(woff, wvs + 2621440, ldsk + 83968);
        SBAR();
        xf[0] = lds128(xr[1] + 36864);
        mm16(acc[2][0], cat8(wl[1][0], wh[1][0]), xf[2]);
        mm16(acc[2][1], cat8(wl[1][1], wh[1][1]), xf[2]);
        dma16(woff, wvs + 2621568, ldsk + 84992);
        SBAR();
        mm16(acc[2][2], cat8(wl[1][2], wh[1][2]), xf[2]);
        mm16(acc[2][3], cat8(wl[1][3], wh[1][3]), xf[2]);
        SBAR();
        xf[1] = lds128(xr[1] + 37888);
        mm16(acc[3][0], cat8(wl[1][0], wh[1][0]), xf[3]);
        mm16(acc[3][1], cat8(wl[1][1], wh[1][1]), xf[3]);
        SBAR();
        mm16(acc[3][2], cat8(wl[1][2], wh[1][2]), xf[3]);
        mm16(acc[3][3], cat8(wl[1][3], wh[1][3]), xf[3]);
        SBAR();
        xf[2] = lds128(xr[1] + 38912);
        mm16(acc[4][0], cat8(wl[1][0], wh[1][0]), xf[0]);
        mm16(acc[4][1], cat8(wl[1][1], wh[1][1]), xf[0]);
        SBAR();
        mm16(acc[4][2], cat8(wl[1][2], wh[1][2]), xf[0]);
        mm16(acc[4][3], cat8(wl[1][3], wh[1][3]), xf[0]);
        SBAR();
        xf[3] = lds128(xr[1] + 39936);
        mm16(acc[5][0], cat8(wl[1][0], wh[1][0]), xf[1]);
        mm16(acc[5][1], cat8(wl[1][1], wh[1][1]), xf[1]);
        SBAR();
        mm16(acc[5][2], cat8(wl[1][2], wh[1][2]), xf[1]);
        mm16(acc[5][3], cat8(wl[1][3], wh[1][3]), xf[1]);
        SBAR();
        mm16(acc[6][0], cat8(wl[1][0], wh[1][0]), xf[2]);
        mm16(acc[6][1], cat8(wl[1][1], wh[1][1]), xf[2]);
        SBAR();
        wait_vm_bar<16>();
        wl[0][0] = ldstr(wr0[0] + 0); wh[0][0] = ldstr(wr0[0] + 0 + 256); xf[0] = lds128(xr[0] + 0); wl[0][1] = ldstr(wr1[0] + 0); wh[0][1] = ldstr(wr1[0] + 0 + 256);
        mm16(acc[6][2], cat8(wl[1][2], wh[1][2]), xf[2]);
        mm16(acc[6][3], cat8(wl[1][3], wh[1][3]), xf[2]);
        pv = poll_issue(fbase + F_LN * 32768, pvoff_ln);
        SBAR();
        wl[0][2] = ldstr(wr0[0] + 512); wh[0][2] = ldstr(wr0[0] + 512 + 256); wl[0][3] = ldstr(wr1[0] + 512); wh[0][3] = ldstr(wr1[0] + 512 + 256);
        mm16(acc[7][0], cat8(wl[1][0], wh[1][0]), xf[3]);
        mm16(acc[7][1], cat8(wl[1][1], wh[1][1]), xf[3]);
        dma16(xoff, xs + 704, ldsk + 98304);
        SBAR();
        xf[1] = lds128(xr[0] + 1024);
        mm16(acc[7][2], cat8(wl[1][2], wh[1][2]), xf[3]);
        mm16(acc[7][3], cat8(wl[1][3], wh[1][3]), xf[3]);
        dma16(xoff, xs + 17088, ldsk + 99328);
        SBAR();
        xf[2] = lds128(xr[0] + 2048);
        mm16(acc[0][0], cat8(wl[0][0], wh[0][0]), xf[0]);
        mm16(acc[0][1], cat8(wl[0][1], wh[0][1]), xf[0]);
        dma16(xoff, xs + 33472, ldsk + 100352);
        SBAR();
        mm16(acc[0][2], cat8(wl[0][2], wh[0][2]), xf[0]);
        mm16(acc[0][3], cat8(wl[0][3], wh[0][3]), xf[0]);
        dma16(xoff, xs + 49856, ldsk + 101376);
        SBAR();
        xf[3] = lds128(xr[0] + 3072);
        mm16(acc[1][0], cat8(wl[0][0], wh[0][0]), xf[1]);
        mm16(acc[1][1], cat8(wl[0][1], wh[0][1]), xf[1]);
        dma16(woff, wks + 2883584, ldsk + 114688);
        SBAR();
        mm16(acc[1][2], cat8(wl[0][2], wh[0][2]), xf[1]);
        mm16(acc[1][3], cat8(wl[0][3], wh[0][3]), xf[1]);
        dma16(woff, wks + 2883712, ldsk + 115712);
        SBAR();
        xf[0] = lds128(xr[0] + 4096);
        mm16(acc[2][0], cat8(wl[0][0], wh[0][0]), xf[2]);
        mm16(acc[2][1], cat8(wl[0][1], wh[0][1]), xf[2]);
        dma16(woff, wvs + 2883584, ldsk + 116736);
        SBAR();
        mm16(acc[2][2], cat8(wl[0][2], wh[0][2]), xf[2]);
        mm16(acc[2][3], cat8(wl[0][3], wh[0][3]), xf[2]);
        dma16(woff, wvs + 2883712, ldsk + 117760);
        SBAR();
        xf[1] = lds128(xr[0] + 5120);
        mm16(acc[3][0], cat8(wl[0][0], wh[0][0]), xf[3]);
        mm16(acc[3][1], cat8(wl[0][1], wh[0][1]), xf[3]);
        SBAR();
        mm16(acc[3][2], cat8(wl[0][2], wh[0][2]), xf[3]);
        mm16(acc[3][3], cat8(wl[0][3], wh[0][3]), xf[3]);
        SBAR();
        xf[2] = lds128(xr[0] + 6144);
        mm16(acc[4][0], cat8(wl[0][0], wh[0][0]), xf[0]);
        mm16(acc[4][1], cat8(wl[0][1], wh[0][1]), xf[0]);
        SBAR();
        mm16(acc[4][2], cat8(wl[0][2], wh[0][2]), xf[0]);
        mm16(acc[4][3], cat8(wl[0][3], wh[0][3]), xf[0]);
        SBAR();
        xf[3] = lds128(xr[0] + 7168);
        mm16(acc[5][0], cat8(wl[0][0], wh[0][0]), xf[1]);
        mm16(acc[5][1], cat8(wl[0][1], wh[0][1]), xf[1]);
        SBAR();
        mm16(acc[5][2], cat8(wl[0][2], wh[0][2]), xf[1]);
        mm16(acc[5][3], cat8(wl[0][3], wh[0][3]), xf[1]);
        SBAR();
        mm16(acc[6][0], cat8(wl[0][0], wh[0][0]), xf[2]);
        mm16(acc[6][1], cat8(wl[0][1], wh[0][1]), xf[2]);
        SBAR();
        wait_vm_bar<17>();
        wl[1][0] = ldstr(wr0[0] + 32768); wh[1][0] = ldstr(wr0[0] + 32768 + 256); xf[0] = lds128(xr[0] + 32768); wl[1][1] = ldstr(wr1[0] + 32768); wh[1][1] = ldstr(wr1[0] + 32768 + 256);
        mm16(acc[6][2], cat8(wl[0][2], wh[0][2]), xf[2]);
        mm16(acc[6][3], cat8(wl[0][3], wh[0][3]), xf[2]);
        dma16(xoff, xs + 768, ldsk + 0);
        SBAR();
        wl[1][2] = ldstr(wr0[0] + 33280); wh[1][2] = ldstr(wr0[0] + 33280 + 256); wl[1][3] = ldstr(wr1[0] + 33280); wh[1][3] = ldstr(wr1[0] + 33280 + 256);
        mm16(acc[7][0], cat8(wl[0][0], wh[0][0]), xf[3]);
        mm16(acc[7][1], cat8(wl[0][1], wh[0][1]), xf[3]);
        dma16(xoff, xs + 17152, ldsk + 1024);
        SBAR();
        xf[1] = lds128(xr[0] + 33792);
        mm16(acc[7][2], cat8(wl[0][2], wh[0][2]), xf[3]);
        mm16(acc[7][3], cat8(wl[0][3], wh[0][3]), xf[3]);
        dma16(xoff, xs + 33536, ldsk + 2048);
        SBAR();
        xf[2] = lds128(xr[0] + 34816);
        mm16(acc[0][0], cat8(wl[1][0], wh[1][0]), xf[0]);
        mm16(acc[0][1], cat8(wl[1][1], wh[1][1]), xf[0]);
        dma16(xoff, xs + 49920, ldsk + 3072);
        SBAR();
        mm16(acc[0][2], cat8(wl[1][2], wh[1][2]), xf[0]);
        mm16(acc[0][3], cat8(wl[1][3], wh[1][3]), xf[0]);
        dma16(woff, wks + 3145728, ldsk + 16384);
        SBAR();
        xf[3] = lds128(xr[0] + 35840);
        mm16(acc[1][0], cat8(wl[1][0], wh[1][0]), xf[1]);
        mm16(acc[1][1], cat8(wl[1][1], wh[1][1]), xf[1]);
        dma16(woff, wks + 3145856, ldsk + 17408);
        SBAR();
        mm16(acc[1][2], cat8(wl[1][2], wh[1][2]), xf[1]);
        mm16(acc[1][3], cat8(wl[1][3], wh[1][3]), xf[1]);
        dma16(woff, wvs + 3145728, ldsk + 18432);
        SBAR();
        xf[0] = lds128(xr[0] + 36864);
        mm16(acc[2][0], cat8(wl[1][0], wh[1][0]), xf[2]);
        mm16(acc[2][1], cat8(wl[1][1], wh[1][1]), xf[2]);
        dma16(woff, wvs + 3145856, ldsk + 19456);
        SBAR();
        mm16(acc[2][2], cat8(wl[1][2], wh[1][2]), xf[2]);
        mm16(acc[2][3], cat8(wl[1][3], wh[1][3]), xf[2]);
        SBAR();
        xf[1] = lds128(xr[0] + 37888);
        mm16(acc[3][0], cat8(wl[1][0], wh[1][0]), xf[3]);
        mm16(acc[3][1], cat8(wl[1][1], wh[1][1]), xf[3]);
        SBAR();
        mm16(acc[3][2], cat8(wl[1][2], wh[1][2]), xf[3]);
        mm16(acc[3][3], cat8(wl[1][3], wh[1][3]), xf[3]);
        SBAR();
        xf[2] = lds128(xr[0] + 38912);
        mm16(acc[4][0], cat8(wl[1][0], wh[1][0]), xf[0]);
        mm16(acc[4][1], cat8(wl[1][1], wh[1][1]), xf[0]);
        SBAR();
        mm16(acc[4][2], cat8(wl[1][2], wh[1][2]), xf[0]);
        mm16(acc[4][3], cat8(wl[1][3], wh[1][3]), xf[0]);
        SBAR();
        xf[3] = lds128(xr[0] + 39936);
        mm16(acc[5][0], cat8(wl[1][0], wh[1][0]), xf[1]);
        mm16(acc[5][1], cat8(wl[1][1], wh[1][1]), xf[1]);
        SBAR();
        mm16(acc[5][2], cat8(wl[1][2], wh[1][2]), xf[1]);
        mm16(acc[5][3], cat8(wl[1][3], wh[1][3]), xf[1]);
        SBAR();
        mm16(acc[6][0], cat8(wl[1][0], wh[1][0]), xf[2]);
        mm16(acc[6][1], cat8(wl[1][1], wh[1][1]), xf[2]);
        SBAR();
        wait_vm_bar<17>();
        wl[0][0] = ldstr(wr0[1] + 0); wh[0][0] = ldstr(wr0[1] + 0 + 256); xf[0] = lds128(xr[1] + 0); wl[0][1] = ldstr(wr1[1] + 0); wh[0][1] = ldstr(wr1[1] + 0 + 256);
        mm16(acc[6][2], cat8(wl[1][2], wh[1][2]), xf[2]);
        mm16(acc[6][3], cat8(wl[1][3], wh[1][3]), xf[2]);
        dma16(xoff, xs + 832, ldsk + 32768);
        SBAR();
        wl[0][2] = ldstr(wr0[1] + 512); wh[0][2] = ldstr(wr0[1] + 512 + 256); wl[0][3] = ldstr(wr1[1] + 512); wh[0][3] = ldstr(wr1[1] + 512 + 256);
        mm16(acc[7][0], cat8(wl[1][0], wh[1][0]), xf[3]);
        mm16(acc[7][1], cat8(wl[1][1], wh[1][1]), xf[3]);
        dma16(xoff, xs + 17216, ldsk + 33792);
        SBAR();
        xf[1] = lds128(xr[1] + 1024);
        mm16(acc[7][2], cat8(wl[1][2], wh[1][2]), xf[3]);
        mm16(acc[7][3], cat8(wl[1][3], wh[1][3]), xf[3]);
        dma16(xoff, xs + 33600, ldsk + 34816);
        SBAR();
        xf[2] = lds128(xr[1] + 2048);
        mm16(acc[0][0], cat8(wl[0][0], wh[0][0]), xf[0]);
        mm16(acc[0][1], cat8(wl[0][1], wh[0][1]), xf[0]);
        dma16(xoff, xs + 49984, ldsk + 35840);
        SBAR();
        mm16(acc[0][2], cat8(wl[0][2], wh[0][2]), xf[0]);
        mm16(acc[0][3], cat8(wl[0][3], wh[0][3]), xf[0]);
        dma16(woff, wks + 3407872, ldsk + 49152);
        SBAR();
        xf[3] = lds128(xr[1] + 3072);
        mm16(acc[1][0], cat8(wl[0][0], wh[0][0]), xf[1]);
        mm16(acc[1][1], cat8(wl[0][1], wh[0][1]), xf[1]);
        dma16(woff, wks + 3408000, ldsk + 50176);
        SBAR();
        mm16(acc[1][2], cat8(wl[0][2], wh[0][2]), xf[1]);
        mm16(acc[1][3], cat8(wl[0][3], wh[0][3]), xf[1]);
        dma16(woff, wvs + 3407872, ldsk + 51200);
        SBAR();
        xf[0] = lds128(xr[1] + 4096);
        mm16(acc[2][0], cat8(wl[0][0], wh[0][0]), xf[2]);
        mm16(acc[2][1], cat8(wl[0][1], wh[0][1]), xf[2]);
        dma16(woff, wvs + 3408000, ldsk + 52224);
        SBAR();
        mm16(acc[2][2], cat8(wl[0][2], wh[0][2]), xf[2]);
        mm16(acc[2][3], cat8(wl[0][3], wh[0][3]), xf[2]);
        SBAR();
        xf[1] = lds128(xr[1] + 5120);
        mm16(acc[3][0], cat8(wl[0][0], wh[0][0]), xf[3]);
        mm16(acc[3][1], cat8(wl[0][1], wh[0][1]), xf[3]);
        SBAR();
        mm16(acc[3][2], cat8(wl[0][2], wh[0][2]), xf[3]);
        mm16(acc[3][3], cat8(wl[0][3], wh[0][3]), xf[3]);
        SBAR();
        xf[2] = lds128(xr[1] + 6144);
        mm16(acc[4][0], cat8(wl[0][0], wh[0][0]), xf[0]);
        mm16(acc[4][1], cat8(wl[0][1], wh[0][1]), xf[0]);
        SBAR();
        mm16(acc[4][2], cat8(wl[0][2], wh[0][2]), xf[0]);
        mm16(acc[4][3], cat8(wl[0][3], wh[0][3]), xf[0]);
        SBAR();
        xf[3] = lds128(xr[1] + 7168);
        mm16(acc[5][0], cat8(wl[0][0], wh[0][0]), xf[1]);
        mm16(acc[5][1], cat8(wl[0][1], wh[0][1]), xf[1]);
        SBAR();
        mm16(acc[5][2], cat8(wl[0][2], wh[0][2]), xf[1]);
        mm16(acc[5][3], cat8(wl[0][3], wh[0][3]), xf[1]);
        SBAR();
        mm16(acc[6][0], cat8(wl[0][0], wh[0][0]), xf[2]);
        mm16(acc[6][1], cat8(wl[0][1], wh[0][1]), xf[2]);
        SBAR();
        wait_vm_bar<16>();
        wl[1][0] = ldstr(wr0[1] + 32768); wh[1][0] = ldstr(wr0[1] + 32768 + 256); xf[0] = lds128(xr[1] + 32768); wl[1][1] = ldstr(wr1[1] + 32768); wh[1][1] = ldstr(wr1[1] + 32768 + 256);
        mm16(acc[6][2], cat8(wl[0][2], wh[0][2]), xf[2]);
        mm16(acc[6][3], cat8(wl[0][3], wh[0][3]), xf[2]);
        dma16(xoff, xs + 896, ldsk + 65536);
        SBAR();
        wl[1][2] = ldstr(wr0[1] + 33280); wh[1][2] = ldstr(wr0[1] + 33280 + 256); wl[1][3] = ldstr(wr1[1] + 33280); wh[1][3] = ldstr(wr1[1] + 33280 + 256);
        mm16(acc[7][0], cat8(wl[0][0], wh[0][0]), xf[3]);
        mm16(acc[7][1], cat8(wl[0][1], wh[0][1]), xf[3]);
        dma16(xoff, xs + 17280, ldsk + 66560);
        SBAR();
        xf[1] = lds128(xr[1] + 33792);
        mm16(acc[7][2], cat8(wl[0][2], wh[0][2]), xf[3]);
        mm16(acc[7][3], cat8(wl[0][3], wh[0][3]), xf[3]);
        dma16(xoff, xs + 33664, ldsk + 67584);
        SBAR();
        xf[2] = lds128(xr[1] + 34816);
        mm16(acc[0][0], cat8(wl[1][0], wh[1][0]), xf[0]);
        mm16(acc[0][1], cat8(wl[1][1], wh[1][1]), xf[0]);
        dma16(xoff, xs + 50048, ldsk + 68608);
        SBAR();
        mm16(acc[0][2], cat8(wl[1][2], wh[1][2]), xf[0]);
        mm16(acc[0][3], cat8(wl[1][3], wh[1][3]), xf[0]);
        dma16(woff, wks + 3670016, ldsk + 81920);
        SBAR();
        xf[3] = lds128(xr[1] + 35840);
        mm16(acc[1][0], cat8(wl[1][0], wh[1][0]), xf[1]);
        mm16(acc[1][1], cat8(wl[1][1], wh[1][1]), xf[1]);
        dma16(woff, wks + 3670144, ldsk + 82944);
        SBAR();
        mm16(acc[1][2], cat8(wl[1][2], wh[1][2]), xf[1]);
        mm16(acc[1][3], cat8(wl[1][3], wh[1][3]), xf[1]);
        dma16(woff, wvs + 3670016, ldsk + 83968);
        SBAR();
        xf[0] = lds128(xr[1] + 36864);
        mm16(acc[2][0], cat8(wl[1][0], wh[1][0]), xf[2]);
        mm16(acc[2][1], cat8(wl[1][1], wh[1][1]), xf[2]);
        dma16(woff, wvs + 3670144, ldsk + 84992);
        SBAR();
        mm16(acc[2][2], cat8(wl[1][2], wh[1][2]), xf[2]);
        mm16(acc[2][3], cat8(wl[1][3], wh[1][3]), xf[2]);
        SBAR();
        xf[1] = lds128(xr[1] + 37888);
        mm16(acc[3][0], cat8(wl[1][0], wh[1][0]), xf[3]);
        mm16(acc[3][1], cat8(wl[1][1], wh[1][1]), xf[3]);
        SBAR();
        mm16(acc[3][2], cat8(wl[1][2], wh[1][2]), xf[3]);
        mm16(acc[3][3], cat8(wl[1][3], wh[1][3]), xf[3]);
        SBAR();
        xf[2] = lds128(xr[1] + 38912);
        mm16(acc[4][0], cat8(wl[1][0], wh[1][0]), xf[0]);
        mm16(acc[4][1], cat8(wl[1][1], wh[1][1]), xf[0]);
        SBAR();
        mm16(acc[4][2], cat8(wl[1][2], wh[1][2]), xf[0]);
        mm16(acc[4][3], cat8(wl[1][3], wh[1][3]), xf[0]);
        SBAR();
        xf[3] = lds128(xr[1] + 39936);
        mm16(acc[5][0], cat8(wl[1][0], wh[1][0]), xf[1]);
        mm16(acc[5][1], cat8(wl[1][1], wh[1][1]), xf[1]);
        SBAR();
        mm16(acc[5][2], cat8(wl[1][2], wh[1][2]), xf[1]);
        mm16(acc[5][3], cat8(wl[1][3], wh[1][3]), xf[1]);
        SBAR();
        mm16(acc[6][0], cat8(wl[1][0], wh[1][0]), xf[2]);
        mm16(acc[6][1], cat8(wl[1][1], wh[1][1]), xf[2]);
        SBAR();
        wait_vm_bar<16>();
        poll_check(pv, fbase + F_LN * 32768, pvoff_ln, tag);
        wl[0][0] = ldstr(wr0[0] + 0); wh[0][0] = ldstr(wr0[0] + 0 + 256); xf[0] = lds128(xr[0] + 0); wl[0][1] = ldstr(wr1[0] + 0); wh[0][1] = ldstr(wr1[0] + 0 + 256);
        mm16(acc[6][2], cat8(wl[1][2], wh[1][2]), xf[2]);
        mm16(acc[6][3], cat8(wl[1][3], wh[1][3]), xf[2]);
        dma16(xoff, xs + 960, ldsk + 98304);
        SBAR();
        wl[0][2] = ldstr(wr0[0] + 512); wh[0][2] = ldstr(wr0[0] + 512 + 256); wl[0][3] = ldstr(wr1[0] + 512); wh[0][3] = ldstr(wr1[0] + 512 + 256);
        mm16(acc[7][0], cat8(wl[1][0], wh[1][0]), xf[3]);
        mm16(acc[7][1], cat8(wl[1][1], wh[1][1]), xf[3]);
        dma16(xoff, xs + 17344, ldsk + 99328);
        SBAR();
        xf[1] = lds128(xr[0] + 1024);
        mm16(acc[7][2], cat8(wl[1][2], wh[1][2]), xf[3]);
        mm16(acc[7][3], cat8(wl[1][3], wh[1][3]), xf[3]);
        dma16(xoff, xs + 33728, ldsk + 100352);
        SBAR();
        xf[2] = lds128(xr[0] + 2048);
        mm16(acc[0][0], cat8(wl[0][0], wh[0][0]), xf[0]);
        mm16(acc[0][1], cat8(wl[0][1], wh[0][1]), xf[0]);
        dma16(xoff, xs + 50112, ldsk + 101376);
        SBAR();
        mm16(acc[0][2], cat8(wl[0][2], wh[0][2]), xf[0]);
        mm16(acc[0][3], cat8(wl[0][3], wh[0][3]), xf[0]);
        dma16(woff, wks + 3932160, ldsk + 114688);
        SBAR();
        xf[3] = lds128(xr[0] + 3072);
        mm16(acc[1][0], cat8(wl[0][0], wh[0][0]), xf[1]);
        mm16(acc[1][1], cat8(wl[0][1], wh[0][1]), xf[1]);
        dma16(woff, wks + 3932288, ldsk + 115712);
        SBAR();
        mm16(acc[1][2], cat8(wl[0][2], wh[0][2]), xf[1]);
        mm16(acc[1][3], cat8(wl[0][3], wh[0][3]), xf[1]);
        dma16(woff, wvs + 3932160, ldsk + 116736);
        SBAR();
        xf[0] = lds128(xr[0] + 4096);
        mm16(acc[2][0], cat8(wl[0][0], wh[0][0]), xf[2]);
        mm16(acc[2][1], cat8(wl[0][1], wh[0][1]), xf[2]);
        dma16(woff, wvs + 3932288, ldsk + 117760);
        SBAR();
        mm16(acc[2][2], cat8(wl[0][2], wh[0][2]), xf[2]);
        mm16(acc[2][3], cat8(wl[0][3], wh[0][3]), xf[2]);
        SBAR();
        xf[1] = lds128(xr[0] + 5120);
        mm16(acc[3][0], cat8(wl[0][0], wh[0][0]), xf[3]);
        mm16(acc[3][1], cat8(wl[0][1], wh[0][1]), xf[3]);
        SBAR();
        mm16(acc[3][2], cat8(wl[0][2], wh[0][2]), xf[3]);
        mm16(acc[3][3], cat8(wl[0][3], wh[0][3]), xf[3]);
        SBAR();
        xf[2] = lds128(xr[0] + 6144);
        mm16(acc[4][0], cat8(wl[0][0], wh[0][0]), xf[0]);
        mm16(acc[4][1], cat8(wl[0][1], wh[0][1]), xf[0]);
        SBAR();
        mm16(acc[4][2], cat8(wl[0][2], wh[0][2]), xf[0]);
        mm16(acc[4][3], cat8(wl[0][3], wh[0][3]), xf[0]);
        SBAR();
        xf[3] = lds128(xr[0] + 7168);
        mm16(acc[5][0], cat8(wl[0][0], wh[0][0]), xf[1]);
        mm16(acc[5][1], cat8(wl[0][1], wh[0][1]), xf[1]);
        SBAR();
        mm16(acc[5][2], cat8(wl[0][2], wh[0][2]), xf[1]);
        mm16(acc[5][3], cat8(wl[0][3], wh[0][3]), xf[1]);
        SBAR();
        mm16(acc[6][0], cat8(wl[0][0], wh[0][0]), xf[2]);
        mm16(acc[6][1], cat8(wl[0][1], wh[0][1]), xf[2]);
        SBAR();
        wait_vm_bar<16>();
        wl[1][0] = ldstr(wr0[0] + 32768); wh[1][0] = ldstr(wr0[0] + 32768 + 256); xf[0] = lds128(xr[0] + 32768); wl[1][1] = ldstr(wr1[0] + 32768); wh[1][1] = ldstr(wr1[0] + 32768 + 256);
        mm16(acc[6][2], cat8(wl[0][2], wh[0][2]), xf[2]);
        mm16(acc[6][3], cat8(wl[0][3], wh[0][3]), xf[2]);
        dma16c(l16, xsrc0 + 0, ldsx + 0);
        SBAR();
        wl[1][2] = ldstr(wr0[0] + 33280); wh[1][2] = ldstr(wr0[0] + 33280 + 256); wl[1][3] = ldstr(wr1[0] + 33280); wh[1][3] = ldstr(wr1[0] + 33280 + 256);
        mm16(acc[7][0], cat8(wl[0][0], wh[0][0]), xf[3]);
        mm16(acc[7][1], cat8(wl[0][1], wh[0][1]), xf[3]);
        dma16c(l16, xsrc0 + 1024, ldsx + 1024);
        SBAR();
        xf[1] = lds128(xr[0] + 33792);
        mm16(acc[7][2], cat8(wl[0][2], wh[0][2]), xf[3]);
        mm16(acc[7][3], cat8(wl[0][3], wh[0][3]), xf[3]);
        dma16c(l16, xsrc1 + 0, ldsx + 8192);
        SBAR();
        xf[2] = lds128(xr[0] + 34816);
        mm16(acc[0][0], cat8(wl[1][0], wh[1][0]), xf[0]);
        mm16(acc[0][1], cat8(wl[1][1], wh[1][1]), xf[0]);
        dma16c(l16, xsrc1 + 1024, ldsx + 9216);
        SBAR();
        mm16(acc[0][2], cat8(wl[1][2], wh[1][2]), xf[0]);
        mm16(acc[0][3], cat8(wl[1][3], wh[1][3]), xf[0]);
        dma16(pvo[0], psb[0] + (size_t)0 * pst[0], lds0 + 0 + pdst[0]);
        SBAR();
        xf[3] = lds128(xr[0] + 35840);
        mm16(acc[1][0], cat8(wl[1][0], wh[1][0]), xf[1]);
        mm16(acc[1][1], cat8(wl[1][1], wh[1][1]), xf[1]);
        dma16(pvo[1], psb[1] + (size_t)0 * pst[1], lds0 + 0 + pdst[1]);
        SBAR();
        mm16(acc[1][2], cat8(wl[1][2], wh[1][2]), xf[1]);
        mm16(acc[1][3], cat8(wl[1][3], wh[1][3]), xf[1]);
        dma16(pvo[2], psb[2] + (size_t)0 * pst[2], lds0 + 0 + pdst[2]);
        SBAR();
        xf[0] = lds128(xr[0] + 36864);
        mm16(acc[2][0], cat8(wl[1][0], wh[1][0]), xf[2]);
        mm16(acc[2][1], cat8(wl[1][1], wh[1][1]), xf[2]);
        dma16(pvo[3], psb[3] + (size_t)0 * pst[3], lds0 + 0 + pdst[3]);
        SBAR();
        mm16(acc[2][2], cat8(wl[1][2], wh[1][2]), xf[2]);
        mm16(acc[2][3], cat8(wl[1][3], wh[1][3]), xf[2]);
        SBAR();
        xf[1] = lds128(xr[0] + 37888);
        mm16(acc[3][0], cat8(wl[1][0], wh[1][0]), xf[3]);
        mm16(acc[3][1], cat8(wl[1][1], wh[1][1]), xf[3]);
        SBAR();
        mm16(acc[3][2], cat8(wl[1][2], wh[1][2]), xf[3]);
        mm16(acc[3][3], cat8(wl[1][3], wh[1][3]), xf[3]);
        SBAR();
        xf[2] = lds128(xr[0] + 38912);
        mm16(acc[4][0], cat8(wl[1][0], wh[1][0]), xf[0]);
        mm16(acc[4][1], cat8(wl[1][1], wh[1][1]), xf[0]);
        SBAR();
        mm16(acc[4][2], cat8(wl[1][2], wh[1][2]), xf[0]);
        mm16(acc[4][3], cat8(wl[1][3], wh[1][3]), xf[0]);
        SBAR();
        xf[3] = lds128(xr[0] + 39936);
        mm16(acc[5][0], cat8(wl[1][0], wh[1][0]), xf[1]);
        mm16(acc[5][1], cat8(wl[1][1], wh[1][1]), xf[1]);
        SBAR();
        mm16(acc[5][2], cat8(wl[1][2], wh[1][2]), xf[1]);
        mm16(acc[5][3], cat8(wl[1][3], wh[1][3]), xf[1]);
        SBAR();
        mm16(acc[6][0], cat8(wl[1][0], wh[1][0]), xf[2]);
        mm16(acc[6][1], cat8(wl[1][1], wh[1][1]), xf[2]);
        SBAR();
        wait_vm_bar<16>();
        wl[0][0] = ldstr(wr0[1] + 0); wh[0][0] = ldstr(wr0[1] + 0 + 256); xf[0] = lds128(xr[1] + 0); wl[0][1] = ldstr(wr1[1] + 0); wh[0][1] = ldstr(wr1[1] + 0 + 256);
        mm16(acc[6][2], cat8(wl[1][2], wh[1][2]), xf[2]);
        mm16(acc[6][3], cat8(wl[1][3], wh[1][3]), xf[2]);
        dma16c(l16, xsrc0 + 2048, ldsx + 32768);
        SBAR();
        wl[0][2] = ldstr(wr0[1] + 512); wh[0][2] = ldstr(wr0[1] + 512 + 256); wl[0][3] = ldstr(wr1[1] + 512); wh[0][3] = ldstr(wr1[1] + 512 + 256);
        mm16(acc[7][0], cat8(wl[1][0], wh[1][0]), xf[3]);
        mm16(acc[7][1], cat8(wl[1][1], wh[1][1]), xf[3]);
        dma16c(l16, xsrc0 + 3072, ldsx + 33792);
        SBAR();
        xf[1] = lds128(xr[1] + 1024);
        mm16(acc[7][2], cat8(wl[1][2], wh[1][2]), xf[3]);
        mm16(acc[7][3], cat8(wl[1][3], wh[1][3]), xf[3]);
        dma16c(l16, xsrc1 + 2048, ldsx + 40960);
        SBAR();
        xf[2] = lds128(xr[1] + 2048);
        mm16(acc[0][0], cat8(wl[0][0], wh[0][0]), xf[0]);
        mm16(acc[0][1], cat8(wl[0][1], wh[0][1]), xf[0]);
        dma16c(l16, xsrc1 + 3072, ldsx + 41984);
        SBAR();
        mm16(acc[0][2], cat8(wl[0][2], wh[0][2]), xf[0]);
        mm16(acc[0][3], cat8(wl[0][3], wh[0][3]), xf[0]);
        dma16(pvo[0], psb[0] + (size_t)1 * pst[0], lds0 + 32768 + pdst[0]);
        SBAR();
        xf[3] = lds128(xr[1] + 3072);
        mm16(acc[1][0], cat8(wl[0][0], wh[0][0]), xf[1]);
        mm16(acc[1][1], cat8(wl[0][1], wh[0][1]), xf[1]);
        dma16(pvo[1], psb[1] + (size_t)1 * pst[1], lds0 + 32768 + pdst[1]);
        SBAR();
        mm16(acc[1][2], cat8(wl[0][2], wh[0][2]), xf[1]);
        mm16(acc[1][3], cat8(wl[0][3], wh[0][3]), xf[1]);
        dma16(pvo[2], psb[2] + (size_t)1 * pst[2], lds0 + 32768 + pdst[2]);
        SBAR();
        xf[0] = lds128(xr[1] + 4096);
        mm16(acc[2][0], cat8(wl[0][0], wh[0][0]), xf[2]);
        mm16(acc[2][1], cat8(wl[0][1], wh[0][1]), xf[2]);
        dma16(pvo[3], psb[3] + (size_t)1 * pst[3], lds0 + 32768 + pdst[3]);
        SBAR();
        mm16(acc[2][2], cat8(wl[0][2], wh[0][2]), xf[2]);
        mm16(acc[2][3], cat8(wl[0][3], wh[0][3]), xf[2]);
        SBAR();
        xf[1] = lds128(xr[1] + 5120);
        mm16(acc[3][0], cat8(wl[0][0], wh[0][0]), xf[3]);
        mm16(acc[3][1], cat8(wl[0][1], wh[0][1]), xf[3]);
        SBAR();
        mm16(acc[3][2], cat8(wl[0][2], wh[0][2]), xf[3]);
        mm16(acc[3][3], cat8(wl[0][3], wh[0][3]), xf[3]);
        SBAR();
        xf[2] = lds128(xr[1] + 6144);
        mm16(acc[4][0], cat8(wl[0][0], wh[0][0]), xf[0]);
        mm16(acc[4][1], cat8(wl[0][1], wh[0][1]), xf[0]);
        SBAR();
        mm16(acc[4][2], cat8(wl[0][2], wh[0][2]), xf[0]);
        mm16(acc[4][3], cat8(wl[0][3], wh[0][3]), xf[0]);
        SBAR();
        xf[3] = lds128(xr[1] + 7168);
        mm16(acc[5][0], cat8(wl[0][0], wh[0][0]), xf[1]);
        mm16(acc[5][1], cat8(wl[0][1], wh[0][1]), xf[1]);
        SBAR();
        mm16(acc[5][2], cat8(wl[0][2], wh[0][2]), xf[1]);
        mm16(acc[5][3], cat8(wl[0][3], wh[0][3]), xf[1]);
        SBAR();
        mm16(acc[6][0], cat8(wl[0][0], wh[0][0]), xf[2]);
        mm16(acc[6][1], cat8(wl[0][1], wh[0][1]), xf[2]);
        SBAR();
        wait_vm_bar<16>();
        wl[1][0] = ldstr(wr0[1] + 32768); wh[1][0] = ldstr(wr0[1] + 32768 + 256); xf[0] = lds128(xr[1] + 32768); wl[1][1] = ldstr(wr1[1] + 32768); wh[1][1] = ldstr(wr1[1] + 32768 + 256);
        mm16(acc[6][2], cat8(wl[0][2], wh[0][2]), xf[2]);
        mm16(acc[6][3], cat8(wl[0][3], wh[0][3]), xf[2]);
        dma16c(l16, xsrc0 + 4096, ldsx + 65536);
        SBAR();
        wl[1][2] = ldstr(wr0[1] + 33280); wh[1][2] = ldstr(wr0[1] + 33280 + 256); wl[1][3] = ldstr(wr1[1] + 33280); wh[1][3] = ldstr(wr1[1] + 33280 + 256);
        mm16(acc[7][0], cat8(wl[0][0], wh[0][0]), xf[3]);
        mm16(acc[7][1], cat8(wl[0][1], wh[0][1]), xf[3]);
        dma16c(l16, xsrc0 + 5120, ldsx + 66560);
        SBAR();
        xf[1] = lds128(xr[1] + 33792);
        mm16(acc[7][2], cat8(wl[0][2], wh[0][2]), xf[3]);
        mm16(acc[7][3], cat8(wl[0][3], wh[0][3]), xf[3]);
        dma16c(l16, xsrc1 + 4096, ldsx + 73728);
        SBAR();
        xf[2] = lds128(xr[1] + 34816);
        mm16(acc[0][0], cat8(wl[1][0], wh[1][0]), xf[0]);
        mm16(acc[0][1], cat8(wl[1][1], wh[1][1]), xf[0]);
        dma16c(l16, xsrc1 + 5120, ldsx + 74752);
        SBAR();
        mm16(acc[0][2], cat8(wl[1][2], wh[1][2]), xf[0]);
        mm16(acc[0][3], cat8(wl[1][3], wh[1][3]), xf[0]);
        dma16(pvo[0], psb[0] + (size_t)2 * pst[0], lds0 + 65536 + pdst[0]);
        SBAR();
        xf[3] = lds128(xr[1] + 35840);
        mm16(acc[1][0], cat8(wl[1][0], wh[1][0]), xf[1]);
        mm16(acc[1][1], cat8(wl[1][1], wh[1][1]), xf[1]);
        dma16(pvo[1], psb[1] + (size_t)2 * pst[1], lds0 + 65536 + pdst[1]);
        SBAR();
        mm16(acc[1][2], cat8(wl[1][2], wh[1][2]), xf[1]);
        mm16(acc[1][3], cat8(wl[1][3], wh[1][3]), xf[1]);
        SBAR();
        held[0] = pack2(acc[0][0], acc[0][1]); PIN(held[0]);
        dma16(pvo[2], psb[2] + (size_t)2 * pst[2], lds0 + 65536 + pdst[2]);
        SBAR();
        xf[0] = lds128(xr[1] + 36864);
        mm16(acc[2][0], cat8(wl[1][0], wh[1][0]), xf[2]);
        mm16(acc[2][1], cat8(wl[1][1], wh[1][1]), xf[2]);
        SBAR();
        held[1] = pack2(acc[0][2], acc[0][3]); PIN(held[1]);
        dma16(pvo[3], psb[3] + (size_t)2 * pst[3], lds0 + 65536 + pdst[3]);
        SBAR();
        mm16(acc[2][2], cat8(wl[1][2], wh[1][2]), xf[2]);
        mm16(acc[2][3], cat8(wl[1][3], wh[1][3]), xf[2]);
        SBAR();
        held[2] = pack2(acc[1][0], acc[1][1]); PIN(held[2]);
        SBAR();
        xf[1] = lds128(xr[1] + 37888);
        mm16(acc[3][0], cat8(wl[1][0], wh[1][0]), xf[3]);
        mm16(acc[3][1], cat8(wl[1][1], wh[1][1]), xf[3]);
        SBAR();
        held[3] = pack2(acc[1][2], acc[1][3]); PIN(held[3]);
        SBAR();
        mm16(acc[3][2], cat8(wl[1][2], wh[1][2]), xf[3]);
        mm16(acc[3][3], cat8(wl[1][3], wh[1][3]), xf[3]);
        SBAR();
        held[4] = pack2(acc[2][0], acc[2][1]); PIN(held[4]);
        SBAR();
        xf[2] = lds128(xr[1] + 38912);
        mm16(acc[4][0], cat8(wl[1][0], wh[1][0]), xf[0]);
        mm16(acc[4][1], cat8(wl[1][1], wh[1][1]), xf[0]);
        SBAR();
        held[5] = pack2(acc[2][2], acc[2][3]); PIN(held[5]);
        SBAR();
        mm16(acc[4][2], cat8(wl[1][2], wh[1][2]), xf[0]);
        mm16(acc[4][3], cat8(wl[1][3], wh[1][3]), xf[0]);
        SBAR();
        held[6] = pack2(acc[3][0], acc[3][1]); PIN(held[6]);
        SBAR();
        xf[3] = lds128(xr[1] + 39936);
        mm16(acc[5][0], cat8(wl[1][0], wh[1][0]), xf[1]);
        mm16(acc[5][1], cat8(wl[1][1], wh[1][1]), xf[1]);
        SBAR();
        held[7] = pack2(acc[3][2], acc[3][3]); PIN(held[7]);
        SBAR();
        mm16(acc[5][2], cat8(wl[1][2], wh[1][2]), xf[1]);
        mm16(acc[5][3], cat8(wl[1][3], wh[1][3]), xf[1]);
        SBAR();
        held[8] = pack2(acc[4][0], acc[4][1]); PIN(held[8]);
        SBAR();
        mm16(acc[6][0], cat8(wl[1][0], wh[1][0]), xf[2]);
        mm16(acc[6][1], cat8(wl[1][1], wh[1][1]), xf[2]);
        SBAR();
        held[9] = pack2(acc[4][2], acc[4][3]); PIN(held[9]);
        SBAR();
        wait_vm_bar<16>();
        mm16(acc[6][2], cat8(wl[1][2], wh[1][2]), xf[2]);
        mm16(acc[6][3], cat8(wl[1][3], wh[1][3]), xf[2]);
        SBAR();
        held[10] = pack2(acc[5][0], acc[5][1]); PIN(held[10]);
        dma16c(l16, xsrc0 + 6144, ldsx + 98304);
        SBAR();
        mm16(acc[7][0], cat8(wl[1][0], wh[1][0]), xf[3]);
        mm16(acc[7][1], cat8(wl[1][1], wh[1][1]), xf[3]);
        SBAR();
        held[11] = pack2(acc[5][2], acc[5][3]); PIN(held[11]);
        dma16c(l16, xsrc0 + 7168, ldsx + 99328);
        SBAR();
        mm16(acc[7][2], cat8(wl[1][2], wh[1][2]), xf[3]);
        mm16(acc[7][3], cat8(wl[1][3], wh[1][3]), xf[3]);
        SBAR();
        held[12] = pack2(acc[6][0], acc[6][1]); PIN(held[12]);
        dma16c(l16, xsrc1 + 6144, ldsx + 106496);
        SBAR();
        asm volatile("s_nop 7" ::: "memory"); SBAR();
        held[13] = pack2(acc[6][2], acc[6][3]);
        held[14] = pack2(acc[7][0], acc[7][1]);
        held[15] = pack2(acc[7][2], acc[7][3]);
        PJ_SETUP(PJ_WQ + g4 * 2048 + q4 * 64 + (g4 & 1) * 32 + p4 * 8, PJ_WQ + g4 * 2048 + q4 * 64 + (1 - (g4 & 1)) * 32 + p4 * 8, 0);
        pl[0] = ldstr(pw0[0] + 0); ph[0] = ldstr(pw0[0] + 0 + 256);
        px[0] = lds128(pxr[0] + 0);
        px[1] = lds128(pxr[0] + 256);
        pl[1] = ldstr(pw1[0] + 0); ph[1] = ldstr(pw1[0] + 0 + 256);
        px[2] = lds128(pxr[0] + 8192);
        px[3] = lds128(pxr[0] + 8448);
        SBAR();
        LAS char* qbw = (LAS char*)lds + LDS_QB + wid * 8192 + kvo; LAS char* qaw = (LAS char*)lds + (wid >> 1) * 32768 + 16384 + (wid & 1) * 8192;
        pl[2] = ldstr(pw0[0] + 512); ph[2] = ldstr(pw0[0] + 512 + 256);
        mm16z(pacc[0][0], cat8(pl[0], ph[0]), px[0]);
        mm16z(pacc[0][1], cat8(pl[0], ph[0]), px[1]);
        dma16c(l16, xsrc1 + 7168, ldsx + 107520);
        SBAR();
        mm16z(pacc[1][0], cat8(pl[1], ph[1]), px[0]);
        mm16z(pacc[1][1], cat8(pl[1], ph[1]), px[1]);
        dma16(pvo[0], psb[0] + (size_t)3 * pst[0], lds0 + 98304 + pdst[0]);
        SBAR();
        pl[3] = ldstr(pw1[0] + 512); ph[3] = ldstr(pw1[0] + 512 + 256);
        mm16z(pacc[0][2], cat8(pl[0], ph[0]), px[2]);
        mm16z(pacc[0][3], cat8(pl[0], ph[0]), px[3]);
        dma16(pvo[1], psb[1] + (size_t)3 * pst[1], lds0 + 98304 + pdst[1]);
        SBAR();
        mm16z(pacc[1][2], cat8(pl[1], ph[1]), px[2]);
        mm16z(pacc[1][3], cat8(pl[1], ph[1]), px[3]);
        dma16(pvo[2], psb[2] + (size_t)3 * pst[2], lds0 + 98304 + pdst[2]);
        SBAR();
        pl[0] = ldstr(pw0[0] + 1024); ph[0] = ldstr(pw0[0] + 1024 + 256);
        mm16z(pacc[2][0], cat8(pl[2], ph[2]), px[0]);
        mm16z(pacc[2][1], cat8(pl[2], ph[2]), px[1]);
        dma16(pvo[3], psb[3] + (size_t)3 * pst[3], lds0 + 98304 + pdst[3]);
        SBAR();
        mm16z(pacc[2][2], cat8(pl[2], ph[2]), px[2]);
        mm16z(pacc[2][3], cat8(pl[2], ph[2]), px[3]);
        *(LAS u32x4*)(hand + 0) = held[0];
        *(LAS u32x4*)(hand + 1024) = held[1];
        SBAR();
        pl[1] = ldstr(pw1[0] + 1024); ph[1] = ldstr(pw1[0] + 1024 + 256);
        mm16z(pacc[3][0], cat8(pl[3], ph[3]), px[0]);
        mm16z(pacc[3][1], cat8(pl[3], ph[3]), px[1]);
        *(LAS u32x4*)(hand + 2048) = held[2];
        *(LAS u32x4*)(hand + 3072) = held[3];
        SBAR();
        mm16z(pacc[3][2], cat8(pl[3], ph[3]), px[2]);
        mm16z(pacc[3][3], cat8(pl[3], ph[3]), px[3]);
        *(LAS u32x4*)(hand + 4096) = held[4];
        *(LAS u32x4*)(hand + 5120) = held[5];
        SBAR();
        pl[2] = ldstr(pw0[0] + 1536); ph[2] = ldstr(pw0[0] + 1536 + 256);
        mm16z(pacc[4][0], cat8(pl[0], ph[0]), px[0]);
        mm16z(pacc[4][1], cat8(pl[0], ph[0]), px[1]);
        *(LAS u32x4*)(hand + 6144) = held[6];
        *(LAS u32x4*)(hand + 7168) = held[7];
        SBAR();
        mm16z(pacc[4][2], cat8(pl[0], ph[0]), px[2]);
        mm16z(pacc[4][3], cat8(pl[0], ph[0]), px[3]);
        SBAR();
        pl[3] = ldstr(pw1[0] + 1536); ph[3] = ldstr(pw1[0] + 1536 + 256);
        mm16z(pacc[5][0], cat8(pl[1], ph[1]), px[0]);
        mm16z(pacc[5][1], cat8(pl[1], ph[1]), px[1]);
        SBAR();
        mm16z(pacc[5][2], cat8(pl[1], ph[1]), px[2]);
        mm16z(pacc[5][3], cat8(pl[1], ph[1]), px[3]);
        SBAR();
        mm16z(pacc[6][0], cat8(pl[2], ph[2]), px[0]);
        mm16z(pacc[6][1], cat8(pl[2], ph[2]), px[1]);
        SBAR();
        wait_vm_bar<16>();
        pl[0] = ldstr(pw0[0] + 32768); ph[0] = ldstr(pw0[0] + 32768 + 256);
        mm16z(pacc[7][0], cat8(pl[3], ph[3]), px[0]);
        mm16z(pacc[7][1], cat8(pl[3], ph[3]), px[1]);
        dma16c(l16, xsrc0 + 8192, ldsx + 0);
        SBAR();
        px[0] = lds128(pxr[0] + 32768); px[1] = lds128(pxr[0] + 33024); pl[1] = ldstr(pw1[0] + 32768); ph[1] = ldstr(pw1[0] + 32768 + 256);
        mm16z(pacc[6][2], cat8(pl[2], ph[2]), px[2]);
        mm16z(pacc[6][3], cat8(pl[2], ph[2]), px[3]);
        dma16c(l16, xsrc0 + 9216, ldsx + 1024);
        SBAR();
        mm16z(pacc[7][2], cat8(pl[3], ph[3]), px[2]);
        mm16z(pacc[7][3], cat8(pl[3], ph[3]), px[3]);
        dma16c(l16, xsrc1 + 8192, ldsx + 8192);
        SBAR();
        px[2] = lds128(pxr[0] + 40960); px[3] = lds128(pxr[0] + 41216);
        pl[2] = ldstr(pw0[0] + 33280); ph[2] = ldstr(pw0[0] + 33280 + 256);
        mm16(pacc[0][0], cat8(pl[0], ph[0]), px[0]);
        mm16(pacc[0][1], cat8(pl[0], ph[0]), px[1]);
        dma16c(l16, xsrc1 + 9216, ldsx + 9216);
        SBAR();
        mm16(pacc[1][0], cat8(pl[1], ph[1]), px[0]);
        mm16(pacc[1][1], cat8(pl[1], ph[1]), px[1]);
        dma16(pvo[0], psb[0] + (size_t)4 * pst[0], lds0 + 0 + pdst[0]);
        SBAR();
        pl[3] = ldstr(pw1[0] + 33280); ph[3] = ldstr(pw1[0] + 33280 + 256);
        mm16(pacc[0][2], cat8(pl[0], ph[0]), px[2]);
        mm16(pacc[0][3], cat8(pl[0], ph[0]), px[3]);
        dma16(pvo[1], psb[1] + (size_t)4 * pst[1], lds0 + 0 + pdst[1]);
        SBAR();
        mm16(pacc[1][2], cat8(pl[1], ph[1]), px[2]);
        mm16(pacc[1][3], cat8(pl[1], ph[1]), px[3]);
        dma16(pvo[2], psb[2] + (size_t)4 * pst[2], lds0 + 0 + pdst[2]);
        SBAR();
        pl[0] = ldstr(pw0[0] + 33792); ph[0] = ldstr(pw0[0] + 33792 + 256);
        mm16(pacc[2][0], cat8(pl[2], ph[2]), px[0]);
        mm16(pacc[2][1], cat8(pl[2], ph[2]), px[1]);
        dma16(pvo[3], psb[3] + (size_t)4 * pst[3], lds0 + 0 + pdst[3]);
        SBAR();
        mm16(pacc[2][2], cat8(pl[2], ph[2]), px[2]);
        mm16(pacc[2][3], cat8(pl[2], ph[2]), px[3]);
        SBAR();
        pl[1] = ldstr(pw1[0] + 33792); ph[1] = ldstr(pw1[0] + 33792 + 256);
        mm16(pacc[3][0], cat8(pl[3], ph[3]), px[0]);
        mm16(pacc[3][1], cat8(pl[3], ph[3]), px[1]);
        SBAR();
        mm16(pacc[3][2], cat8(pl[3], ph[3]), px[2]);
        mm16(pacc[3][3], cat8(pl[3], ph[3]), px[3]);
        SBAR();
        pl[2] = ldstr(pw0[0] + 34304); ph[2] = ldstr(pw0[0] + 34304 + 256);
        mm16(pacc[4][0], cat8(pl[0], ph[0]), px[0]);
        mm16(pacc[4][1], cat8(pl[0], ph[0]), px[1]);
        SBAR();
        mm16(pacc[4][2], cat8(pl[0], ph[0]), px[2]);
        mm16(pacc[4][3], cat8(pl[0], ph[0]), px[3]);
        SBAR();
        pl[3] = ldstr(pw1[0] + 34304); ph[3] = ldstr(pw1[0] + 34304 + 256);
        mm16(pacc[5][0], cat8(pl[1], ph[1]), px[0]);
        mm16(pacc[5][1], cat8(pl[1], ph[1]), px[1]);
        SBAR();
        mm16(pacc[5][2], cat8(pl[1], ph[1]), px[2]);
        mm16(pacc[5][3], cat8(pl[1], ph[1]), px[3]);
        SBAR();
        mm16(pacc[6][0], cat8(pl[2], ph[2]), px[0]);
        mm16(pacc[6][1], cat8(pl[2], ph[2]), px[1]);
        SBAR();
        wait_vm_bar<16>();
        pl[0] = ldstr(pw0[1] + 0); ph[0] = ldstr(pw0[1] + 0 + 256);
        mm16(pacc[7][0], cat8(pl[3], ph[3]), px[0]);
        mm16(pacc[7][1], cat8(pl[3], ph[3]), px[1]);
        dma16c(l16, xsrc0 + 10240, ldsx + 32768);
        SBAR();
        px[0] = lds128(pxr[1] + 0); px[1] = lds128(pxr[1] + 256); pl[1] = ldstr(pw1[1] + 0); ph[1] = ldstr(pw1[1] + 0 + 256);
        mm16(pacc[6][2], cat8(pl[2], ph[2]), px[2]);
        mm16(pacc[6][3], cat8(pl[2], ph[2]), px[3]);
        dma16c(l16, xsrc0 + 11264, ldsx + 33792);
        SBAR();
        mm16(pacc[7][2], cat8(pl[3], ph[3]), px[2]);
        mm16(pacc[7][3], cat8(pl[3], ph[3]), px[3]);
        dma16c(l16, xsrc1 + 10240, ldsx + 40960);
        SBAR();
        px[2] = lds128(pxr[1] + 8192); px[3] = lds128(pxr[1] + 8448);
        pl[2] = ldstr(pw0[1] + 512); ph[2] = ldstr(pw0[1] + 512 + 256);
        mm16(pacc[0][0], cat8(pl[0], ph[0]), px[0]);
        mm16(pacc[0][1], cat8(pl[0], ph[0]), px[1]);
        dma16c(l16, xsrc1 + 11264, ldsx + 41984);
        SBAR();
        mm16(pacc[1][0], cat8(pl[1], ph[1]), px[0]);
        mm16(pacc[1][1], cat8(pl[1], ph[1]), px[1]);
        dma16(pvo[0], psb[0] + (size_t)5 * pst[0], lds0 + 32768 + pdst[0]);
        SBAR();
        pl[3] = ldstr(pw1[1] + 512); ph[3] = ldstr(pw1[1] + 512 + 256);
        mm16(pacc[0][2], cat8(pl[0], ph[0]), px[2]);
        mm16(pacc[0][3], cat8(pl[0], ph[0]), px[3]);
        dma16(pvo[1], psb[1] + (size_t)5 * pst[1], lds0 + 32768 + pdst[1]);
        SBAR();
        mm16(pacc[1][2], cat8(pl[1], ph[1]), px[2]);
        mm16(pacc[1][3], cat8(pl[1], ph[1]), px[3]);
        dma16(pvo[2], psb[2] + (size_t)5 * pst[2], lds0 + 32768 + pdst[2]);
        SBAR();
        pl[0] = ldstr(pw0[1] + 1024); ph[0] = ldstr(pw0[1] + 1024 + 256);
        mm16(pacc[2][0], cat8(pl[2], ph[2]), px[0]);
        mm16(pacc[2][1], cat8(pl[2], ph[2]), px[1]);
        dma16(pvo[3], psb[3] + (size_t)5 * pst[3], lds0 + 32768 + pdst[3]);
        SBAR();
        mm16(pacc[2][2], cat8(pl[2], ph[2]), px[2]);
        mm16(pacc[2][3], cat8(pl[2], ph[2]), px[3]);
        SBAR();
        pl[1] = ldstr(pw1[1] + 1024); ph[1] = ldstr(pw1[1] + 1024 + 256);
        mm16(pacc[3][0], cat8(pl[3], ph[3]), px[0]);
        mm16(pacc[3][1], cat8(pl[3], ph[3]), px[1]);
        SBAR();
        mm16(pacc[3][2], cat8(pl[3], ph[3]), px[2]);
        mm16(pacc[3][3], cat8(pl[3], ph[3]), px[3]);
        SBAR();
        pl[2] = ldstr(pw0[1] + 1536); ph[2] = ldstr(pw0[1] + 1536 + 256);
        mm16(pacc[4][0], cat8(pl[0], ph[0]), px[0]);
        mm16(pacc[4][1], cat8(pl[0], ph[0]), px[1]);
        SBAR();
        mm16(pacc[4][2], cat8(pl[0], ph[0]), px[2]);
        mm16(pacc[4][3], cat8(pl[0], ph[0]), px[3]);
        SBAR();
        pl[3] = ldstr(pw1[1] + 1536); ph[3] = ldstr(pw1[1] + 1536 + 256);
        mm16(pacc[5][0], cat8(pl[1], ph[1]), px[0]);
        mm16(pacc[5][1], cat8(pl[1], ph[1]), px[1]);
        SBAR();
        mm16(pacc[5][2], cat8(pl[1], ph[1]), px[2]);
        mm16(pacc[5][3], cat8(pl[1], ph[1]), px[3]);
        SBAR();
        mm16(pacc[6][0], cat8(pl[2], ph[2]), px[0]);
        mm16(pacc[6][1], cat8(pl[2], ph[2]), px[1]);
        SBAR();
        wait_vm_bar<16>();
        pl[0] = ldstr(pw0[1] + 32768); ph[0] = ldstr(pw0[1] + 32768 + 256);
        mm16(pacc[7][0], cat8(pl[3], ph[3]), px[0]);
        mm16(pacc[7][1], cat8(pl[3], ph[3]), px[1]);
        dma16c(l16, xsrc0 + 12288, ldsx + 65536);
        SBAR();
        px[0] = lds128(pxr[1] + 32768); px[1] = lds128(pxr[1] + 33024); pl[1] = ldstr(pw1[1] + 32768); ph[1] = ldstr(pw1[1] + 32768 + 256);
        mm16(pacc[6][2], cat8(pl[2], ph[2]), px[2]);
        mm16(pacc[6][3], cat8(pl[2], ph[2]), px[3]);
        dma16c(l16, xsrc0 + 13312, ldsx + 66560);
        SBAR();
        mm16(pacc[7][2], cat8(pl[3], ph[3]), px[2]);
        mm16(pacc[7][3], cat8(pl[3], ph[3]), px[3]);
        dma16c(l16, xsrc1 + 12288, ldsx + 73728);
        SBAR();
        px[2] = lds128(pxr[1] + 40960); px[3] = lds128(pxr[1] + 41216);
        pl[2] = ldstr(pw0[1] + 33280); ph[2] = ldstr(pw0[1] + 33280 + 256);
        mm16(pacc[0][0], cat8(pl[0], ph[0]), px[0]);
        mm16(pacc[0][1], cat8(pl[0], ph[0]), px[1]);
        dma16c(l16, xsrc1 + 13312, ldsx + 74752);
        SBAR();
        mm16(pacc[1][0], cat8(pl[1], ph[1]), px[0]);
        mm16(pacc[1][1], cat8(pl[1], ph[1]), px[1]);
        dma16(pvo[0], psb[0] + (size_t)6 * pst[0], lds0 + 65536 + pdst[0]);
        SBAR();
        pl[3] = ldstr(pw1[1] + 33280); ph[3] = ldstr(pw1[1] + 33280 + 256);
        mm16(pacc[0][2], cat8(pl[0], ph[0]), px[2]);
        mm16(pacc[0][3], cat8(pl[0], ph[0]), px[3]);
        dma16(pvo[1], psb[1] + (size_t)6 * pst[1], lds0 + 65536 + pdst[1]);
        SBAR();
        mm16(pacc[1][2], cat8(pl[1], ph[1]), px[2]);
        mm16(pacc[1][3], cat8(pl[1], ph[1]), px[3]);
        dma16(pvo[2], psb[2] + (size_t)6 * pst[2], lds0 + 65536 + pdst[2]);
        SBAR();
        pl[0] = ldstr(pw0[1] + 33792); ph[0] = ldstr(pw0[1] + 33792 + 256);
        mm16(pacc[2][0], cat8(pl[2], ph[2]), px[0]);
        mm16(pacc[2][1], cat8(pl[2], ph[2]), px[1]);
        dma16(pvo[3], psb[3] + (size_t)6 * pst[3], lds0 + 65536 + pdst[3]);
        SBAR();
        mm16(pacc[2][2], cat8(pl[2], ph[2]), px[2]);
        mm16(pacc[2][3], cat8(pl[2], ph[2]), px[3]);
        SBAR();
        pl[1] = ldstr(pw1[1] + 33792); ph[1] = ldstr(pw1[1] + 33792 + 256);
        mm16(pacc[3][0], cat8(pl[3], ph[3]), px[0]);
        mm16(pacc[3][1], cat8(pl[3], ph[3]), px[1]);
        SBAR();
        mm16(pacc[3][2], cat8(pl[3], ph[3]), px[2]);
        mm16(pacc[3][3], cat8(pl[3], ph[3]), px[3]);
        SBAR();
        pl[2] = ldstr(pw0[1] + 34304); ph[2] = ldstr(pw0[1] + 34304 + 256);
        mm16(pacc[4][0], cat8(pl[0], ph[0]), px[0]);
        mm16(pacc[4][1], cat8(pl[0], ph[0]), px[1]);
        SBAR();
        mm16(pacc[4][2], cat8(pl[0], ph[0]), px[2]);
        mm16(pacc[4][3], cat8(pl[0], ph[0]), px[3]);
        SBAR();
        pl[3] = ldstr(pw1[1] + 34304); ph[3] = ldstr(pw1[1] + 34304 + 256);
        mm16(pacc[5][0], cat8(pl[1], ph[1]), px[0]);
        mm16(pacc[5][1], cat8(pl[1], ph[1]), px[1]);
        SBAR();
        mm16(pacc[5][2], cat8(pl[1], ph[1]), px[2]);
        mm16(pacc[5][3], cat8(pl[1], ph[1]), px[3]);
        SBAR();
        mm16(pacc[6][0], cat8(pl[2], ph[2]), px[0]);
        mm16(pacc[6][1], cat8(pl[2], ph[2]), px[1]);
        SBAR();
        wait_vm_bar<16>();
        pl[0] = ldstr(pw0[0] + 0); ph[0] = ldstr(pw0[0] + 0 + 256);
        mm16(pacc[7][0], cat8(pl[3], ph[3]), px[0]);
        mm16(pacc[7][1], cat8(pl[3], ph[3]), px[1]);
        dma16c(l16, xsrc0 + 14336, ldsx + 98304);
        SBAR();
        px[0] = lds128(pxr[0] + 0); px[1] = lds128(pxr[0] + 256); pl[1] = ldstr(pw1[0] + 0); ph[1] = ldstr(pw1[0] + 0 + 256);
        mm16(pacc[6][2], cat8(pl[2], ph[2]), px[2]);
        mm16(pacc[6][3], cat8(pl[2], ph[2]), px[3]);
        dma16c(l16, xsrc0 + 15360, ldsx + 99328);
        SBAR();
        mm16(pacc[7][2], cat8(pl[3], ph[3]), px[2]);
        mm16(pacc[7][3], cat8(pl[3], ph[3]), px[3]);
        dma16c(l16, xsrc1 + 14336, ldsx + 106496);
        SBAR();
        px[2] = lds128(pxr[0] + 8192); px[3] = lds128(pxr[0] + 8448);
        pl[2] = ldstr(pw0[0] + 512); ph[2] = ldstr(pw0[0] + 512 + 256);
        mm16(pacc[0][0], cat8(pl[0], ph[0]), px[0]);
        mm16(pacc[0][1], cat8(pl[0], ph[0]), px[1]);
        dma16c(l16, xsrc1 + 15360, ldsx + 107520);
        SBAR();
        mm16(pacc[1][0], cat8(pl[1], ph[1]), px[0]);
        mm16(pacc[1][1], cat8(pl[1], ph[1]), px[1]);
        dma16(pvo[0], psb[0] + (size_t)7 * pst[0], lds0 + 98304 + pdst[0]);
        SBAR();
        pl[3] = ldstr(pw1[0] + 512); ph[3] = ldstr(pw1[0] + 512 + 256);
        mm16(pacc[0][2], cat8(pl[0], ph[0]), px[2]);
        mm16(pacc[0][3], cat8(pl[0], ph[0]), px[3]);
        dma16(pvo[1], psb[1] + (size_t)7 * pst[1], lds0 + 98304 + pdst[1]);
        SBAR();
        mm16(pacc[1][2], cat8(pl[1], ph[1]), px[2]);
        mm16(pacc[1][3], cat8(pl[1], ph[1]), px[3]);
        dma16(pvo[2], psb[2] + (size_t)7 * pst[2], lds0 + 98304 + pdst[2]);
        SBAR();
        pl[0] = ldstr(pw0[0] + 1024); ph[0] = ldstr(pw0[0] + 1024 + 256);
        mm16(pacc[2][0], cat8(pl[2], ph[2]), px[0]);
        mm16(pacc[2][1], cat8(pl[2], ph[2]), px[1]);
        dma16(pvo[3], psb[3] + (size_t)7 * pst[3], lds0 + 98304 + pdst[3]);
        SBAR();
        mm16(pacc[2][2], cat8(pl[2], ph[2]), px[2]);
        mm16(pacc[2][3], cat8(pl[2], ph[2]), px[3]);
        SBAR();
        pl[1] = ldstr(pw1[0] + 1024); ph[1] = ldstr(pw1[0] + 1024 + 256);
        mm16(pacc[3][0], cat8(pl[3], ph[3]), px[0]);
        mm16(pacc[3][1], cat8(pl[3], ph[3]), px[1]);
        SBAR();
        mm16(pacc[3][2], cat8(pl[3], ph[3]), px[2]);
        mm16(pacc[3][3], cat8(pl[3], ph[3]), px[3]);
        SBAR();
        pl[2] = ldstr(pw0[0] + 1536); ph[2] = ldstr(pw0[0] + 1536 + 256);
        mm16(pacc[4][0], cat8(pl[0], ph[0]), px[0]);
        mm16(pacc[4][1], cat8(pl[0], ph[0]), px[1]);
        SBAR();
        mm16(pacc[4][2], cat8(pl[0], ph[0]), px[2]);
        mm16(pacc[4][3], cat8(pl[0], ph[0]), px[3]);
        SBAR();
        pl[3] = ldstr(pw1[0] + 1536); ph[3] = ldstr(pw1[0] + 1536 + 256);
        mm16(pacc[5][0], cat8(pl[1], ph[1]), px[0]);
        mm16(pacc[5][1], cat8(pl[1], ph[1]), px[1]);
        SBAR();
        mm16(pacc[5][2], cat8(pl[1], ph[1]), px[2]);
        mm16(pacc[5][3], cat8(pl[1], ph[1]), px[3]);
        SBAR();
        mm16(pacc[6][0], cat8(pl[2], ph[2]), px[0]);
        mm16(pacc[6][1], cat8(pl[2], ph[2]), px[1]);
        SBAR();
        wait_vm_bar<16>();
        pl[0] = ldstr(pw0[0] + 32768); ph[0] = ldstr(pw0[0] + 32768 + 256);
        mm16(pacc[7][0], cat8(pl[3], ph[3]), px[0]);
        mm16(pacc[7][1], cat8(pl[3], ph[3]), px[1]);
        dma16c(l16, xsrc0 + 16384, ldsx + 0);
        SBAR();
        px[0] = lds128(pxr[0] + 32768); px[1] = lds128(pxr[0] + 33024); pl[1] = ldstr(pw1[0] + 32768); ph[1] = ldstr(pw1[0] + 32768 + 256);
        mm16(pacc[6][2], cat8(pl[2], ph[2]), px[2]);
        mm16(pacc[6][3], cat8(pl[2], ph[2]), px[3]);
        dma16c(l16, xsrc0 + 17408, ldsx + 1024);
        SBAR();
        mm16(pacc[7][2], cat8(pl[3], ph[3]), px[2]);
        mm16(pacc[7][3], cat8(pl[3], ph[3]), px[3]);
        dma16c(l16, xsrc1 + 16384, ldsx + 8192);
        SBAR();
        px[2] = lds128(pxr[0] + 40960); px[3] = lds128(pxr[0] + 41216);
        pl[2] = ldstr(pw0[0] + 33280); ph[2] = ldstr(pw0[0] + 33280 + 256);
        mm16(pacc[0][0], cat8(pl[0], ph[0]), px[0]);
        mm16(pacc[0][1], cat8(pl[0], ph[0]), px[1]);
        dma16c(l16, xsrc1 + 17408, ldsx + 9216);
        SBAR();
        mm16(pacc[1][0], cat8(pl[1], ph[1]), px[0]);
        mm16(pacc[1][1], cat8(pl[1], ph[1]), px[1]);
        dma16(pvo[0], psb[0] + (size_t)8 * pst[0], lds0 + 0 + pdst[0]);
        SBAR();
        pl[3] = ldstr(pw1[0] + 33280); ph[3] = ldstr(pw1[0] + 33280 + 256);
        mm16(pacc[0][2], cat8(pl[0], ph[0]), px[2]);
        mm16(pacc[0][3], cat8(pl[0], ph[0]), px[3]);
        dma16(pvo[1], psb[1] + (size_t)8 * pst[1], lds0 + 0 + pdst[1]);
        SBAR();
        mm16(pacc[1][2], cat8(pl[1], ph[1]), px[2]);
        mm16(pacc[1][3], cat8(pl[1], ph[1]), px[3]);
        dma16(pvo[2], psb[2] + (size_t)8 * pst[2], lds0 + 0 + pdst[2]);
        SBAR();
        pl[0] = ldstr(pw0[0] + 33792); ph[0] = ldstr(pw0[0] + 33792 + 256);
        mm16(pacc[2][0], cat8(pl[2], ph[2]), px[0]);
        mm16(pacc[2][1], cat8(pl[2], ph[2]), px[1]);
        dma16(pvo[3], psb[3] + (size_t)8 * pst[3], lds0 + 0 + pdst[3]);
        SBAR();
        mm16(pacc[2][2], cat8(pl[2], ph[2]), px[2]);
        mm16(pacc[2][3], cat8(pl[2], ph[2]), px[3]);
        *(LAS u32x4*)(hand + 0) = held[8];
        *(LAS u32x4*)(hand + 1024) = held[9];
        SBAR();
        pl[1] = ldstr(pw1[0] + 33792); ph[1] = ldstr(pw1[0] + 33792 + 256);
        mm16(pacc[3][0], cat8(pl[3], ph[3]), px[0]);
        mm16(pacc[3][1], cat8(pl[3], ph[3]), px[1]);
        *(LAS u32x4*)(hand + 2048) = held[10];
        *(LAS u32x4*)(hand + 3072) = held[11];
        SBAR();
        mm16(pacc[3][2], cat8(pl[3], ph[3]), px[2]);
        mm16(pacc[3][3], cat8(pl[3], ph[3]), px[3]);
        *(LAS u32x4*)(hand + 4096) = held[12];
        *(LAS u32x4*)(hand + 5120) = held[13];
        SBAR();
        pl[2] = ldstr(pw0[0] + 34304); ph[2] = ldstr(pw0[0] + 34304 + 256);
        mm16(pacc[4][0], cat8(pl[0], ph[0]), px[0]);
        mm16(pacc[4][1], cat8(pl[0], ph[0]), px[1]);
        *(LAS u32x4*)(hand + 6144) = held[14];
        *(LAS u32x4*)(hand + 7168) = held[15];
        SBAR();
        mm16(pacc[4][2], cat8(pl[0], ph[0]), px[2]);
        mm16(pacc[4][3], cat8(pl[0], ph[0]), px[3]);
        SBAR();
        pl[3] = ldstr(pw1[0] + 34304); ph[3] = ldstr(pw1[0] + 34304 + 256);
        mm16(pacc[5][0], cat8(pl[1], ph[1]), px[0]);
        mm16(pacc[5][1], cat8(pl[1], ph[1]), px[1]);
        SBAR();
        mm16(pacc[5][2], cat8(pl[1], ph[1]), px[2]);
        mm16(pacc[5][3], cat8(pl[1], ph[1]), px[3]);
        SBAR();
        mm16(pacc[6][0], cat8(pl[2], ph[2]), px[0]);
        mm16(pacc[6][1], cat8(pl[2], ph[2]), px[1]);
        SBAR();
        wait_vm_bar<16>();
        pl[0] = ldstr(pw0[1] + 0); ph[0] = ldstr(pw0[1] + 0 + 256);
        mm16(pacc[7][0], cat8(pl[3], ph[3]), px[0]);
        mm16(pacc[7][1], cat8(pl[3], ph[3]), px[1]);
        dma16c(l16, xsrc0 + 18432, ldsx + 32768);
        SBAR();
        px[0] = lds128(pxr[1] + 0); px[1] = lds128(pxr[1] + 256); pl[1] = ldstr(pw1[1] + 0); ph[1] = ldstr(pw1[1] + 0 + 256);
        mm16(pacc[6][2], cat8(pl[2], ph[2]), px[2]);
        mm16(pacc[6][3], cat8(pl[2], ph[2]), px[3]);
        dma16c(l16, xsrc0 + 19456, ldsx + 33792);
        SBAR();
        mm16(pacc[7][2], cat8(pl[3], ph[3]), px[2]);
        mm16(pacc[7][3], cat8(pl[3], ph[3]), px[3]);
        dma16c(l16, xsrc1 + 18432, ldsx + 40960);
        SBAR();
        px[2] = lds128(pxr[1] + 8192); px[3] = lds128(pxr[1] + 8448);
        pl[2] = ldstr(pw0[1] + 512); ph[2] = ldstr(pw0[1] + 512 + 256);
        mm16(pacc[0][0], cat8(pl[0], ph[0]), px[0]);
        mm16(pacc[0][1], cat8(pl[0], ph[0]), px[1]);
        dma16c(l16, xsrc1 + 19456, ldsx + 41984);
        SBAR();
        mm16(pacc[1][0], cat8(pl[1], ph[1]), px[0]);
        mm16(pacc[1][1], cat8(pl[1], ph[1]), px[1]);
        dma16(pvo[0], psb[0] + (size_t)9 * pst[0], lds0 + 32768 + pdst[0]);
        SBAR();
        pl[3] = ldstr(pw1[1] + 512); ph[3] = ldstr(pw1[1] + 512 + 256);
        mm16(pacc[0][2], cat8(pl[0], ph[0]), px[2]);
        mm16(pacc[0][3], cat8(pl[0], ph[0]), px[3]);
        dma16(pvo[1], psb[1] + (size_t)9 * pst[1], lds0 + 32768 + pdst[1]);
        SBAR();
        mm16(pacc[1][2], cat8(pl[1], ph[1]), px[2]);
        mm16(pacc[1][3], cat8(pl[1], ph[1]), px[3]);
        dma16(pvo[2], psb[2] + (size_t)9 * pst[2], lds0 + 32768 + pdst[2]);
        SBAR();
        pl[0] = ldstr(pw0[1] + 1024); ph[0] = ldstr(pw0[1] + 1024 + 256);
        mm16(pacc[2][0], cat8(pl[2], ph[2]), px[0]);
        mm16(pacc[2][1], cat8(pl[2], ph[2]), px[1]);
        dma16(pvo[3], psb[3] + (size_t)9 * pst[3], lds0 + 32768 + pdst[3]);
        SBAR();
        mm16(pacc[2][2], cat8(pl[2], ph[2]), px[2]);
        mm16(pacc[2][3], cat8(pl[2], ph[2]), px[3]);
        SBAR();
        pl[1] = ldstr(pw1[1] + 1024); ph[1] = ldstr(pw1[1] + 1024 + 256);
        mm16(pacc[3][0], cat8(pl[3], ph[3]), px[0]);
        mm16(pacc[3][1], cat8(pl[3], ph[3]), px[1]);
        SBAR();
        mm16(pacc[3][2], cat8(pl[3], ph[3]), px[2]);
        mm16(pacc[3][3], cat8(pl[3], ph[3]), px[3]);
        SBAR();
        pl[2] = ldstr(pw0[1] + 1536); ph[2] = ldstr(pw0[1] + 1536 + 256);
        mm16(pacc[4][0], cat8(pl[0], ph[0]), px[0]);
        mm16(pacc[4][1], cat8(pl[0], ph[0]), px[1]);
        SBAR();
        mm16(pacc[4][2], cat8(pl[0], ph[0]), px[2]);
        mm16(pacc[4][3], cat8(pl[0], ph[0]), px[3]);
        SBAR();
        pl[3] = ldstr(pw1[1] + 1536); ph[3] = ldstr(pw1[1] + 1536 + 256);
        mm16(pacc[5][0], cat8(pl[1], ph[1]), px[0]);
        mm16(pacc[5][1], cat8(pl[1], ph[1]), px[1]);
        SBAR();
        mm16(pacc[5][2], cat8(pl[1], ph[1]), px[2]);
        mm16(pacc[5][3], cat8(pl[1], ph[1]), px[3]);
        SBAR();
        mm16(pacc[6][0], cat8(pl[2], ph[2]), px[0]);
        mm16(pacc[6][1], cat8(pl[2], ph[2]), px[1]);
        SBAR();
        wait_vm_bar<16>();
        pl[0] = ldstr(pw0[1] + 32768); ph[0] = ldstr(pw0[1] + 32768 + 256);
        mm16(pacc[7][0], cat8(pl[3], ph[3]), px[0]);
        mm16(pacc[7][1], cat8(pl[3], ph[3]), px[1]);
        dma16c(l16, xsrc0 + 20480, ldsx + 65536);
        SBAR();
        px[0] = lds128(pxr[1] + 32768); px[1] = lds128(pxr[1] + 33024); pl[1] = ldstr(pw1[1] + 32768); ph[1] = ldstr(pw1[1] + 32768 + 256);
        mm16(pacc[6][2], cat8(pl[2], ph[2]), px[2]);
        mm16(pacc[6][3], cat8(pl[2], ph[2]), px[3]);
        dma16c(l16, xsrc0 + 21504, ldsx + 66560);
        SBAR();
        mm16(pacc[7][2], cat8(pl[3], ph[3]), px[2]);
        mm16(pacc[7][3], cat8(pl[3], ph[3]), px[3]);
        dma16c(l16, xsrc1 + 20480, ldsx + 73728);
        SBAR();
        px[2] = lds128(pxr[1] + 40960); px[3] = lds128(pxr[1] + 41216);
        pl[2] = ldstr(pw0[1] + 33280); ph[2] = ldstr(pw0[1] + 33280 + 256);
        mm16(pacc[0][0], cat8(pl[0], ph[0]), px[0]);
        mm16(pacc[0][1], cat8(pl[0], ph[0]), px[1]);
        dma16c(l16, xsrc1 + 21504, ldsx + 74752);
        SBAR();
        mm16(pacc[1][0], cat8(pl[1], ph[1]), px[0]);
        mm16(pacc[1][1], cat8(pl[1], ph[1]), px[1]);
        dma16(pvo[0], psb[0] + (size_t)10 * pst[0], lds0 + 65536 + pdst[0]);
        SBAR();
        pl[3] = ldstr(pw1[1] + 33280); ph[3] = ldstr(pw1[1] + 33280 + 256);
        mm16(pacc[0][2], cat8(pl[0], ph[0]), px[2]);
        mm16(pacc[0][3], cat8(pl[0], ph[0]), px[3]);
        dma16(pvo[1], psb[1] + (size_t)10 * pst[1], lds0 + 65536 + pdst[1]);
        SBAR();
        mm16(pacc[1][2], cat8(pl[1], ph[1]), px[2]);
        mm16(pacc[1][3], cat8(pl[1], ph[1]), px[3]);
        dma16(pvo[2], psb[2] + (size_t)10 * pst[2], lds0 + 65536 + pdst[2]);
        SBAR();
        pl[0] = ldstr(pw0[1] + 33792); ph[0] = ldstr(pw0[1] + 33792 + 256);
        mm16(pacc[2][0], cat8(pl[2], ph[2]), px[0]);
        mm16(pacc[2][1], cat8(pl[2], ph[2]), px[1]);
        dma16(pvo[3], psb[3] + (size_t)10 * pst[3], lds0 + 65536 + pdst[3]);
        SBAR();
        mm16(pacc[2][2], cat8(pl[2], ph[2]), px[2]);
        mm16(pacc[2][3], cat8(pl[2], ph[2]), px[3]);
        SBAR();
        pl[1] = ldstr(pw1[1] + 33792); ph[1] = ldstr(pw1[1] + 33792 + 256);
        mm16(pacc[3][0], cat8(pl[3], ph[3]), px[0]);
        mm16(pacc[3][1], cat8(pl[3], ph[3]), px[1]);
        SBAR();
        mm16(pacc[3][2], cat8(pl[3], ph[3]), px[2]);
        mm16(pacc[3][3], cat8(pl[3], ph[3]), px[3]);
        SBAR();
        pl[2] = ldstr(pw0[1] + 34304); ph[2] = ldstr(pw0[1] + 34304 + 256);
        mm16(pacc[4][0], cat8(pl[0], ph[0]), px[0]);
        mm16(pacc[4][1], cat8(pl[0], ph[0]), px[1]);
        SBAR();
        mm16(pacc[4][2], cat8(pl[0], ph[0]), px[2]);
        mm16(pacc[4][3], cat8(pl[0], ph[0]), px[3]);
        SBAR();
        pl[3] = ldstr(pw1[1] + 34304); ph[3] = ldstr(pw1[1] + 34304 + 256);
        mm16(pacc[5][0], cat8(pl[1], ph[1]), px[0]);
        mm16(pacc[5][1], cat8(pl[1], ph[1]), px[1]);
        SBAR();
        mm16(pacc[5][2], cat8(pl[1], ph[1]), px[2]);
        mm16(pacc[5][3], cat8(pl[1], ph[1]), px[3]);
        SBAR();
        mm16(pacc[6][0], cat8(pl[2], ph[2]), px[0]);
        mm16(pacc[6][1], cat8(pl[2], ph[2]), px[1]);
        SBAR();
        wait_vm_bar<16>();
        pl[0] = ldstr(pw0[0] + 0); ph[0] = ldstr(pw0[0] + 0 + 256);
        mm16(pacc[7][0], cat8(pl[3], ph[3]), px[0]);
        mm16(pacc[7][1], cat8(pl[3], ph[3]), px[1]);
        dma16c(l16, xsrc0 + 22528, ldsx + 98304);
        SBAR();
        px[0] = lds128(pxr[0] + 0); px[1] = lds128(pxr[0] + 256); pl[1] = ldstr(pw1[0] + 0); ph[1] = ldstr(pw1[0] + 0 + 256);
        mm16(pacc[6][2], cat8(pl[2], ph[2]), px[2]);
        mm16(pacc[6][3], cat8(pl[2], ph[2]), px[3]);
        dma16c(l16, xsrc0 + 23552, ldsx + 99328);
        SBAR();
        mm16(pacc[7][2], cat8(pl[3], ph[3]), px[2]);
        mm16(pacc[7][3], cat8(pl[3], ph[3]), px[3]);
        dma16c(l16, xsrc1 + 22528, ldsx + 106496);
        SBAR();
        px[2] = lds128(pxr[0] + 8192); px[3] = lds128(pxr[0] + 8448);
        pl[2] = ldstr(pw0[0] + 512); ph[2] = ldstr(pw0[0] + 512 + 256);
        mm16(pacc[0][0], cat8(pl[0], ph[0]), px[0]);
        mm16(pacc[0][1], cat8(pl[0], ph[0]), px[1]);
        dma16c(l16, xsrc1 + 23552, ldsx + 107520);
        SBAR();
        mm16(pacc[1][0], cat8(pl[1], ph[1]), px[0]);
        mm16(pacc[1][1], cat8(pl[1], ph[1]), px[1]);
        dma16(pvo[0], psb[0] + (size_t)11 * pst[0], lds0 + 98304 + pdst[0]);
        SBAR();
        pl[3] = ldstr(pw1[0] + 512); ph[3] = ldstr(pw1[0] + 512 + 256);
        mm16(pacc[0][2], cat8(pl[0], ph[0]), px[2]);
        mm16(pacc[0][3], cat8(pl[0], ph[0]), px[3]);
        dma16(pvo[1], psb[1] + (size_t)11 * pst[1], lds0 + 98304 + pdst[1]);
        SBAR();
        mm16(pacc[1][2], cat8(pl[1], ph[1]), px[2]);
        mm16(pacc[1][3], cat8(pl[1], ph[1]), px[3]);
        dma16(pvo[2], psb[2] + (size_t)11 * pst[2], lds0 + 98304 + pdst[2]);
        SBAR();
        pl[0] = ldstr(pw0[0] + 1024); ph[0] = ldstr(pw0[0] + 1024 + 256);
        mm16(pacc[2][0], cat8(pl[2], ph[2]), px[0]);
        mm16(pacc[2][1], cat8(pl[2], ph[2]), px[1]);
        dma16(pvo[3], psb[3] + (size_t)11 * pst[3], lds0 + 98304 + pdst[3]);
        SBAR();
        mm16(pacc[2][2], cat8(pl[2], ph[2]), px[2]);
        mm16(pacc[2][3], cat8(pl[2], ph[2]), px[3]);
        SBAR();
        pl[1] = ldstr(pw1[0] + 1024); ph[1] = ldstr(pw1[0] + 1024 + 256);
        mm16(pacc[3][0], cat8(pl[3], ph[3]), px[0]);
        mm16(pacc[3][1], cat8(pl[3], ph[3]), px[1]);
        SBAR();
        mm16(pacc[3][2], cat8(pl[3], ph[3]), px[2]);
        mm16(pacc[3][3], cat8(pl[3], ph[3]), px[3]);
        SBAR();
        pl[2] = ldstr(pw0[0] + 1536); ph[2] = ldstr(pw0[0] + 1536 + 256);
        mm16(pacc[4][0], cat8(pl[0], ph[0]), px[0]);
        mm16(pacc[4][1], cat8(pl[0], ph[0]), px[1]);
        SBAR();
        mm16(pacc[4][2], cat8(pl[0], ph[0]), px[2]);
        mm16(pacc[4][3], cat8(pl[0], ph[0]), px[3]);
        SBAR();
        pl[3] = ldstr(pw1[0] + 1536); ph[3] = ldstr(pw1[0] + 1536 + 256);
        mm16(pacc[5][0], cat8(pl[1], ph[1]), px[0]);
        mm16(pacc[5][1], cat8(pl[1], ph[1]), px[1]);
        SBAR();
        mm16(pacc[5][2], cat8(pl[1], ph[1]), px[2]);
        mm16(pacc[5][3], cat8(pl[1], ph[1]), px[3]);
        SBAR();
        mm16(pacc[6][0], cat8(pl[2], ph[2]), px[0]);
        mm16(pacc[6][1], cat8(pl[2], ph[2]), px[1]);
        SBAR();
        wait_vm_bar<16>();
        pl[0] = ldstr(pw0[0] + 32768); ph[0] = ldstr(pw0[0] + 32768 + 256);
        mm16(pacc[7][0], cat8(pl[3], ph[3]), px[0]);
        mm16(pacc[7][1], cat8(pl[3], ph[3]), px[1]);
        dma16c(l16, xsrc0 + 24576, ldsx + 0);
        SBAR();
        px[0] = lds128(pxr[0] + 32768); px[1] = lds128(pxr[0] + 33024); pl[1] = ldstr(pw1[0] + 32768); ph[1] = ldstr(pw1[0] + 32768 + 256);
        mm16(pacc[6][2], cat8(pl[2], ph[2]), px[2]);
        mm16(pacc[6][3], cat8(pl[2], ph[2]), px[3]);
        dma16c(l16, xsrc0 + 25600, ldsx + 1024);
        SBAR();
        mm16(pacc[7][2], cat8(pl[3], ph[3]), px[2]);
        mm16(pacc[7][3], cat8(pl[3], ph[3]), px[3]);
        dma16c(l16, xsrc1 + 24576, ldsx + 8192);
        SBAR();
        px[2] = lds128(pxr[0] + 40960); px[3] = lds128(pxr[0] + 41216);
        pl[2] = ldstr(pw0[0] + 33280); ph[2] = ldstr(pw0[0] + 33280 + 256);
        mm16(pacc[0][0], cat8(pl[0], ph[0]), px[0]);
        mm16(pacc[0][1], cat8(pl[0], ph[0]), px[1]);
        dma16c(l16, xsrc1 + 25600, ldsx + 9216);
        SBAR();
        mm16(pacc[1][0], cat8(pl[1], ph[1]), px[0]);
        mm16(pacc[1][1], cat8(pl[1], ph[1]), px[1]);
        dma16(pvo[0], psb[0] + (size_t)12 * pst[0], lds0 + 0 + pdst[0]);
        SBAR();
        pl[3] = ldstr(pw1[0] + 33280); ph[3] = ldstr(pw1[0] + 33280 + 256);
        mm16(pacc[0][2], cat8(pl[0], ph[0]), px[2]);
        mm16(pacc[0][3], cat8(pl[0], ph[0]), px[3]);
        dma16(pvo[1], psb[1] + (size_t)12 * pst[1], lds0 + 0 + pdst[1]);
        SBAR();
        mm16(pacc[1][2], cat8(pl[1], ph[1]), px[2]);
        mm16(pacc[1][3], cat8(pl[1], ph[1]), px[3]);
        dma16(pvo[2], psb[2] + (size_t)12 * pst[2], lds0 + 0 + pdst[2]);
        SBAR();
        pl[0] = ldstr(pw0[0] + 33792); ph[0] = ldstr(pw0[0] + 33792 + 256);
        mm16(pacc[2][0], cat8(pl[2], ph[2]), px[0]);
        mm16(pacc[2][1], cat8(pl[2], ph[2]), px[1]);
        dma16(pvo[3], psb[3] + (size_t)12 * pst[3], lds0 + 0 + pdst[3]);
        SBAR();
        mm16(pacc[2][2], cat8(pl[2], ph[2]), px[2]);
        mm16(pacc[2][3], cat8(pl[2], ph[2]), px[3]);
        SBAR();
        pl[1] = ldstr(pw1[0] + 33792); ph[1] = ldstr(pw1[0] + 33792 + 256);
        mm16(pacc[3][0], cat8(pl[3], ph[3]), px[0]);
        mm16(pacc[3][1], cat8(pl[3], ph[3]), px[1]);
        SBAR();
        mm16(pacc[3][2], cat8(pl[3], ph[3]), px[2]);
        mm16(pacc[3][3], cat8(pl[3], ph[3]), px[3]);
        SBAR();
        pl[2] = ldstr(pw0[0] + 34304); ph[2] = ldstr(pw0[0] + 34304 + 256);
        mm16(pacc[4][0], cat8(pl[0], ph[0]), px[0]);
        mm16(pacc[4][1], cat8(pl[0], ph[0]), px[1]);
        SBAR();
        mm16(pacc[4][2], cat8(pl[0], ph[0]), px[2]);
        mm16(pacc[4][3], cat8(pl[0], ph[0]), px[3]);
        SBAR();
        pl[3] = ldstr(pw1[0] + 34304); ph[3] = ldstr(pw1[0] + 34304 + 256);
        mm16(pacc[5][0], cat8(pl[1], ph[1]), px[0]);
        mm16(pacc[5][1], cat8(pl[1], ph[1]), px[1]);
        SBAR();
        mm16(pacc[5][2], cat8(pl[1], ph[1]), px[2]);
        mm16(pacc[5][3], cat8(pl[1], ph[1]), px[3]);
        SBAR();
        mm16(pacc[6][0], cat8(pl[2], ph[2]), px[0]);
        mm16(pacc[6][1], cat8(pl[2], ph[2]), px[1]);
        SBAR();
        wait_vm_bar<16>();
        pl[0] = ldstr(pw0[1] + 0); ph[0] = ldstr(pw0[1] + 0 + 256);
        mm16(pacc[7][0], cat8(pl[3], ph[3]), px[0]);
        mm16(pacc[7][1], cat8(pl[3], ph[3]), px[1]);
        dma16c(l16, xsrc0 + 26624, ldsx + 32768);
        SBAR();
        px[0] = lds128(pxr[1] + 0); px[1] = lds128(pxr[1] + 256); pl[1] = ldstr(pw1[1] + 0); ph[1] = ldstr(pw1[1] + 0 + 256);
        mm16(pacc[6][2], cat8(pl[2], ph[2]), px[2]);
        mm16(pacc[6][3], cat8(pl[2], ph[2]), px[3]);
        dma16c(l16, xsrc0 + 27648, ldsx + 33792);
        SBAR();
        mm16(pacc[7][2], cat8(pl[3], ph[3]), px[2]);
        mm16(pacc[7][3], cat8(pl[3], ph[3]), px[3]);
        dma16c(l16, xsrc1 + 26624, ldsx + 40960);
        SBAR();
        px[2] = lds128(pxr[1] + 8192); px[3] = lds128(pxr[1] + 8448);
        pl[2] = ldstr(pw0[1] + 512); ph[2] = ldstr(pw0[1] + 512 + 256);
        mm16(pacc[0][0], cat8(pl[0], ph[0]), px[0]);
        mm16(pacc[0][1], cat8(pl[0], ph[0]), px[1]);
        dma16c(l16, xsrc1 + 27648, ldsx + 41984);
        SBAR();
        mm16(pacc[1][0], cat8(pl[1], ph[1]), px[0]);
        mm16(pacc[1][1], cat8(pl[1], ph[1]), px[1]);
        dma16(pvo[0], psb[0] + (size_t)13 * pst[0], lds0 + 32768 + pdst[0]);
        SBAR();
        pl[3] = ldstr(pw1[1] + 512); ph[3] = ldstr(pw1[1] + 512 + 256);
        mm16(pacc[0][2], cat8(pl[0], ph[0]), px[2]);
        mm16(pacc[0][3], cat8(pl[0], ph[0]), px[3]);
        dma16(pvo[1], psb[1] + (size_t)13 * pst[1], lds0 + 32768 + pdst[1]);
        SBAR();
        mm16(pacc[1][2], cat8(pl[1], ph[1]), px[2]);
        mm16(pacc[1][3], cat8(pl[1], ph[1]), px[3]);
        dma16(pvo[2], psb[2] + (size_t)13 * pst[2], lds0 + 32768 + pdst[2]);
        SBAR();
        pl[0] = ldstr(pw0[1] + 1024); ph[0] = ldstr(pw0[1] + 1024 + 256);
        mm16(pacc[2][0], cat8(pl[2], ph[2]), px[0]);
        mm16(pacc[2][1], cat8(pl[2], ph[2]), px[1]);
        dma16(pvo[3], psb[3] + (size_t)13 * pst[3], lds0 + 32768 + pdst[3]);
        SBAR();
        mm16(pacc[2][2], cat8(pl[2], ph[2]), px[2]);
        mm16(pacc[2][3], cat8(pl[2], ph[2]), px[3]);
        SBAR();
        pl[1] = ldstr(pw1[1] + 1024); ph[1] = ldstr(pw1[1] + 1024 + 256);
        mm16(pacc[3][0], cat8(pl[3], ph[3]), px[0]);
        mm16(pacc[3][1], cat8(pl[3], ph[3]), px[1]);
        SBAR();
        mm16(pacc[3][2], cat8(pl[3], ph[3]), px[2]);
        mm16(pacc[3][3], cat8(pl[3], ph[3]), px[3]);
        SBAR();
        pl[2] = ldstr(pw0[1] + 1536); ph[2] = ldstr(pw0[1] + 1536 + 256);
        mm16(pacc[4][0], cat8(pl[0], ph[0]), px[0]);
        mm16(pacc[4][1], cat8(pl[0], ph[0]), px[1]);
        SBAR();
        mm16(pacc[4][2], cat8(pl[0], ph[0]), px[2]);
        mm16(pacc[4][3], cat8(pl[0], ph[0]), px[3]);
        SBAR();
        pl[3] = ldstr(pw1[1] + 1536); ph[3] = ldstr(pw1[1] + 1536 + 256);
        mm16(pacc[5][0], cat8(pl[1], ph[1]), px[0]);
        mm16(pacc[5][1], cat8(pl[1], ph[1]), px[1]);
        SBAR();
        mm16(pacc[5][2], cat8(pl[1], ph[1]), px[2]);
        mm16(pacc[5][3], cat8(pl[1], ph[1]), px[3]);
        SBAR();
        mm16(pacc[6][0], cat8(pl[2], ph[2]), px[0]);
        mm16(pacc[6][1], cat8(pl[2], ph[2]), px[1]);
        SBAR();
        wait_vm_bar<16>();
        pl[0] = ldstr(pw0[1] + 32768); ph[0] = ldstr(pw0[1] + 32768 + 256);
        mm16(pacc[7][0], cat8(pl[3], ph[3]), px[0]);
        mm16(pacc[7][1], cat8(pl[3], ph[3]), px[1]);
        dma16c(l16, xsrc0 + 28672, ldsx + 65536);
        SBAR();
        px[0] = lds128(pxr[1] + 32768); px[1] = lds128(pxr[1] + 33024); pl[1] = ldstr(pw1[1] + 32768); ph[1] = ldstr(pw1[1] + 32768 + 256);
        mm16(pacc[6][2], cat8(pl[2], ph[2]), px[2]);
        mm16(pacc[6][3], cat8(pl[2], ph[2]), px[3]);
        dma16c(l16, xsrc0 + 29696, ldsx + 66560);
        SBAR();
        mm16(pacc[7][2], cat8(pl[3], ph[3]), px[2]);
        mm16(pacc[7][3], cat8(pl[3], ph[3]), px[3]);
        dma16c(l16, xsrc1 + 28672, ldsx + 73728);
        SBAR();
        px[2] = lds128(pxr[1] + 40960); px[3] = lds128(pxr[1] + 41216);
        pl[2] = ldstr(pw0[1] + 33280); ph[2] = ldstr(pw0[1] + 33280 + 256);
        mm16(pacc[0][0], cat8(pl[0], ph[0]), px[0]);
        mm16(pacc[0][1], cat8(pl[0], ph[0]), px[1]);
        dma16c(l16, xsrc1 + 29696, ldsx + 74752);
        SBAR();
        mm16(pacc[1][0], cat8(pl[1], ph[1]), px[0]);
        mm16(pacc[1][1], cat8(pl[1], ph[1]), px[1]);
        dma16(pvo[0], psb[0] + (size_t)14 * pst[0], lds0 + 65536 + pdst[0]);
        SBAR();
        pl[3] = ldstr(pw1[1] + 33280); ph[3] = ldstr(pw1[1] + 33280 + 256);
        mm16(pacc[0][2], cat8(pl[0], ph[0]), px[2]);
        mm16(pacc[0][3], cat8(pl[0], ph[0]), px[3]);
        dma16(pvo[1], psb[1] + (size_t)14 * pst[1], lds0 + 65536 + pdst[1]);
        SBAR();
        mm16(pacc[1][2], cat8(pl[1], ph[1]), px[2]);
        mm16(pacc[1][3], cat8(pl[1], ph[1]), px[3]);
        dma16(pvo[2], psb[2] + (size_t)14 * pst[2], lds0 + 65536 + pdst[2]);
        SBAR();
        pl[0] = ldstr(pw0[1] + 33792); ph[0] = ldstr(pw0[1] + 33792 + 256);
        mm16(pacc[2][0], cat8(pl[2], ph[2]), px[0]);
        mm16(pacc[2][1], cat8(pl[2], ph[2]), px[1]);
        dma16(pvo[3], psb[3] + (size_t)14 * pst[3], lds0 + 65536 + pdst[3]);
        SBAR();
        mm16(pacc[2][2], cat8(pl[2], ph[2]), px[2]);
        mm16(pacc[2][3], cat8(pl[2], ph[2]), px[3]);
        SBAR();
        pl[1] = ldstr(pw1[1] + 33792); ph[1] = ldstr(pw1[1] + 33792 + 256);
        mm16(pacc[3][0], cat8(pl[3], ph[3]), px[0]);
        mm16(pacc[3][1], cat8(pl[3], ph[3]), px[1]);
        SBAR();
        mm16(pacc[3][2], cat8(pl[3], ph[3]), px[2]);
        mm16(pacc[3][3], cat8(pl[3], ph[3]), px[3]);
        SBAR();
        pl[2] = ldstr(pw0[1] + 34304); ph[2] = ldstr(pw0[1] + 34304 + 256);
        mm16(pacc[4][0], cat8(pl[0], ph[0]), px[0]);
        mm16(pacc[4][1], cat8(pl[0], ph[0]), px[1]);
        SBAR();
        mm16(pacc[4][2], cat8(pl[0], ph[0]), px[2]);
        mm16(pacc[4][3], cat8(pl[0], ph[0]), px[3]);
        SBAR();
        pl[3] = ldstr(pw1[1] + 34304); ph[3] = ldstr(pw1[1] + 34304 + 256);
        mm16(pacc[5][0], cat8(pl[1], ph[1]), px[0]);
        mm16(pacc[5][1], cat8(pl[1], ph[1]), px[1]);
        SBAR();
        mm16(pacc[5][2], cat8(pl[1], ph[1]), px[2]);
        mm16(pacc[5][3], cat8(pl[1], ph[1]), px[3]);
        SBAR();
        mm16(pacc[6][0], cat8(pl[2], ph[2]), px[0]);
        mm16(pacc[6][1], cat8(pl[2], ph[2]), px[1]);
        SBAR();
        wait_vm_bar<16>();
        pl[0] = ldstr(pw0[0] + 0); ph[0] = ldstr(pw0[0] + 0 + 256);
        mm16(pacc[7][0], cat8(pl[3], ph[3]), px[0]);
        mm16(pacc[7][1], cat8(pl[3], ph[3]), px[1]);
        dma16c(l16, xsrc0 + 30720, ldsx + 98304);
        SBAR();
        px[0] = lds128(pxr[0] + 0); px[1] = lds128(pxr[0] + 256); pl[1] = ldstr(pw1[0] + 0); ph[1] = ldstr(pw1[0] + 0 + 256);
        mm16(pacc[6][2], cat8(pl[2], ph[2]), px[2]);
        mm16(pacc[6][3], cat8(pl[2], ph[2]), px[3]);
        dma16c(l16, xsrc0 + 31744, ldsx + 99328);
        SBAR();
        mm16(pacc[7][2], cat8(pl[3], ph[3]), px[2]);
        mm16(pacc[7][3], cat8(pl[3], ph[3]), px[3]);
        dma16c(l16, xsrc1 + 30720, ldsx + 106496);
        SBAR();
        px[2] = lds128(pxr[0] + 8192); px[3] = lds128(pxr[0] + 8448);
        pl[2] = ldstr(pw0[0] + 512); ph[2] = ldstr(pw0[0] + 512 + 256);
        mm16(pacc[0][0], cat8(pl[0], ph[0]), px[0]);
        mm16(pacc[0][1], cat8(pl[0], ph[0]), px[1]);
        dma16c(l16, xsrc1 + 31744, ldsx + 107520);
        SBAR();
        mm16(pacc[1][0], cat8(pl[1], ph[1]), px[0]);
        mm16(pacc[1][1], cat8(pl[1], ph[1]), px[1]);
        dma16(pvo[0], psb[0] + (size_t)15 * pst[0], lds0 + 98304 + pdst[0]);
        SBAR();
        pl[3] = ldstr(pw1[0] + 512); ph[3] = ldstr(pw1[0] + 512 + 256);
        mm16(pacc[0][2], cat8(pl[0], ph[0]), px[2]);
        mm16(pacc[0][3], cat8(pl[0], ph[0]), px[3]);
        dma16(pvo[1], psb[1] + (size_t)15 * pst[1], lds0 + 98304 + pdst[1]);
        SBAR();
        mm16(pacc[1][2], cat8(pl[1], ph[1]), px[2]);
        mm16(pacc[1][3], cat8(pl[1], ph[1]), px[3]);
        dma16(pvo[2], psb[2] + (size_t)15 * pst[2], lds0 + 98304 + pdst[2]);
        SBAR();
        pl[0] = ldstr(pw0[0] + 1024); ph[0] = ldstr(pw0[0] + 1024 + 256);
        mm16(pacc[2][0], cat8(pl[2], ph[2]), px[0]);
        mm16(pacc[2][1], cat8(pl[2], ph[2]), px[1]);
        dma16(pvo[3], psb[3] + (size_t)15 * pst[3], lds0 + 98304 + pdst[3]);
        SBAR();
        mm16(pacc[2][2], cat8(pl[2], ph[2]), px[2]);
        mm16(pacc[2][3], cat8(pl[2], ph[2]), px[3]);
        SBAR();
        pl[1] = ldstr(pw1[0] + 1024); ph[1] = ldstr(pw1[0] + 1024 + 256);
        mm16(pacc[3][0], cat8(pl[3], ph[3]), px[0]);
        mm16(pacc[3][1], cat8(pl[3], ph[3]), px[1]);
        SBAR();
        mm16(pacc[3][2], cat8(pl[3], ph[3]), px[2]);
        mm16(pacc[3][3], cat8(pl[3], ph[3]), px[3]);
        SBAR();
        pl[2] = ldstr(pw0[0] + 1536); ph[2] = ldstr(pw0[0] + 1536 + 256);
        mm16(pacc[4][0], cat8(pl[0], ph[0]), px[0]);
        mm16(pacc[4][1], cat8(pl[0], ph[0]), px[1]);
        SBAR();
        mm16(pacc[4][2], cat8(pl[0], ph[0]), px[2]);
        mm16(pacc[4][3], cat8(pl[0], ph[0]), px[3]);
        SBAR();
        pl[3] = ldstr(pw1[0] + 1536); ph[3] = ldstr(pw1[0] + 1536 + 256);
        mm16(pacc[5][0], cat8(pl[1], ph[1]), px[0]);
        mm16(pacc[5][1], cat8(pl[1], ph[1]), px[1]);
        SBAR();
        mm16(pacc[5][2], cat8(pl[1], ph[1]), px[2]);
        mm16(pacc[5][3], cat8(pl[1], ph[1]), px[3]);
        SBAR();
        mm16(pacc[6][0], cat8(pl[2], ph[2]), px[0]);
        mm16(pacc[6][1], cat8(pl[2], ph[2]), px[1]);
        SBAR();
        wait_vm_bar<16>();
        pl[0] = ldstr(pw0[0] + 32768); ph[0] = ldstr(pw0[0] + 32768 + 256);
        mm16(pacc[7][0], cat8(pl[3], ph[3]), px[0]);
        mm16(pacc[7][1], cat8(pl[3], ph[3]), px[1]);
        pv = poll_issue(fbase + F_KV * 32768, pvoff_kv & 0x1fffu);
        SBAR();
        px[0] = lds128(pxr[0] + 32768); px[1] = lds128(pxr[0] + 33024); pl[1] = ldstr(pw1[0] + 32768); ph[1] = ldstr(pw1[0] + 32768 + 256);
        mm16(pacc[6][2], cat8(pl[2], ph[2]), px[2]);
        mm16(pacc[6][3], cat8(pl[2], ph[2]), px[3]);
        SBAR();
        mm16(pacc[7][2], cat8(pl[3], ph[3]), px[2]);
        mm16(pacc[7][3], cat8(pl[3], ph[3]), px[3]);
        SBAR();
        px[2] = lds128(pxr[0] + 40960); px[3] = lds128(pxr[0] + 41216);
        pl[2] = ldstr(pw0[0] + 33280); ph[2] = ldstr(pw0[0] + 33280 + 256);
        mm16(pacc[0][0], cat8(pl[0], ph[0]), px[0]);
        mm16(pacc[0][1], cat8(pl[0], ph[0]), px[1]);
        SBAR();
        mm16(pacc[1][0], cat8(pl[1], ph[1]), px[0]);
        mm16(pacc[1][1], cat8(pl[1], ph[1]), px[1]);
        SBAR();
        pl[3] = ldstr(pw1[0] + 33280); ph[3] = ldstr(pw1[0] + 33280 + 256);
        mm16(pacc[0][2], cat8(pl[0], ph[0]), px[2]);
        mm16(pacc[0][3], cat8(pl[0], ph[0]), px[3]);
        SBAR();
        mm16(pacc[1][2], cat8(pl[1], ph[1]), px[2]);
        mm16(pacc[1][3], cat8(pl[1], ph[1]), px[3]);
        SBAR();
        pl[0] = ldstr(pw0[0] + 33792); ph[0] = ldstr(pw0[0] + 33792 + 256);
        mm16(pacc[2][0], cat8(pl[2], ph[2]), px[0]);
        mm16(pacc[2][1], cat8(pl[2], ph[2]), px[1]);
        SBAR();
        mm16(pacc[2][2], cat8(pl[2], ph[2]), px[2]);
        mm16(pacc[2][3], cat8(pl[2], ph[2]), px[3]);
        SBAR();
        pl[1] = ldstr(pw1[0] + 33792); ph[1] = ldstr(pw1[0] + 33792 + 256);
        mm16(pacc[3][0], cat8(pl[3], ph[3]), px[0]);
        mm16(pacc[3][1], cat8(pl[3], ph[3]), px[1]);
        SBAR();
        mm16(pacc[3][2], cat8(pl[3], ph[3]), px[2]);
        mm16(pacc[3][3], cat8(pl[3], ph[3]), px[3]);
        SBAR();
        pl[2] = ldstr(pw0[0] + 34304); ph[2] = ldstr(pw0[0] + 34304 + 256);
        mm16(pacc[4][0], cat8(pl[0], ph[0]), px[0]);
        mm16(pacc[4][1], cat8(pl[0], ph[0]), px[1]);
        SBAR();
        mm16(pacc[4][2], cat8(pl[0], ph[0]), px[2]);
        mm16(pacc[4][3], cat8(pl[0], ph[0]), px[3]);
        SBAR();
        pl[3] = ldstr(pw1[0] + 34304); ph[3] = ldstr(pw1[0] + 34304 + 256);
        mm16(pacc[5][0], cat8(pl[1], ph[1]), px[0]);
        mm16(pacc[5][1], cat8(pl[1], ph[1]), px[1]);
        SBAR();
        mm16(pacc[5][2], cat8(pl[1], ph[1]), px[2]);
        mm16(pacc[5][3], cat8(pl[1], ph[1]), px[3]);
        SBAR();
        mm16(pacc[6][0], cat8(pl[2], ph[2]), px[0]);
        mm16(pacc[6][1], cat8(pl[2], ph[2]), px[1]);
        SBAR();
        wait_vm_bar<9>();
        pl[0] = ldstr(pw0[1] + 0); ph[0] = ldstr(pw0[1] + 0 + 256);
        mm16(pacc[7][0], cat8(pl[3], ph[3]), px[0]);
        mm16(pacc[7][1], cat8(pl[3], ph[3]), px[1]);
        SBAR();
        px[0] = lds128(pxr[1] + 0); px[1] = lds128(pxr[1] + 256); pl[1] = ldstr(pw1[1] + 0); ph[1] = ldstr(pw1[1] + 0 + 256);
        mm16(pacc[6][2], cat8(pl[2], ph[2]), px[2]);
        mm16(pacc[6][3], cat8(pl[2], ph[2]), px[3]);
        SBAR();
        mm16(pacc[7][2], cat8(pl[3], ph[3]), px[2]);
        mm16(pacc[7][3], cat8(pl[3], ph[3]), px[3]);
        SBAR();
        px[2] = lds128(pxr[1] + 8192); px[3] = lds128(pxr[1] + 8448);
        pl[2] = ldstr(pw0[1] + 512); ph[2] = ldstr(pw0[1] + 512 + 256);
        mm16(pacc[0][0], cat8(pl[0], ph[0]), px[0]);
        mm16(pacc[0][1], cat8(pl[0], ph[0]), px[1]);
        SBAR();
        mm16(pacc[1][0], cat8(pl[1], ph[1]), px[0]);
        mm16(pacc[1][1], cat8(pl[1], ph[1]), px[1]);
        SBAR();
        pl[3] = ldstr(pw1[1] + 512); ph[3] = ldstr(pw1[1] + 512 + 256);
        mm16(pacc[0][2], cat8(pl[0], ph[0]), px[2]);
        mm16(pacc[0][3], cat8(pl[0], ph[0]), px[3]);
        SBAR();
        mm16(pacc[1][2], cat8(pl[1], ph[1]), px[2]);
        mm16(pacc[1][3], cat8(pl[1], ph[1]), px[3]);
        SBAR();
        pl[0] = ldstr(pw0[1] + 1024); ph[0] = ldstr(pw0[1] + 1024 + 256);
        mm16(pacc[2][0], cat8(pl[2], ph[2]), px[0]);
        mm16(pacc[2][1], cat8(pl[2], ph[2]), px[1]);
        SBAR();
        mm16(pacc[2][2], cat8(pl[2], ph[2]), px[2]);
        mm16(pacc[2][3], cat8(pl[2], ph[2]), px[3]);
        SBAR();
        pl[1] = ldstr(pw1[1] + 1024); ph[1] = ldstr(pw1[1] + 1024 + 256);
        mm16(pacc[3][0], cat8(pl[3], ph[3]), px[0]);
        mm16(pacc[3][1], cat8(pl[3], ph[3]), px[1]);
        SBAR();
        mm16(pacc[3][2], cat8(pl[3], ph[3]), px[2]);
        mm16(pacc[3][3], cat8(pl[3], ph[3]), px[3]);
        SBAR();
        pl[2] = ldstr(pw0[1] + 1536); ph[2] = ldstr(pw0[1] + 1536 + 256);
        mm16(pacc[4][0], cat8(pl[0], ph[0]), px[0]);
        mm16(pacc[4][1], cat8(pl[0], ph[0]), px[1]);
        SBAR();
        mm16(pacc[4][2], cat8(pl[0], ph[0]), px[2]);
        mm16(pacc[4][3], cat8(pl[0], ph[0]), px[3]);
        SBAR();
        pl[3] = ldstr(pw1[1] + 1536); ph[3] = ldstr(pw1[1] + 1536 + 256);
        mm16(pacc[5][0], cat8(pl[1], ph[1]), px[0]);
        mm16(pacc[5][1], cat8(pl[1], ph[1]), px[1]);
        SBAR();
        mm16(pacc[5][2], cat8(pl[1], ph[1]), px[2]);
        mm16(pacc[5][3], cat8(pl[1], ph[1]), px[3]);
        SBAR();
        mm16(pacc[6][0], cat8(pl[2], ph[2]), px[0]);
        mm16(pacc[6][1], cat8(pl[2], ph[2]), px[1]);
        SBAR();
        wait_vm_bar<0>();
        poll_check(pv, fbase + F_KV * 32768, pvoff_kv & 0x1fffu, tag);
        pl[0] = ldstr(pw0[1] + 32768); ph[0] = ldstr(pw0[1] + 32768 + 256);
        mm16(pacc[7][0], cat8(pl[3], ph[3]), px[0]);
        mm16(pacc[7][1], cat8(pl[3], ph[3]), px[1]);
        dmab<0>(kvoff, ksrd, lds0w);
        SBAR();
        px[0] = lds128(pxr[1] + 32768); px[1] = lds128(pxr[1] + 33024); pl[1] = ldstr(pw1[1] + 32768); ph[1] = ldstr(pw1[1] + 32768 + 256);
        mm16(pacc[6][2], cat8(pl[2], ph[2]), px[2]);
        mm16(pacc[6][3], cat8(pl[2], ph[2]), px[3]);
        dmab<1024>(kvoff, ksrd, lds0w);
        SBAR();
        mm16(pacc[7][2], cat8(pl[3], ph[3]), px[2]);
        mm16(pacc[7][3], cat8(pl[3], ph[3]), px[3]);
        dmab<2048>(kvoff, ksrd, lds0w);
        SBAR();
        px[2] = lds128(pxr[1] + 40960); px[3] = lds128(pxr[1] + 41216);
        pl[2] = ldstr(pw0[1] + 33280); ph[2] = ldstr(pw0[1] + 33280 + 256);
        mm16(pacc[0][0], cat8(pl[0], ph[0]), px[0]);
        mm16(pacc[0][1], cat8(pl[0], ph[0]), px[1]);
        dmab<3072>(kvoff, ksrd, lds0w);
        SBAR();
        mm16(pacc[1][0], cat8(pl[1], ph[1]), px[0]);
        mm16(pacc[1][1], cat8(pl[1], ph[1]), px[1]);
        kad += 16384u; ksrd[0] = (unsigned)kad; ksrd[1] = (unsigned)(kad >> 32);
        dmab<0>(kvoff, ksrd, lds0w + 32768);
        SBAR();
        pl[3] = ldstr(pw1[1] + 33280); ph[3] = ldstr(pw1[1] + 33280 + 256);
        mm16(pacc[0][2], cat8(pl[0], ph[0]), px[2]);
        mm16(pacc[0][3], cat8(pl[0], ph[0]), px[3]);
        dmab<1024>(kvoff, ksrd, lds0w + 32768);
        SBAR();
        mm16(pacc[1][2], cat8(pl[1], ph[1]), px[2]);
        mm16(pacc[1][3], cat8(pl[1], ph[1]), px[3]);
        dmab<2048>(kvoff, ksrd, lds0w + 32768);
        SBAR();
        pl[0] = ldstr(pw0[1] + 33792); ph[0] = ldstr(pw0[1] + 33792 + 256);
        mm16(pacc[2][0], cat8(pl[2], ph[2]), px[0]);
        mm16(pacc[2][1], cat8(pl[2], ph[2]), px[1]);
        dmab<3072>(kvoff, ksrd, lds0w + 32768);
        SBAR();
        mm16(pacc[2][2], cat8(pl[2], ph[2]), px[2]);
        mm16(pacc[2][3], cat8(pl[2], ph[2]), px[3]);
        dmab<0>(kvoff, vsrd, lds0w + 65536);
        SBAR();
        pl[1] = ldstr(pw1[1] + 33792); ph[1] = ldstr(pw1[1] + 33792 + 256);
        mm16(pacc[3][0], cat8(pl[3], ph[3]), px[0]);
        mm16(pacc[3][1], cat8(pl[3], ph[3]), px[1]);
        SBAR();
        *(LAS u32x4*)(qbw + 0) = pack2(pacc[0][2] * QSCALE, pacc[1][2] * QSCALE);
        dmab<1024>(kvoff, vsrd, lds0w + 65536);
        SBAR();
        mm16(pacc[3][2], cat8(pl[3], ph[3]), px[2]);
        mm16(pacc[3][3], cat8(pl[3], ph[3]), px[3]);
        SBAR();
        *(LAS u32x4*)(qbw + 256) = pack2(pacc[0][3] * QSCALE, pacc[1][3] * QSCALE);
        dmab<2048>(kvoff, vsrd, lds0w + 65536);
        SBAR();
        pl[2] = ldstr(pw0[1] + 34304); ph[2] = ldstr(pw0[1] + 34304 + 256);
        mm16(pacc[4][0], cat8(pl[0], ph[0]), px[0]);
        mm16(pacc[4][1], cat8(pl[0], ph[0]), px[1]);
        SBAR();
        *(LAS u32x4*)(qaw + kvo + 0) = pack2(pacc[0][0] * QSCALE, pacc[1][0] * QSCALE);
        dmab<3072>(kvoff, vsrd, lds0w + 65536);
        SBAR();
        mm16(pacc[4][2], cat8(pl[0], ph[0]), px[2]);
        mm16(pacc[4][3], cat8(pl[0], ph[0]), px[3]);
        SBAR();
        *(LAS u32x4*)(qaw + kvo + 256) = pack2(pacc[0][1] * QSCALE, pacc[1][1] * QSCALE);
        SBAR();
        pl[3] = ldstr(pw1[1] + 34304); ph[3] = ldstr(pw1[1] + 34304 + 256);
        mm16(pacc[5][0], cat8(pl[1], ph[1]), px[0]);
        mm16(pacc[5][1], cat8(pl[1], ph[1]), px[1]);
        SBAR();
        *(LAS u32x4*)(qbw + 2048) = pack2(pacc[2][2] * QSCALE, pacc[3][2] * QSCALE);
        SBAR();
        mm16(pacc[5][2], cat8(pl[1], ph[1]), px[2]);
        mm16(pacc[5][3], cat8(pl[1], ph[1]), px[3]);
        SBAR();
        *(LAS u32x4*)(qbw + 2304) = pack2(pacc[2][3] * QSCALE, pacc[3][3] * QSCALE);
        *(LAS u32x4*)(qaw + kvo + 2048) = pack2(pacc[2][0] * QSCALE, pacc[3][0] * QSCALE);
        SBAR();
        mm16(pacc[6][0], cat8(pl[2], ph[2]), px[0]);
        mm16(pacc[6][1], cat8(pl[2], ph[2]), px[1]);
        SBAR();
        *(LAS u32x4*)(qaw + kvo + 2304) = pack2(pacc[2][1] * QSCALE, pacc[3][1] * QSCALE);
        SBAR();
        mm16(pacc[7][0], cat8(pl[3], ph[3]), px[0]);
        mm16(pacc[7][1], cat8(pl[3], ph[3]), px[1]);
        SBAR();
        *(LAS u32x4*)(qbw + 4096) = pack2(pacc[4][2] * QSCALE, pacc[5][2] * QSCALE);
        *(LAS u32x4*)(qbw + 4352) = pack2(pacc[4][3] * QSCALE, pacc[5][3] * QSCALE);
        SBAR();
        mm16(pacc[6][2], cat8(pl[2], ph[2]), px[2]);
        mm16(pacc[6][3], cat8(pl[2], ph[2]), px[3]);
        SBAR();
        *(LAS u32x4*)(qaw + kvo + 4096) = pack2(pacc[4][0] * QSCALE, pacc[5][0] * QSCALE);
        SBAR();
        mm16(pacc[7][2], cat8(pl[3], ph[3]), px[2]);
        mm16(pacc[7][3], cat8(pl[3], ph[3]), px[3]);
        SBAR();
        *(LAS u32x4*)(qaw + kvo + 4352) = pack2(pacc[4][1] * QSCALE, pacc[5][1] * QSCALE);
        SBAR();
        asm volatile("s_nop 7" ::: "memory"); SBAR();
        *(LAS u32x4*)(qbw + 6144) = pack2(pacc[6][2] * QSCALE, pacc[7][2] * QSCALE);
        *(LAS u32x4*)(qbw + 6400) = pack2(pacc[6][3] * QSCALE, pacc[7][3] * QSCALE);
        *(LAS u32x4*)(qaw + kvo + 6144) = pack2(pacc[6][0] * QSCALE, pacc[7][0] * QSCALE);
        *(LAS u32x4*)(qaw + kvo + 6400) = pack2(pacc[6][1] * QSCALE, pacc[7][1] * QSCALE);
        wait_vm_bar<8>();
        ldsp kb0 = (ldsp)lds + opq(l16);
        ldsp qsrc = (ldsp)qaw + l16;
        qr[0] = lds128(qsrc);
        kf[0] = lds128(kb0 + 0);
        kf[1] = lds128(kb0 + 8192);
        kf[2] = lds128(kb0 + 1024);
        kf[3] = lds128(kb0 + 9216);
        qr[1] = lds128(qsrc + 1024);
        qr[2] = lds128(qsrc + 2048);
        qr[3] = lds128(qsrc + 3072);
        qr[4] = lds128(qsrc + 4096);
        qr[5] = lds128(qsrc + 5120);
        qr[6] = lds128(qsrc + 6144);
        qr[7] = lds128(qsrc + 7168);
        SBAR();
        SBAR();
        kf[4] = lds128(kb0 + 2048);
        sA0 = MFMA(kf[0], qr[0], f32x16{}); PIN(sA0);
        SBAR();
        kf[5] = lds128(kb0 + 10240);
        sA1 = MFMA(kf[1], qr[0], f32x16{}); PIN(sA1);
        SBAR();
        kf[6] = lds128(kb0 + 3072);
        sA0 = MFMA(kf[2], qr[1], sA0); PIN(sA0);
        SBAR();
        kf[7] = lds128(kb0 + 11264);
        sA1 = MFMA(kf[3], qr[1], sA1); PIN(sA1);
        SBAR();
        kf[8] = lds128(kb0 + 4096);
        sA0 = MFMA(kf[4], qr[2], sA0); PIN(sA0);
        SBAR();
        kf[9] = lds128(kb0 + 12288);
        sA1 = MFMA(kf[5], qr[2], sA1); PIN(sA1);
        SBAR();
        kf[10] = lds128(kb0 + 5120);
        sA0 = MFMA(kf[6], qr[3], sA0); PIN(sA0);
        SBAR();
        kf[11] = lds128(kb0 + 13312);
        sA1 = MFMA(kf[7], qr[3], sA1); PIN(sA1);
        SBAR();
        kf[12] = lds128(kb0 + 6144);
        sA0 = MFMA(kf[8], qr[4], sA0); PIN(sA0);
        SBAR();
        kf[13] = lds128(kb0 + 14336);
        sA1 = MFMA(kf[9], qr[4], sA1); PIN(sA1);
        SBAR();
        kf[14] = lds128(kb0 + 7168);
        sA0 = MFMA(kf[10], qr[5], sA0); PIN(sA0);
        SBAR();
        kf[15] = lds128(kb0 + 15360);
        sA1 = MFMA(kf[11], qr[5], sA1); PIN(sA1);
        SBAR();
        sA0 = MFMA(kf[12], qr[6], sA0); PIN(sA0);
        SBAR();
        sA1 = MFMA(kf[13], qr[6], sA1); PIN(sA1);
        SBAR();
        sA0 = MFMA(kf[14], qr[7], sA0); PIN(sA0);
        SBAR();
        sA1 = MFMA(kf[15], qr[7], sA1); PIN(sA1);
        SBAR();
        } else {
        KV_SETUP();
        const u32x4 wsr4 = {wsr[0], wsr[1], wid == 4 ? WS_END : 0u, wsr[3]};
        const unsigned vvo = (g4 & 1) * 512 + (g4 >> 1) * 256 + (lane & 15) * 16;
        ldsp lnrd = (ldsp)lds + LDS_QB + (lane & 7) * 1024 + (16 * (wid & 3) + (lane >> 3)) * 16;
        const unsigned lnvo = (8 * (wid & 3) + (lane >> 4)) * 1024 + ((lane >> 3) & 1) * 512 + (lane & 7) * 16, lnso = WS_XN + (b >> 2) * 32768 + (b & 3) * 128;
        wl[0][0] = ldstr(wr0[0] + 0); wh[0][0] = ldstr(wr0[0] + 0 + 256);
        xf[0] = lds128(xr[0] + 0);
        wl[0][1] = ldstr(wr1[0] + 0); wh[0][1] = ldstr(wr1[0] + 0 + 256);
        wl[0][2] = ldstr(wr0[0] + 512); wh[0][2] = ldstr(wr0[0] + 512 + 256);
        wl[0][3] = ldstr(wr1[0] + 512); wh[0][3] = ldstr(wr1[0] + 512 + 256);
        xf[1] = lds128(xr[0] + 1024);
        SBAR();
        xf[2] = lds128(xr[0] + 2048);
        mm16z(acc[0][0], xf[0], cat8(wl[0][0], wh[0][0]));
        mm16z(acc[0][1], xf[0], cat8(wl[0][1], wh[0][1]));
        st16(wsr, lnvo, lnso, *(const LAS u32x4*)lnrd);
        SBAR();
        mm16z(acc[0][2], xf[0], cat8(wl[0][2], wh[0][2]));
        mm16z(acc[0][3], xf[0], cat8(wl[0][3], wh[0][3]));
        st16(wsr, lnvo, lnso + 4096, *(const LAS u32x4*)(lnrd + 128));
        SBAR();
        xf[3] = lds128(xr[0] + 3072);
        mm16z(acc[1][0], xf[1], cat8(wl[0][0], wh[0][0]));
        mm16z(acc[1][1], xf[1], cat8(wl[0][1], wh[0][1]));
        warmb(lane * 128, wp0, w0st < 8 ? 8192u : 0u, lds0 + LDS_QB + 16384);
        SBAR();
        mm16z(acc[1][2], xf[1], cat8(wl[0][2], wh[0][2]));
        mm16z(acc[1][3], xf[1], cat8(wl[0][3], wh[0][3]));
        SBAR();
        xf[0] = lds128(xr[0] + 4096);
        mm16z(acc[2][0], xf[2], cat8(wl[0][0], wh[0][0]));
        mm16z(acc[2][1], xf[2], cat8(wl[0][1], wh[0][1]));
        SBAR();
        mm16z(acc[2][2], xf[2], cat8(wl[0][2], wh[0][2]));
        mm16z(acc[2][3], xf[2], cat8(wl[0][3], wh[0][3]));
        SBAR();
        xf[1] = lds128(xr[0] + 5120);
        mm16z(acc[3][0], xf[3], cat8(wl[0][0], wh[0][0]));
        mm16z(acc[3][1], xf[3], cat8(wl[0][1], wh[0][1]));
        SBAR();
        mm16z(acc[3][2], xf[3], cat8(wl[0][2], wh[0][2]));
        mm16z(acc[3][3], xf[3], cat8(wl[0][3], wh[0][3]));
        SBAR();
        xf[2] = lds128(xr[0] + 6144);
        mm16z(acc[4][0], xf[0], cat8(wl[0][0], wh[0][0]));
        mm16z(acc[4][1], xf[0], cat8(wl[0][1], wh[0][1]));
        SBAR();
        mm16z(acc[4][2], xf[0], cat8(wl[0][2], wh[0][2]));
        mm16z(acc[4][3], xf[0], cat8(wl[0][3], wh[0][3]));
        SBAR();
        xf[3] = lds128(xr[0] + 7168);
        mm16z(acc[5][0], xf[1], cat8(wl[0][0], wh[0][0]));
        mm16z(acc[5][1], xf[1], cat8(wl[0][1], wh[0][1]));
        SBAR();
        mm16z(acc[5][2], xf[1], cat8(wl[0][2], wh[0][2]));
        mm16z(acc[5][3], xf[1], cat8(wl[0][3], wh[0][3]));
        SBAR();
        mm16z(acc[6][0], xf[2], cat8(wl[0][0], wh[0][0]));
        mm16z(acc[6][1], xf[2], cat8(wl[0][1], wh[0][1]));
        SBAR();
        wait_vm_bar<63>();
        wl[1][0] = ldstr(wr0[0] + 32768); wh[1][0] = ldstr(wr0[0] + 32768 + 256); xf[0] = lds128(xr[0] + 32768); wl[1][1] = ldstr(wr1[0] + 32768); wh[1][1] = ldstr(wr1[0] + 32768 + 256);
        mm16z(acc[6][2], xf[2], cat8(wl[0][2], wh[0][2]));
        mm16z(acc[6][3], xf[2], cat8(wl[0][3], wh[0][3]));
        warmb(lane * 128, wp0, w0st == 8 ? 8192u : 0u, lds0 + LDS_QB + 16384);
        SBAR();
        wl[1][2] = ldstr(wr0[0] + 33280); wh[1][2] = ldstr(wr0[0] + 33280 + 256); wl[1][3] = ldstr(wr1[0] + 33280); wh[1][3] = ldstr(wr1[0] + 33280 + 256);
        mm16z(acc[7][0], xf[3], cat8(wl[0][0], wh[0][0]));
        mm16z(acc[7][1], xf[3], cat8(wl[0][1], wh[0][1]));
        SBAR();
        xf[1] = lds128(xr[0] + 33792);
        mm16z(acc[7][2], xf[3], cat8(wl[0][2], wh[0][2]));
        mm16z(acc[7][3], xf[3], cat8(wl[0][3], wh[0][3]));
        SBAR();
        xf[2] = lds128(xr[0] + 34816);
        mm16(acc[0][0], xf[0], cat8(wl[1][0], wh[1][0]));
        mm16(acc[0][1], xf[0], cat8(wl[1][1], wh[1][1]));
        SBAR();
        mm16(acc[0][2], xf[0], cat8(wl[1][2], wh[1][2]));
        mm16(acc[0][3], xf[0], cat8(wl[1][3], wh[1][3]));
        SBAR();
        xf[3] = lds128(xr[0] + 35840);
        mm16(acc[1][0], xf[1], cat8(wl[1][0], wh[1][0]));
        mm16(acc[1][1], xf[1], cat8(wl[1][1], wh[1][1]));
        SBAR();
        mm16(acc[1][2], xf[1], cat8(wl[1][2], wh[1][2]));
        mm16(acc[1][3], xf[1], cat8(wl[1][3], wh[1][3]));
        SBAR();
        xf[0] = lds128(xr[0] + 36864);
        mm16(acc[2][0], xf[2], cat8(wl[1][0], wh[1][0]));
        mm16(acc[2][1], xf[2], cat8(wl[1][1], wh[1][1]));
        SBAR();
        mm16(acc[2][2], xf[2], cat8(wl[1][2], wh[1][2]));
        mm16(acc[2][3], xf[2], cat8(wl[1][3], wh[1][3]));
        SBAR();
        xf[1] = lds128(xr[0] + 37888);
        mm16(acc[3][0], xf[3], cat8(wl[1][0], wh[1][0]));
        mm16(acc[3][1], xf[3], cat8(wl[1][1], wh[1][1]));
        SBAR();
        mm16(acc[3][2], xf[3], cat8(wl[1][2], wh[1][2]));
        mm16(acc[3][3], xf[3], cat8(wl[1][3], wh[1][3]));
        SBAR();
        xf[2] = lds128(xr[0] + 38912);
        mm16(acc[4][0], xf[0], cat8(wl[1][0], wh[1][0]));
        mm16(acc[4][1], xf[0], cat8(wl[1][1], wh[1][1]));
        SBAR();
        mm16(acc[4][2], xf[0], cat8(wl[1][2], wh[1][2]));
        mm16(acc[4][3], xf[0], cat8(wl[1][3], wh[1][3]));
        SBAR();
        xf[3] = lds128(xr[0] + 39936);
        mm16(acc[5][0], xf[1], cat8(wl[1][0], wh[1][0]));
        mm16(acc[5][1], xf[1], cat8(wl[1][1], wh[1][1]));
        SBAR();
        mm16(acc[5][2], xf[1], cat8(wl[1][2], wh[1][2]));
        mm16(acc[5][3], xf[1], cat8(wl[1][3], wh[1][3]));
        SBAR();
        mm16(acc[6][0], xf[2], cat8(wl[1][0], wh[1][0]));
        mm16(acc[6][1], xf[2], cat8(wl[1][1], wh[1][1]));
        SBAR();
        wait_vm_bar<63>();
        wl[0][0] = ldstr(wr0[1] + 0); wh[0][0] = ldstr(wr0[1] + 0 + 256); xf[0] = lds128(xr[1] + 0); wl[0][1] = ldstr(wr1[1] + 0); wh[0][1] = ldstr(wr1[1] + 0 + 256);
        mm16(acc[6][2], xf[2], cat8(wl[1][2], wh[1][2]));
        mm16(acc[6][3], xf[2], cat8(wl[1][3], wh[1][3]));
        warmb(lane * 128, wp0, w0st == 9 ? 8192u : 0u, lds0 + LDS_QB + 16384);
        SBAR();
        wl[0][2] = ldstr(wr0[1] + 512); wh[0][2] = ldstr(wr0[1] + 512 + 256); wl[0][3] = ldstr(wr1[1] + 512); wh[0][3] = ldstr(wr1[1] + 512 + 256);
        mm16(acc[7][0], xf[3], cat8(wl[1][0], wh[1][0]));
        mm16(acc[7][1], xf[3], cat8(wl[1][1], wh[1][1]));
        SBAR();
        xf[1] = lds128(xr[1] + 1024);
        mm16(acc[7][2], xf[3], cat8(wl[1][2], wh[1][2]));
        mm16(acc[7][3], xf[3], cat8(wl[1][3], wh[1][3]));
        SBAR();
        xf[2] = lds128(xr[1] + 2048);
        mm16(acc[0][0], xf[0], cat8(wl[0][0], wh[0][0]));
        mm16(acc[0][1], xf[0], cat8(wl[0][1], wh[0][1]));
        SBAR();
        mm16(acc[0][2], xf[0], cat8(wl[0][2], wh[0][2]));
        mm16(acc[0][3], xf[0], cat8(wl[0][3], wh[0][3]));
        SBAR();
        xf[3] = lds128(xr[1] + 3072);
        mm16(acc[1][0], xf[1], cat8(wl[0][0], wh[0][0]));
        mm16(acc[1][1], xf[1], cat8(wl[0][1], wh[0][1]));
        SBAR();
        mm16(acc[1][2], xf[1], cat8(wl[0][2], wh[0][2]));
        mm16(acc[1][3], xf[1], cat8(wl[0][3], wh[0][3]));
        SBAR();
        xf[0] = lds128(xr[1] + 4096);
        mm16(acc[2][0], xf[2], cat8(wl[0][0], wh[0][0]));
        mm16(acc[2][1], xf[2], cat8(wl[0][1], wh[0][1]));
        SBAR();
        mm16(acc[2][2], xf[2], cat8(wl[0][2], wh[0][2]));
        mm16(acc[2][3], xf[2], cat8(wl[0][3], wh[0][3]));
        SBAR();
        xf[1] = lds128(xr[1] + 5120);
        mm16(acc[3][0], xf[3], cat8(wl[0][0], wh[0][0]));
        mm16(acc[3][1], xf[3], cat8(wl[0][1], wh[0][1]));
        SBAR();
        mm16(acc[3][2], xf[3], cat8(wl[0][2], wh[0][2]));
        mm16(acc[3][3], xf[3], cat8(wl[0][3], wh[0][3]));
        SBAR();
        xf[2] = lds128(xr[1] + 6144);
        mm16(acc[4][0], xf[0], cat8(wl[0][0], wh[0][0]));
        mm16(acc[4][1], xf[0], cat8(wl[0][1], wh[0][1]));
        SBAR();
        mm16(acc[4][2], xf[0], cat8(wl[0][2], wh[0][2]));
        mm16(acc[4][3], xf[0], cat8(wl[0][3], wh[0][3]));
        SBAR();
        xf[3] = lds128(xr[1] + 7168);
        mm16(acc[5][0], xf[1], cat8(wl[0][0], wh[0][0]));
        mm16(acc[5][1], xf[1], cat8(wl[0][1], wh[0][1]));
        SBAR();
        mm16(acc[5][2], xf[1], cat8(wl[0][2], wh[0][2]));
        mm16(acc[5][3], xf[1], cat8(wl[0][3], wh[0][3]));
        SBAR();
        mm16(acc[6][0], xf[2], cat8(wl[0][0], wh[0][0]));
        mm16(acc[6][1], xf[2], cat8(wl[0][1], wh[0][1]));
        SBAR();
        wait_vm_bar<63>();
        wl[1][0] = ldstr(wr0[1] + 32768); wh[1][0] = ldstr(wr0[1] + 32768 + 256); xf[0] = lds128(xr[1] + 32768); wl[1][1] = ldstr(wr1[1] + 32768); wh[1][1] = ldstr(wr1[1] + 32768 + 256);
        mm16(acc[6][2], xf[2], cat8(wl[0][2], wh[0][2]));
        mm16(acc[6][3], xf[2], cat8(wl[0][3], wh[0][3]));
        warmb(lane * 128, wp0, w0st == 10 ? 8192u : 0u, lds0 + LDS_QB + 16384);
        SBAR();
        wl[1][2] = ldstr(wr0[1] + 33280); wh[1][2] = ldstr(wr0[1] + 33280 + 256); wl[1][3] = ldstr(wr1[1] + 33280); wh[1][3] = ldstr(wr1[1] + 33280 + 256);
        mm16(acc[7][0], xf[3], cat8(wl[0][0], wh[0][0]));
        mm16(acc[7][1], xf[3], cat8(wl[0][1], wh[0][1]));
        SBAR();
        xf[1] = lds128(xr[1] + 33792);
        mm16(acc[7][2], xf[3], cat8(wl[0][2], wh[0][2]));
        mm16(acc[7][3], xf[3], cat8(wl[0][3], wh[0][3]));
        SBAR();
        xf[2] = lds128(xr[1] + 34816);
        mm16(acc[0][0], xf[0], cat8(wl[1][0], wh[1][0]));
        mm16(acc[0][1], xf[0], cat8(wl[1][1], wh[1][1]));
        SBAR();
        mm16(acc[0][2], xf[0], cat8(wl[1][2], wh[1][2]));
        mm16(acc[0][3], xf[0], cat8(wl[1][3], wh[1][3]));
        SBAR();
        xf[3] = lds128(xr[1] + 35840);
        mm16(acc[1][0], xf[1], cat8(wl[1][0], wh[1][0]));
        mm16(acc[1][1], xf[1], cat8(wl[1][1], wh[1][1]));
        SBAR();
        mm16(acc[1][2], xf[1], cat8(wl[1][2], wh[1][2]));
        mm16(acc[1][3], xf[1], cat8(wl[1][3], wh[1][3]));
        SBAR();
        xf[0] = lds128(xr[1] + 36864);
        mm16(acc[2][0], xf[2], cat8(wl[1][0], wh[1][0]));
        mm16(acc[2][1], xf[2], cat8(wl[1][1], wh[1][1]));
        SBAR();
        mm16(acc[2][2], xf[2], cat8(wl[1][2], wh[1][2]));
        mm16(acc[2][3], xf[2], cat8(wl[1][3], wh[1][3]));
        SBAR();
        xf[1] = lds128(xr[1] + 37888);
        mm16(acc[3][0], xf[3], cat8(wl[1][0], wh[1][0]));
        mm16(acc[3][1], xf[3], cat8(wl[1][1], wh[1][1]));
        SBAR();
        mm16(acc[3][2], xf[3], cat8(wl[1][2], wh[1][2]));
        mm16(acc[3][3], xf[3], cat8(wl[1][3], wh[1][3]));
        SBAR();
        xf[2] = lds128(xr[1] + 38912);
        mm16(acc[4][0], xf[0], cat8(wl[1][0], wh[1][0]));
        mm16(acc[4][1], xf[0], cat8(wl[1][1], wh[1][1]));
        SBAR();
        mm16(acc[4][2], xf[0], cat8(wl[1][2], wh[1][2]));
        mm16(acc[4][3], xf[0], cat8(wl[1][3], wh[1][3]));
        SBAR();
        xf[3] = lds128(xr[1] + 39936);
        mm16(acc[5][0], xf[1], cat8(wl[1][0], wh[1][0]));
        mm16(acc[5][1], xf[1], cat8(wl[1][1], wh[1][1]));
        SBAR();
        mm16(acc[5][2], xf[1], cat8(wl[1][2], wh[1][2]));
        mm16(acc[5][3], xf[1], cat8(wl[1][3], wh[1][3]));
        SBAR();
        mm16(acc[6][0], xf[2], cat8(wl[1][0], wh[1][0]));
        mm16(acc[6][1], xf[2], cat8(wl[1][1], wh[1][1]));
        SBAR();
        wait_vm_bar<4>();
        wl[0][0] = ldstr(wr0[0] + 0); wh[0][0] = ldstr(wr0[0] + 0 + 256); xf[0] = lds128(xr[0] + 0); wl[0][1] = ldstr(wr1[0] + 0); wh[0][1] = ldstr(wr1[0] + 0 + 256);
        mm16(acc[6][2], xf[2], cat8(wl[1][2], wh[1][2]));
        mm16(acc[6][3], xf[2], cat8(wl[1][3], wh[1][3]));
        warmb(lane * 128, wp0, w0st == 11 ? 8192u : 0u, lds0 + LDS_QB + 16384);
        SBAR();
        wl[0][2] = ldstr(wr0[0] + 512); wh[0][2] = ldstr(wr0[0] + 512 + 256); wl[0][3] = ldstr(wr1[0] + 512); wh[0][3] = ldstr(wr1[0] + 512 + 256);
        mm16(acc[7][0], xf[3], cat8(wl[1][0], wh[1][0]));
        mm16(acc[7][1], xf[3], cat8(wl[1][1], wh[1][1]));
        st_flag(wsr4, WS_FLAG + F_LN * 32768 + b * 128, tag);
        SBAR();
        xf[1] = lds128(xr[0] + 1024);
        mm16(acc[7][2], xf[3], cat8(wl[1][2], wh[1][2]));
        mm16(acc[7][3], xf[3], cat8(wl[1][3], wh[1][3]));
        SBAR();
        xf[2] = lds128(xr[0] + 2048);
        mm16(acc[0][0], xf[0], cat8(wl[0][0], wh[0][0]));
        mm16(acc[0][1], xf[0], cat8(wl[0][1], wh[0][1]));
        SBAR();
        mm16(acc[0][2], xf[0], cat8(wl[0][2], wh[0][2]));
        mm16(acc[0][3], xf[0], cat8(wl[0][3], wh[0][3]));
        SBAR();
        xf[3] = lds128(xr[0] + 3072);
        mm16(acc[1][0], xf[1], cat8(wl[0][0], wh[0][0]));
        mm16(acc[1][1], xf[1], cat8(wl[0][1], wh[0][1]));
        SBAR();
        mm16(acc[1][2], xf[1], cat8(wl[0][2], wh[0][2]));
        mm16(acc[1][3], xf[1], cat8(wl[0][3], wh[0][3]));
        SBAR();
        xf[0] = lds128(xr[0] + 4096);
        mm16(acc[2][0], xf[2], cat8(wl[0][0], wh[0][0]));
        mm16(acc[2][1], xf[2], cat8(wl[0][1], wh[0][1]));
        SBAR();
        mm16(acc[2][2], xf[2], cat8(wl[0][2], wh[0][2]));
        mm16(acc[2][3], xf[2], cat8(wl[0][3], wh[0][3]));
        SBAR();
        xf[1] = lds128(xr[0] + 5120);
        mm16(acc[3][0], xf[3], cat8(wl[0][0], wh[0][0]));
        mm16(acc[3][1], xf[3], cat8(wl[0][1], wh[0][1]));
        SBAR();
        mm16(acc[3][2], xf[3], cat8(wl[0][2], wh[0][2]));
        mm16(acc[3][3], xf[3], cat8(wl[0][3], wh[0][3]));
        SBAR();
        xf[2] = lds128(xr[0] + 6144);
        mm16(acc[4][0], xf[0], cat8(wl[0][0], wh[0][0]));
        mm16(acc[4][1], xf[0], cat8(wl[0][1], wh[0][1]));
        SBAR();
        mm16(acc[4][2], xf[0], cat8(wl[0][2], wh[0][2]));
        mm16(acc[4][3], xf[0], cat8(wl[0][3], wh[0][3]));
        SBAR();
        xf[3] = lds128(xr[0] + 7168);
        mm16(acc[5][0], xf[1], cat8(wl[0][0], wh[0][0]));
        mm16(acc[5][1], xf[1], cat8(wl[0][1], wh[0][1]));
        SBAR();
        mm16(acc[5][2], xf[1], cat8(wl[0][2], wh[0][2]));
        mm16(acc[5][3], xf[1], cat8(wl[0][3], wh[0][3]));
        SBAR();
        mm16(acc[6][0], xf[2], cat8(wl[0][0], wh[0][0]));
        mm16(acc[6][1], xf[2], cat8(wl[0][1], wh[0][1]));
        SBAR();
        wait_vm_bar<63>();
        wl[1][0] = ldstr(wr0[0] + 32768); wh[1][0] = ldstr(wr0[0] + 32768 + 256); xf[0] = lds128(xr[0] + 32768); wl[1][1] = ldstr(wr1[0] + 32768); wh[1][1] = ldstr(wr1[0] + 32768 + 256);
        mm16(acc[6][2], xf[2], cat8(wl[0][2], wh[0][2]));
        mm16(acc[6][3], xf[2], cat8(wl[0][3], wh[0][3]));
        warmb(lane * 128, wp1, j == 0 ? 8192u : 0u, lds0 + LDS_QB + 16384);
        SBAR();
        wl[1][2] = ldstr(wr0[0] + 33280); wh[1][2] = ldstr(wr0[0] + 33280 + 256); wl[1][3] = ldstr(wr1[0] + 33280); wh[1][3] = ldstr(wr1[0] + 33280 + 256);
        mm16(acc[7][0], xf[3], cat8(wl[0][0], wh[0][0]));
        mm16(acc[7][1], xf[3], cat8(wl[0][1], wh[0][1]));
        warmb(lane * 128, wp0, w0st == 12 ? 8192u : 0u, lds0 + LDS_QB + 16384);
        SBAR();
        xf[1] = lds128(xr[0] + 33792);
        mm16(acc[7][2], xf[3], cat8(wl[0][2], wh[0][2]));
        mm16(acc[7][3], xf[3], cat8(wl[0][3], wh[0][3]));
        SBAR();
        xf[2] = lds128(xr[0] + 34816);
        mm16(acc[0][0], xf[0], cat8(wl[1][0], wh[1][0]));
        mm16(acc[0][1], xf[0], cat8(wl[1][1], wh[1][1]));
        SBAR();
        mm16(acc[0][2], xf[0], cat8(wl[1][2], wh[1][2]));
        mm16(acc[0][3], xf[0], cat8(wl[1][3], wh[1][3]));
        SBAR();
        xf[3] = lds128(xr[0] + 35840);
        mm16(acc[1][0], xf[1], cat8(wl[1][0], wh[1][0]));
        mm16(acc[1][1], xf[1], cat8(wl[1][1], wh[1][1]));
        SBAR();
        mm16(acc[1][2], xf[1], cat8(wl[1][2], wh[1][2]));
        mm16(acc[1][3], xf[1], cat8(wl[1][3], wh[1][3]));
        SBAR();
        xf[0] = lds128(xr[0] + 36864);
        mm16(acc[2][0], xf[2], cat8(wl[1][0], wh[1][0]));
        mm16(acc[2][1], xf[2], cat8(wl[1][1], wh[1][1]));
        SBAR();
        mm16(acc[2][2], xf[2], cat8(wl[1][2], wh[1][2]));
        mm16(acc[2][3], xf[2], cat8(wl[1][3], wh[1][3]));
        SBAR();
        xf[1] = lds128(xr[0] + 37888);
        mm16(acc[3][0], xf[3], cat8(wl[1][0], wh[1][0]));
        mm16(acc[3][1], xf[3], cat8(wl[1][1], wh[1][1]));
        SBAR();
        mm16(acc[3][2], xf[3], cat8(wl[1][2], wh[1][2]));
        mm16(acc[3][3], xf[3], cat8(wl[1][3], wh[1][3]));
        SBAR();
        xf[2] = lds128(xr[0] + 38912);
        mm16(acc[4][0], xf[0], cat8(wl[1][0], wh[1][0]));
        mm16(acc[4][1], xf[0], cat8(wl[1][1], wh[1][1]));
        SBAR();
        mm16(acc[4][2], xf[0], cat8(wl[1][2], wh[1][2]));
        mm16(acc[4][3], xf[0], cat8(wl[1][3], wh[1][3]));
        SBAR();
        xf[3] = lds128(xr[0] + 39936);
        mm16(acc[5][0], xf[1], cat8(wl[1][0], wh[1][0]));
        mm16(acc[5][1], xf[1], cat8(wl[1][1], wh[1][1]));
        SBAR();
        mm16(acc[5][2], xf[1], cat8(wl[1][2], wh[1][2]));
        mm16(acc[5][3], xf[1], cat8(wl[1][3], wh[1][3]));
        SBAR();
        mm16(acc[6][0], xf[2], cat8(wl[1][0], wh[1][0]));
        mm16(acc[6][1], xf[2], cat8(wl[1][1], wh[1][1]));
        SBAR();
        wait_vm_bar<63>();
        wl[0][0] = ldstr(wr0[1] + 0); wh[0][0] = ldstr(wr0[1] + 0 + 256); xf[0] = lds128(xr[1] + 0); wl[0][1] = ldstr(wr1[1] + 0); wh[0][1] = ldstr(wr1[1] + 0 + 256);
        mm16(acc[6][2], xf[2], cat8(wl[1][2], wh[1][2]));
        mm16(acc[6][3], xf[2], cat8(wl[1][3], wh[1][3]));
        warmb(lane * 128, wp1, j == 4 ? 8192u : 0u, lds0 + LDS_QB + 16384);
        SBAR();
        wl[0][2] = ldstr(wr0[1] + 512); wh[0][2] = ldstr(wr0[1] + 512 + 256); wl[0][3] = ldstr(wr1[1] + 512); wh[0][3] = ldstr(wr1[1] + 512 + 256);
        mm16(acc[7][0], xf[3], cat8(wl[1][0], wh[1][0]));
        mm16(acc[7][1], xf[3], cat8(wl[1][1], wh[1][1]));
        warmb(lane * 128, wp0, w0st == 13 ? 8192u : 0u, lds0 + LDS_QB + 16384);
        SBAR();
        xf[1] = lds128(xr[1] + 1024);
        mm16(acc[7][2], xf[3], cat8(wl[1][2], wh[1][2]));
        mm16(acc[7][3], xf[3], cat8(wl[1][3], wh[1][3]));
        SBAR();
        xf[2] = lds128(xr[1] + 2048);
        mm16(acc[0][0], xf[0], cat8(wl[0][0], wh[0][0]));
        mm16(acc[0][1], xf[0], cat8(wl[0][1], wh[0][1]));
        SBAR();
        mm16(acc[0][2], xf[0], cat8(wl[0][2], wh[0][2]));
        mm16(acc[0][3], xf[0], cat8(wl[0][3], wh[0][3]));
        SBAR();
        xf[3] = lds128(xr[1] + 3072);
        mm16(acc[1][0], xf[1], cat8(wl[0][0], wh[0][0]));
        mm16(acc[1][1], xf[1], cat8(wl[0][1], wh[0][1]));
        SBAR();
        mm16(acc[1][2], xf[1], cat8(wl[0][2], wh[0][2]));
        mm16(acc[1][3], xf[1], cat8(wl[0][3], wh[0][3]));
        SBAR();
        xf[0] = lds128(xr[1] + 4096);
        mm16(acc[2][0], xf[2], cat8(wl[0][0], wh[0][0]));
        mm16(acc[2][1], xf[2], cat8(wl[0][1], wh[0][1]));
        SBAR();
        mm16(acc[2][2], xf[2], cat8(wl[0][2], wh[0][2]));
        mm16(acc[2][3], xf[2], cat8(wl[0][3], wh[0][3]));
        SBAR();
        xf[1] = lds128(xr[1] + 5120);
        mm16(acc[3][0], xf[3], cat8(wl[0][0], wh[0][0]));
        mm16(acc[3][1], xf[3], cat8(wl[0][1], wh[0][1]));
        SBAR();
        mm16(acc[3][2], xf[3], cat8(wl[0][2], wh[0][2]));
        mm16(acc[3][3], xf[3], cat8(wl[0][3], wh[0][3]));
        SBAR();
        xf[2] = lds128(xr[1] + 6144);
        mm16(acc[4][0], xf[0], cat8(wl[0][0], wh[0][0]));
        mm16(acc[4][1], xf[0], cat8(wl[0][1], wh[0][1]));
        SBAR();
        mm16(acc[4][2], xf[0], cat8(wl[0][2], wh[0][2]));
        mm16(acc[4][3], xf[0], cat8(wl[0][3], wh[0][3]));
        SBAR();
        xf[3] = lds128(xr[1] + 7168);
        mm16(acc[5][0], xf[1], cat8(wl[0][0], wh[0][0]));
        mm16(acc[5][1], xf[1], cat8(wl[0][1], wh[0][1]));
        SBAR();
        mm16(acc[5][2], xf[1], cat8(wl[0][2], wh[0][2]));
        mm16(acc[5][3], xf[1], cat8(wl[0][3], wh[0][3]));
        SBAR();
        mm16(acc[6][0], xf[2], cat8(wl[0][0], wh[0][0]));
        mm16(acc[6][1], xf[2], cat8(wl[0][1], wh[0][1]));
        SBAR();
        wait_vm_bar<63>();
        wl[1][0] = ldstr(wr0[1] + 32768); wh[1][0] = ldstr(wr0[1] + 32768 + 256); xf[0] = lds128(xr[1] + 32768); wl[1][1] = ldstr(wr1[1] + 32768); wh[1][1] = ldstr(wr1[1] + 32768 + 256);
        mm16(acc[6][2], xf[2], cat8(wl[0][2], wh[0][2]));
        mm16(acc[6][3], xf[2], cat8(wl[0][3], wh[0][3]));
        warmb(lane * 128, wp1, j == 6 ? 8192u : 0u, lds0 + LDS_QB + 16384);
        SBAR();
        wl[1][2] = ldstr(wr0[1] + 33280); wh[1][2] = ldstr(wr0[1] + 33280 + 256); wl[1][3] = ldstr(wr1[1] + 33280); wh[1][3] = ldstr(wr1[1] + 33280 + 256);
        mm16(acc[7][0], xf[3], cat8(wl[0][0], wh[0][0]));
        mm16(acc[7][1], xf[3], cat8(wl[0][1], wh[0][1]));
        warmb(lane * 128, wp0, w0st == 14 ? 8192u : 0u, lds0 + LDS_QB + 16384);
        SBAR();
        xf[1] = lds128(xr[1] + 33792);
        mm16(acc[7][2], xf[3], cat8(wl[0][2], wh[0][2]));
        mm16(acc[7][3], xf[3], cat8(wl[0][3], wh[0][3]));
        SBAR();
        xf[2] = lds128(xr[1] + 34816);
        mm16(acc[0][0], xf[0], cat8(wl[1][0], wh[1][0]));
        mm16(acc[0][1], xf[0], cat8(wl[1][1], wh[1][1]));
        SBAR();
        mm16(acc[0][2], xf[0], cat8(wl[1][2], wh[1][2]));
        mm16(acc[0][3], xf[0], cat8(wl[1][3], wh[1][3]));
        SBAR();
        xf[3] = lds128(xr[1] + 35840);
        mm16(acc[1][0], xf[1], cat8(wl[1][0], wh[1][0]));
        mm16(acc[1][1], xf[1], cat8(wl[1][1], wh[1][1]));
        SBAR();
        mm16(acc[1][2], xf[1], cat8(wl[1][2], wh[1][2]));
        mm16(acc[1][3], xf[1], cat8(wl[1][3], wh[1][3]));
        SBAR();
        xf[0] = lds128(xr[1] + 36864);
        mm16(acc[2][0], xf[2], cat8(wl[1][0], wh[1][0]));
        mm16(acc[2][1], xf[2], cat8(wl[1][1], wh[1][1]));
        SBAR();
        mm16(acc[2][2], xf[2], cat8(wl[1][2], wh[1][2]));
        mm16(acc[2][3], xf[2], cat8(wl[1][3], wh[1][3]));
        SBAR();
        xf[1] = lds128(xr[1] + 37888);
        mm16(acc[3][0], xf[3], cat8(wl[1][0], wh[1][0]));
        mm16(acc[3][1], xf[3], cat8(wl[1][1], wh[1][1]));
        SBAR();
        mm16(acc[3][2], xf[3], cat8(wl[1][2], wh[1][2]));
        mm16(acc[3][3], xf[3], cat8(wl[1][3], wh[1][3]));
        SBAR();
        xf[2] = lds128(xr[1] + 38912);
        mm16(acc[4][0], xf[0], cat8(wl[1][0], wh[1][0]));
        mm16(acc[4][1], xf[0], cat8(wl[1][1], wh[1][1]));
        SBAR();
        mm16(acc[4][2], xf[0], cat8(wl[1][2], wh[1][2]));
        mm16(acc[4][3], xf[0], cat8(wl[1][3], wh[1][3]));
        SBAR();
        xf[3] = lds128(xr[1] + 39936);
        mm16(acc[5][0], xf[1], cat8(wl[1][0], wh[1][0]));
        mm16(acc[5][1], xf[1], cat8(wl[1][1], wh[1][1]));
        SBAR();
        mm16(acc[5][2], xf[1], cat8(wl[1][2], wh[1][2]));
        mm16(acc[5][3], xf[1], cat8(wl[1][3], wh[1][3]));
        SBAR();
        mm16(acc[6][0], xf[2], cat8(wl[1][0], wh[1][0]));
        mm16(acc[6][1], xf[2], cat8(wl[1][1], wh[1][1]));
        SBAR();
        wait_vm_bar<63>();
        wl[0][0] = ldstr(wr0[0] + 0); wh[0][0] = ldstr(wr0[0] + 0 + 256); xf[0] = lds128(xr[0] + 0); wl[0][1] = ldstr(wr1[0] + 0); wh[0][1] = ldstr(wr1[0] + 0 + 256);
        mm16(acc[6][2], xf[2], cat8(wl[1][2], wh[1][2]));
        mm16(acc[6][3], xf[2], cat8(wl[1][3], wh[1][3]));
        warmb(lane * 128, wp1, j == 1 ? 8192u : 0u, lds0 + LDS_QB + 16384);
        SBAR();
        wl[0][2] = ldstr(wr0[0] + 512); wh[0][2] = ldstr(wr0[0] + 512 + 256); wl[0][3] = ldstr(wr1[0] + 512); wh[0][3] = ldstr(wr1[0] + 512 + 256);
        mm16(acc[7][0], xf[3], cat8(wl[1][0], wh[1][0]));
        mm16(acc[7][1], xf[3], cat8(wl[1][1], wh[1][1]));
        warmb(lane * 128, wp0, w0st == 15 ? 8192u : 0u, lds0 + LDS_QB + 16384);
        SBAR();
        xf[1] = lds128(xr[0] + 1024);
        mm16(acc[7][2], xf[3], cat8(wl[1][2], wh[1][2]));
        mm16(acc[7][3], xf[3], cat8(wl[1][3], wh[1][3]));
        SBAR();
        xf[2] = lds128(xr[0] + 2048);
        mm16(acc[0][0], xf[0], cat8(wl[0][0], wh[0][0]));
        mm16(acc[0][1], xf[0], cat8(wl[0][1], wh[0][1]));
        SBAR();
        mm16(acc[0][2], xf[0], cat8(wl[0][2], wh[0][2]));
        mm16(acc[0][3], xf[0], cat8(wl[0][3], wh[0][3]));
        SBAR();
        xf[3] = lds128(xr[0] + 3072);
        mm16(acc[1][0], xf[1], cat8(wl[0][0], wh[0][0]));
        mm16(acc[1][1], xf[1], cat8(wl[0][1], wh[0][1]));
        SBAR();
        mm16(acc[1][2], xf[1], cat8(wl[0][2], wh[0][2]));
        mm16(acc[1][3], xf[1], cat8(wl[0][3], wh[0][3]));
        SBAR();
        xf[0] = lds128(xr[0] + 4096);
        mm16(acc[2][0], xf[2], cat8(wl[0][0], wh[0][0]));
        mm16(acc[2][1], xf[2], cat8(wl[0][1], wh[0][1]));
        SBAR();
        mm16(acc[2][2], xf[2], cat8(wl[0][2], wh[0][2]));
        mm16(acc[2][3], xf[2], cat8(wl[0][3], wh[0][3]));
        SBAR();
        xf[1] = lds128(xr[0] + 5120);
        mm16(acc[3][0], xf[3], cat8(wl[0][0], wh[0][0]));
        mm16(acc[3][1], xf[3], cat8(wl[0][1], wh[0][1]));
        SBAR();
        mm16(acc[3][2], xf[3], cat8(wl[0][2], wh[0][2]));
        mm16(acc[3][3], xf[3], cat8(wl[0][3], wh[0][3]));
        SBAR();
        xf[2] = lds128(xr[0] + 6144);
        mm16(acc[4][0], xf[0], cat8(wl[0][0], wh[0][0]));
        mm16(acc[4][1], xf[0], cat8(wl[0][1], wh[0][1]));
        SBAR();
        mm16(acc[4][2], xf[0], cat8(wl[0][2], wh[0][2]));
        mm16(acc[4][3], xf[0], cat8(wl[0][3], wh[0][3]));
        SBAR();
        xf[3] = lds128(xr[0] + 7168);
        mm16(acc[5][0], xf[1], cat8(wl[0][0], wh[0][0]));
        mm16(acc[5][1], xf[1], cat8(wl[0][1], wh[0][1]));
        SBAR();
        mm16(acc[5][2], xf[1], cat8(wl[0][2], wh[0][2]));
        mm16(acc[5][3], xf[1], cat8(wl[0][3], wh[0][3]));
        SBAR();
        mm16(acc[6][0], xf[2], cat8(wl[0][0], wh[0][0]));
        mm16(acc[6][1], xf[2], cat8(wl[0][1], wh[0][1]));
        SBAR();
        wait_vm_bar<63>();
        wl[1][0] = ldstr(wr0[0] + 32768); wh[1][0] = ldstr(wr0[0] + 32768 + 256); xf[0] = lds128(xr[0] + 32768); wl[1][1] = ldstr(wr1[0] + 32768); wh[1][1] = ldstr(wr1[0] + 32768 + 256);
        mm16(acc[6][2], xf[2], cat8(wl[0][2], wh[0][2]));
        mm16(acc[6][3], xf[2], cat8(wl[0][3], wh[0][3]));
        warmb(lane * 128, wp1, j == 5 ? 8192u : 0u, lds0 + LDS_QB + 16384);
        SBAR();
        wl[1][2] = ldstr(wr0[0] + 33280); wh[1][2] = ldstr(wr0[0] + 33280 + 256); wl[1][3] = ldstr(wr1[0] + 33280); wh[1][3] = ldstr(wr1[0] + 33280 + 256);
        mm16(acc[7][0], xf[3], cat8(wl[0][0], wh[0][0]));
        mm16(acc[7][1], xf[3], cat8(wl[0][1], wh[0][1]));
        SBAR();
        xf[1] = lds128(xr[0] + 33792);
        mm16(acc[7][2], xf[3], cat8(wl[0][2], wh[0][2]));
        mm16(acc[7][3], xf[3], cat8(wl[0][3], wh[0][3]));
        SBAR();
        xf[2] = lds128(xr[0] + 34816);
        mm16(acc[0][0], xf[0], cat8(wl[1][0], wh[1][0]));
        mm16(acc[0][1], xf[0], cat8(wl[1][1], wh[1][1]));
        SBAR();
        mm16(acc[0][2], xf[0], cat8(wl[1][2], wh[1][2]));
        mm16(acc[0][3], xf[0], cat8(wl[1][3], wh[1][3]));
        SBAR();
        xf[3] = lds128(xr[0] + 35840);
        mm16(acc[1][0], xf[1], cat8(wl[1][0], wh[1][0]));
        mm16(acc[1][1], xf[1], cat8(wl[1][1], wh[1][1]));
        SBAR();
        mm16(acc[1][2], xf[1], cat8(wl[1][2], wh[1][2]));
        mm16(acc[1][3], xf[1], cat8(wl[1][3], wh[1][3]));
        SBAR();
        xf[0] = lds128(xr[0] + 36864);
        mm16(acc[2][0], xf[2], cat8(wl[1][0], wh[1][0]));
        mm16(acc[2][1], xf[2], cat8(wl[1][1], wh[1][1]));
        SBAR();
        mm16(acc[2][2], xf[2], cat8(wl[1][2], wh[1][2]));
        mm16(acc[2][3], xf[2], cat8(wl[1][3], wh[1][3]));
        SBAR();
        xf[1] = lds128(xr[0] + 37888);
        mm16(acc[3][0], xf[3], cat8(wl[1][0], wh[1][0]));
        mm16(acc[3][1], xf[3], cat8(wl[1][1], wh[1][1]));
        SBAR();
        mm16(acc[3][2], xf[3], cat8(wl[1][2], wh[1][2]));
        mm16(acc[3][3], xf[3], cat8(wl[1][3], wh[1][3]));
        SBAR();
        xf[2] = lds128(xr[0] + 38912);
        mm16(acc[4][0], xf[0], cat8(wl[1][0], wh[1][0]));
        mm16(acc[4][1], xf[0], cat8(wl[1][1], wh[1][1]));
        SBAR();
        mm16(acc[4][2], xf[0], cat8(wl[1][2], wh[1][2]));
        mm16(acc[4][3], xf[0], cat8(wl[1][3], wh[1][3]));
        SBAR();
        xf[3] = lds128(xr[0] + 39936);
        mm16(acc[5][0], xf[1], cat8(wl[1][0], wh[1][0]));
        mm16(acc[5][1], xf[1], cat8(wl[1][1], wh[1][1]));
        SBAR();
        mm16(acc[5][2], xf[1], cat8(wl[1][2], wh[1][2]));
        mm16(acc[5][3], xf[1], cat8(wl[1][3], wh[1][3]));
        SBAR();
        mm16(acc[6][0], xf[2], cat8(wl[1][0], wh[1][0]));
        mm16(acc[6][1], xf[2], cat8(wl[1][1], wh[1][1]));
        SBAR();
        wait_vm_bar<63>();
        wl[0][0] = ldstr(wr0[1] + 0); wh[0][0] = ldstr(wr0[1] + 0 + 256); xf[0] = lds128(xr[1] + 0); wl[0][1] = ldstr(wr1[1] + 0); wh[0][1] = ldstr(wr1[1] + 0 + 256);
        mm16(acc[6][2], xf[2], cat8(wl[1][2], wh[1][2]));
        mm16(acc[6][3], xf[2], cat8(wl[1][3], wh[1][3]));
        warmb(lane * 128, wp1, j == 7 ? 8192u : 0u, lds0 + LDS_QB + 16384);
        SBAR();
        wl[0][2] = ldstr(wr0[1] + 512); wh[0][2] = ldstr(wr0[1] + 512 + 256); wl[0][3] = ldstr(wr1[1] + 512); wh[0][3] = ldstr(wr1[1] + 512 + 256);
        mm16(acc[7][0], xf[3], cat8(wl[1][0], wh[1][0]));
        mm16(acc[7][1], xf[3], cat8(wl[1][1], wh[1][1]));
        SBAR();
        xf[1] = lds128(xr[1] + 1024);
        mm16(acc[7][2], xf[3], cat8(wl[1][2], wh[1][2]));
        mm16(acc[7][3], xf[3], cat8(wl[1][3], wh[1][3]));
        SBAR();
        xf[2] = lds128(xr[1] + 2048);
        mm16(acc[0][0], xf[0], cat8(wl[0][0], wh[0][0]));
        mm16(acc[0][1], xf[0], cat8(wl[0][1], wh[0][1]));
        SBAR();
        mm16(acc[0][2], xf[0], cat8(wl[0][2], wh[0][2]));
        mm16(acc[0][3], xf[0], cat8(wl[0][3], wh[0][3]));
        SBAR();
        xf[3] = lds128(xr[1] + 3072);
        mm16(acc[1][0], xf[1], cat8(wl[0][0], wh[0][0]));
        mm16(acc[1][1], xf[1], cat8(wl[0][1], wh[0][1]));
        SBAR();
        mm16(acc[1][2], xf[1], cat8(wl[0][2], wh[0][2]));
        mm16(acc[1][3], xf[1], cat8(wl[0][3], wh[0][3]));
        SBAR();
        xf[0] = lds128(xr[1] + 4096);
        mm16(acc[2][0], xf[2], cat8(wl[0][0], wh[0][0]));
        mm16(acc[2][1], xf[2], cat8(wl[0][1], wh[0][1]));
        SBAR();
        mm16(acc[2][2], xf[2], cat8(wl[0][2], wh[0][2]));
        mm16(acc[2][3], xf[2], cat8(wl[0][3], wh[0][3]));
        SBAR();
        xf[1] = lds128(xr[1] + 5120);
        mm16(acc[3][0], xf[3], cat8(wl[0][0], wh[0][0]));
        mm16(acc[3][1], xf[3], cat8(wl[0][1], wh[0][1]));
        SBAR();
        mm16(acc[3][2], xf[3], cat8(wl[0][2], wh[0][2]));
        mm16(acc[3][3], xf[3], cat8(wl[0][3], wh[0][3]));
        SBAR();
        xf[2] = lds128(xr[1] + 6144);
        mm16(acc[4][0], xf[0], cat8(wl[0][0], wh[0][0]));
        mm16(acc[4][1], xf[0], cat8(wl[0][1], wh[0][1]));
        SBAR();
        mm16(acc[4][2], xf[0], cat8(wl[0][2], wh[0][2]));
        mm16(acc[4][3], xf[0], cat8(wl[0][3], wh[0][3]));
        SBAR();
        xf[3] = lds128(xr[1] + 7168);
        mm16(acc[5][0], xf[1], cat8(wl[0][0], wh[0][0]));
        mm16(acc[5][1], xf[1], cat8(wl[0][1], wh[0][1]));
        SBAR();
        mm16(acc[5][2], xf[1], cat8(wl[0][2], wh[0][2]));
        mm16(acc[5][3], xf[1], cat8(wl[0][3], wh[0][3]));
        SBAR();
        mm16(acc[6][0], xf[2], cat8(wl[0][0], wh[0][0]));
        mm16(acc[6][1], xf[2], cat8(wl[0][1], wh[0][1]));
        SBAR();
        wait_vm_bar<63>();
        wl[1][0] = ldstr(wr0[1] + 32768); wh[1][0] = ldstr(wr0[1] + 32768 + 256); xf[0] = lds128(xr[1] + 32768); wl[1][1] = ldstr(wr1[1] + 32768); wh[1][1] = ldstr(wr1[1] + 32768 + 256);
        mm16(acc[6][2], xf[2], cat8(wl[0][2], wh[0][2]));
        mm16(acc[6][3], xf[2], cat8(wl[0][3], wh[0][3]));
        warmb(lane * 128, wp1, j == 2 ? 8192u : 0u, lds0 + LDS_QB + 16384);
        SBAR();
        wl[1][2] = ldstr(wr0[1] + 33280); wh[1][2] = ldstr(wr0[1] + 33280 + 256); wl[1][3] = ldstr(wr1[1] + 33280); wh[1][3] = ldstr(wr1[1] + 33280 + 256);
        mm16(acc[7][0], xf[3], cat8(wl[0][0], wh[0][0]));
        mm16(acc[7][1], xf[3], cat8(wl[0][1], wh[0][1]));
        SBAR();
        xf[1] = lds128(xr[1] + 33792);
        mm16(acc[7][2], xf[3], cat8(wl[0][2], wh[0][2]));
        mm16(acc[7][3], xf[3], cat8(wl[0][3], wh[0][3]));
        SBAR();
        xf[2] = lds128(xr[1] + 34816);
        mm16(acc[0][0], xf[0], cat8(wl[1][0], wh[1][0]));
        mm16(acc[0][1], xf[0], cat8(wl[1][1], wh[1][1]));
        SBAR();
        mm16(acc[0][2], xf[0], cat8(wl[1][2], wh[1][2]));
        mm16(acc[0][3], xf[0], cat8(wl[1][3], wh[1][3]));
        SBAR();
        xf[3] = lds128(xr[1] + 35840);
        mm16(acc[1][0], xf[1], cat8(wl[1][0], wh[1][0]));
        mm16(acc[1][1], xf[1], cat8(wl[1][1], wh[1][1]));
        SBAR();
        mm16(acc[1][2], xf[1], cat8(wl[1][2], wh[1][2]));
        mm16(acc[1][3], xf[1], cat8(wl[1][3], wh[1][3]));
        SBAR();
        xf[0] = lds128(xr[1] + 36864);
        mm16(acc[2][0], xf[2], cat8(wl[1][0], wh[1][0]));
        mm16(acc[2][1], xf[2], cat8(wl[1][1], wh[1][1]));
        SBAR();
        mm16(acc[2][2], xf[2], cat8(wl[1][2], wh[1][2]));
        mm16(acc[2][3], xf[2], cat8(wl[1][3], wh[1][3]));
        SBAR();
        xf[1] = lds128(xr[1] + 37888);
        mm16(acc[3][0], xf[3], cat8(wl[1][0], wh[1][0]));
        mm16(acc[3][1], xf[3], cat8(wl[1][1], wh[1][1]));
        SBAR();
        mm16(acc[3][2], xf[3], cat8(wl[1][2], wh[1][2]));
        mm16(acc[3][3], xf[3], cat8(wl[1][3], wh[1][3]));
        SBAR();
        xf[2] = lds128(xr[1] + 38912);
        mm16(acc[4][0], xf[0], cat8(wl[1][0], wh[1][0]));
        mm16(acc[4][1], xf[0], cat8(wl[1][1], wh[1][1]));
        SBAR();
        mm16(acc[4][2], xf[0], cat8(wl[1][2], wh[1][2]));
        mm16(acc[4][3], xf[0], cat8(wl[1][3], wh[1][3]));
        SBAR();
        xf[3] = lds128(xr[1] + 39936);
        mm16(acc[5][0], xf[1], cat8(wl[1][0], wh[1][0]));
        mm16(acc[5][1], xf[1], cat8(wl[1][1], wh[1][1]));
        SBAR();
        mm16(acc[5][2], xf[1], cat8(wl[1][2], wh[1][2]));
        mm16(acc[5][3], xf[1], cat8(wl[1][3], wh[1][3]));
        SBAR();
        mm16(acc[6][0], xf[2], cat8(wl[1][0], wh[1][0]));
        mm16(acc[6][1], xf[2], cat8(wl[1][1], wh[1][1]));
        SBAR();
        wait_vm_bar<63>();
        wl[0][0] = ldstr(wr0[0] + 0); wh[0][0] = ldstr(wr0[0] + 0 + 256); xf[0] = lds128(xr[0] + 0); wl[0][1] = ldstr(wr1[0] + 0); wh[0][1] = ldstr(wr1[0] + 0 + 256);
        mm16(acc[6][2], xf[2], cat8(wl[1][2], wh[1][2]));
        mm16(acc[6][3], xf[2], cat8(wl[1][3], wh[1][3]));
        warmb(lane * 128, wp1, j == 3 ? 8192u : 0u, lds0 + LDS_QB + 16384);
        SBAR();
        wl[0][2] = ldstr(wr0[0] + 512); wh[0][2] = ldstr(wr0[0] + 512 + 256); wl[0][3] = ldstr(wr1[0] + 512); wh[0][3] = ldstr(wr1[0] + 512 + 256);
        mm16(acc[7][0], xf[3], cat8(wl[1][0], wh[1][0]));
        mm16(acc[7][1], xf[3], cat8(wl[1][1], wh[1][1]));
        SBAR();
        xf[1] = lds128(xr[0] + 1024);
        mm16(acc[7][2], xf[3], cat8(wl[1][2], wh[1][2]));
        mm16(acc[7][3], xf[3], cat8(wl[1][3], wh[1][3]));
        SBAR();
        xf[2] = lds128(xr[0] + 2048);
        mm16(acc[0][0], xf[0], cat8(wl[0][0], wh[0][0]));
        mm16(acc[0][1], xf[0], cat8(wl[0][1], wh[0][1]));
        SBAR();
        mm16(acc[0][2], xf[0], cat8(wl[0][2], wh[0][2]));
        mm16(acc[0][3], xf[0], cat8(wl[0][3], wh[0][3]));
        SBAR();
        xf[3] = lds128(xr[0] + 3072);
        mm16(acc[1][0], xf[1], cat8(wl[0][0], wh[0][0]));
        mm16(acc[1][1], xf[1], cat8(wl[0][1], wh[0][1]));
        SBAR();
        mm16(acc[1][2], xf[1], cat8(wl[0][2], wh[0][2]));
        mm16(acc[1][3], xf[1], cat8(wl[0][3], wh[0][3]));
        SBAR();
        xf[0] = lds128(xr[0] + 4096);
        mm16(acc[2][0], xf[2], cat8(wl[0][0], wh[0][0]));
        mm16(acc[2][1], xf[2], cat8(wl[0][1], wh[0][1]));
        SBAR();
        mm16(acc[2][2], xf[2], cat8(wl[0][2], wh[0][2]));
        mm16(acc[2][3], xf[2], cat8(wl[0][3], wh[0][3]));
        SBAR();
        xf[1] = lds128(xr[0] + 5120);
        mm16(acc[3][0], xf[3], cat8(wl[0][0], wh[0][0]));
        mm16(acc[3][1], xf[3], cat8(wl[0][1], wh[0][1]));
        SBAR();
        mm16(acc[3][2], xf[3], cat8(wl[0][2], wh[0][2]));
        mm16(acc[3][3], xf[3], cat8(wl[0][3], wh[0][3]));
        SBAR();
        xf[2] = lds128(xr[0] + 6144);
        mm16(acc[4][0], xf[0], cat8(wl[0][0], wh[0][0]));
        mm16(acc[4][1], xf[0], cat8(wl[0][1], wh[0][1]));
        SBAR();
        mm16(acc[4][2], xf[0], cat8(wl[0][2], wh[0][2]));
        mm16(acc[4][3], xf[0], cat8(wl[0][3], wh[0][3]));
        SBAR();
        xf[3] = lds128(xr[0] + 7168);
        mm16(acc[5][0], xf[1], cat8(wl[0][0], wh[0][0]));
        mm16(acc[5][1], xf[1], cat8(wl[0][1], wh[0][1]));
        SBAR();
        mm16(acc[5][2], xf[1], cat8(wl[0][2], wh[0][2]));
        mm16(acc[5][3], xf[1], cat8(wl[0][3], wh[0][3]));
        SBAR();
        mm16(acc[6][0], xf[2], cat8(wl[0][0], wh[0][0]));
        mm16(acc[6][1], xf[2], cat8(wl[0][1], wh[0][1]));
        SBAR();
        wait_vm_bar<63>();
        wl[1][0] = ldstr(wr0[0] + 32768); wh[1][0] = ldstr(wr0[0] + 32768 + 256); xf[0] = lds128(xr[0] + 32768); wl[1][1] = ldstr(wr1[0] + 32768); wh[1][1] = ldstr(wr1[0] + 32768 + 256);
        mm16(acc[6][2], xf[2], cat8(wl[0][2], wh[0][2]));
        mm16(acc[6][3], xf[2], cat8(wl[0][3], wh[0][3]));
        SBAR();
        wl[1][2] = ldstr(wr0[0] + 33280); wh[1][2] = ldstr(wr0[0] + 33280 + 256); wl[1][3] = ldstr(wr1[0] + 33280); wh[1][3] = ldstr(wr1[0] + 33280 + 256);
        mm16(acc[7][0], xf[3], cat8(wl[0][0], wh[0][0]));
        mm16(acc[7][1], xf[3], cat8(wl[0][1], wh[0][1]));
        SBAR();
        xf[1] = lds128(xr[0] + 33792);
        mm16(acc[7][2], xf[3], cat8(wl[0][2], wh[0][2]));
        mm16(acc[7][3], xf[3], cat8(wl[0][3], wh[0][3]));
        SBAR();
        xf[2] = lds128(xr[0] + 34816);
        mm16(acc[0][0], xf[0], cat8(wl[1][0], wh[1][0]));
        mm16(acc[0][1], xf[0], cat8(wl[1][1], wh[1][1]));
        SBAR();
        mm16(acc[0][2], xf[0], cat8(wl[1][2], wh[1][2]));
        mm16(acc[0][3], xf[0], cat8(wl[1][3], wh[1][3]));
        SBAR();
        xf[3] = lds128(xr[0] + 35840);
        mm16(acc[1][0], xf[1], cat8(wl[1][0], wh[1][0]));
        mm16(acc[1][1], xf[1], cat8(wl[1][1], wh[1][1]));
        SBAR();
        mm16(acc[1][2], xf[1], cat8(wl[1][2], wh[1][2]));
        mm16(acc[1][3], xf[1], cat8(wl[1][3], wh[1][3]));
        SBAR();
        xf[0] = lds128(xr[0] + 36864);
        mm16(acc[2][0], xf[2], cat8(wl[1][0], wh[1][0]));
        mm16(acc[2][1], xf[2], cat8(wl[1][1], wh[1][1]));
        SBAR();
        mm16(acc[2][2], xf[2], cat8(wl[1][2], wh[1][2]));
        mm16(acc[2][3], xf[2], cat8(wl[1][3], wh[1][3]));
        SBAR();
        xf[1] = lds128(xr[0] + 37888);
        mm16(acc[3][0], xf[3], cat8(wl[1][0], wh[1][0]));
        mm16(acc[3][1], xf[3], cat8(wl[1][1], wh[1][1]));
        SBAR();
        mm16(acc[3][2], xf[3], cat8(wl[1][2], wh[1][2]));
        mm16(acc[3][3], xf[3], cat8(wl[1][3], wh[1][3]));
        SBAR();
        xf[2] = lds128(xr[0] + 38912);
        mm16(acc[4][0], xf[0], cat8(wl[1][0], wh[1][0]));
        mm16(acc[4][1], xf[0], cat8(wl[1][1], wh[1][1]));
        SBAR();
        mm16(acc[4][2], xf[0], cat8(wl[1][2], wh[1][2]));
        mm16(acc[4][3], xf[0], cat8(wl[1][3], wh[1][3]));
        SBAR();
        xf[3] = lds128(xr[0] + 39936);
        mm16(acc[5][0], xf[1], cat8(wl[1][0], wh[1][0]));
        mm16(acc[5][1], xf[1], cat8(wl[1][1], wh[1][1]));
        SBAR();
        mm16(acc[5][2], xf[1], cat8(wl[1][2], wh[1][2]));
        mm16(acc[5][3], xf[1], cat8(wl[1][3], wh[1][3]));
        SBAR();
        mm16(acc[6][0], xf[2], cat8(wl[1][0], wh[1][0]));
        mm16(acc[6][1], xf[2], cat8(wl[1][1], wh[1][1]));
        SBAR();
        wait_vm_bar<63>();
        wl[0][0] = ldstr(wr0[1] + 0); wh[0][0] = ldstr(wr0[1] + 0 + 256); xf[0] = lds128(xr[1] + 0); wl[0][1] = ldstr(wr1[1] + 0); wh[0][1] = ldstr(wr1[1] + 0 + 256);
        mm16(acc[6][2], xf[2], cat8(wl[1][2], wh[1][2]));
        mm16(acc[6][3], xf[2], cat8(wl[1][3], wh[1][3]));
        SBAR();
        wl[0][2] = ldstr(wr0[1] + 512); wh[0][2] = ldstr(wr0[1] + 512 + 256); wl[0][3] = ldstr(wr1[1] + 512); wh[0][3] = ldstr(wr1[1] + 512 + 256);
        mm16(acc[7][0], xf[3], cat8(wl[1][0], wh[1][0]));
        mm16(acc[7][1], xf[3], cat8(wl[1][1], wh[1][1]));
        SBAR();
        xf[1] = lds128(xr[1] + 1024);
        mm16(acc[7][2], xf[3], cat8(wl[1][2], wh[1][2]));
        mm16(acc[7][3], xf[3], cat8(wl[1][3], wh[1][3]));
        SBAR();
        xf[2] = lds128(xr[1] + 2048);
        mm16(acc[0][0], xf[0], cat8(wl[0][0], wh[0][0]));
        mm16(acc[0][1], xf[0], cat8(wl[0][1], wh[0][1]));
        SBAR();
        mm16(acc[0][2], xf[0], cat8(wl[0][2], wh[0][2]));
        mm16(acc[0][3], xf[0], cat8(wl[0][3], wh[0][3]));
        SBAR();
        xf[3] = lds128(xr[1] + 3072);
        mm16(acc[1][0], xf[1], cat8(wl[0][0], wh[0][0]));
        mm16(acc[1][1], xf[1], cat8(wl[0][1], wh[0][1]));
        SBAR();
        mm16(acc[1][2], xf[1], cat8(wl[0][2], wh[0][2]));
        mm16(acc[1][3], xf[1], cat8(wl[0][3], wh[0][3]));
        SBAR();
        xf[0] = lds128(xr[1] + 4096);
        mm16(acc[2][0], xf[2], cat8(wl[0][0], wh[0][0]));
        mm16(acc[2][1], xf[2], cat8(wl[0][1], wh[0][1]));
        SBAR();
        mm16(acc[2][2], xf[2], cat8(wl[0][2], wh[0][2]));
        mm16(acc[2][3], xf[2], cat8(wl[0][3], wh[0][3]));
        SBAR();
        xf[1] = lds128(xr[1] + 5120);
        mm16(acc[3][0], xf[3], cat8(wl[0][0], wh[0][0]));
        mm16(acc[3][1], xf[3], cat8(wl[0][1], wh[0][1]));
        SBAR();
        mm16(acc[3][2], xf[3], cat8(wl[0][2], wh[0][2]));
        mm16(acc[3][3], xf[3], cat8(wl[0][3], wh[0][3]));
        SBAR();
        xf[2] = lds128(xr[1] + 6144);
        mm16(acc[4][0], xf[0], cat8(wl[0][0], wh[0][0]));
        mm16(acc[4][1], xf[0], cat8(wl[0][1], wh[0][1]));
        SBAR();
        mm16(acc[4][2], xf[0], cat8(wl[0][2], wh[0][2]));
        mm16(acc[4][3], xf[0], cat8(wl[0][3], wh[0][3]));
        SBAR();
        xf[3] = lds128(xr[1] + 7168);
        mm16(acc[5][0], xf[1], cat8(wl[0][0], wh[0][0]));
        mm16(acc[5][1], xf[1], cat8(wl[0][1], wh[0][1]));
        SBAR();
        mm16(acc[5][2], xf[1], cat8(wl[0][2], wh[0][2]));
        mm16(acc[5][3], xf[1], cat8(wl[0][3], wh[0][3]));
        SBAR();
        mm16(acc[6][0], xf[2], cat8(wl[0][0], wh[0][0]));
        mm16(acc[6][1], xf[2], cat8(wl[0][1], wh[0][1]));
        SBAR();
        wait_vm_bar<63>();
        wl[1][0] = ldstr(wr0[1] + 32768); wh[1][0] = ldstr(wr0[1] + 32768 + 256); xf[0] = lds128(xr[1] + 32768); wl[1][1] = ldstr(wr1[1] + 32768); wh[1][1] = ldstr(wr1[1] + 32768 + 256);
        mm16(acc[6][2], xf[2], cat8(wl[0][2], wh[0][2]));
        mm16(acc[6][3], xf[2], cat8(wl[0][3], wh[0][3]));
        SBAR();
        wl[1][2] = ldstr(wr0[1] + 33280); wh[1][2] = ldstr(wr0[1] + 33280 + 256); wl[1][3] = ldstr(wr1[1] + 33280); wh[1][3] = ldstr(wr1[1] + 33280 + 256);
        mm16(acc[7][0], xf[3], cat8(wl[0][0], wh[0][0]));
        mm16(acc[7][1], xf[3], cat8(wl[0][1], wh[0][1]));
        SBAR();
        xf[1] = lds128(xr[1] + 33792);
        mm16(acc[7][2], xf[3], cat8(wl[0][2], wh[0][2]));
        mm16(acc[7][3], xf[3], cat8(wl[0][3], wh[0][3]));
        SBAR();
        xf[2] = lds128(xr[1] + 34816);
        mm16(acc[0][0], xf[0], cat8(wl[1][0], wh[1][0]));
        mm16(acc[0][1], xf[0], cat8(wl[1][1], wh[1][1]));
        SBAR();
        mm16(acc[0][2], xf[0], cat8(wl[1][2], wh[1][2]));
        mm16(acc[0][3], xf[0], cat8(wl[1][3], wh[1][3]));
        SBAR();
        xf[3] = lds128(xr[1] + 35840);
        mm16(acc[1][0], xf[1], cat8(wl[1][0], wh[1][0]));
        mm16(acc[1][1], xf[1], cat8(wl[1][1], wh[1][1]));
        SBAR();
        mm16(acc[1][2], xf[1], cat8(wl[1][2], wh[1][2]));
        mm16(acc[1][3], xf[1], cat8(wl[1][3], wh[1][3]));
        SBAR();
        held[0] = packT(acc[0][0], acc[0][1]); PIN(held[0]);
        SBAR();
        xf[0] = lds128(xr[1] + 36864);
        mm16(acc[2][0], xf[2], cat8(wl[1][0], wh[1][0]));
        mm16(acc[2][1], xf[2], cat8(wl[1][1], wh[1][1]));
        SBAR();
        held[1] = packT(acc[0][2], acc[0][3]); PIN(held[1]);
        SBAR();
        mm16(acc[2][2], xf[2], cat8(wl[1][2], wh[1][2]));
        mm16(acc[2][3], xf[2], cat8(wl[1][3], wh[1][3]));
        SBAR();
        held[2] = packT(acc[1][0], acc[1][1]); PIN(held[2]);
        SBAR();
        xf[1] = lds128(xr[1] + 37888);
        mm16(acc[3][0], xf[3], cat8(wl[1][0], wh[1][0]));
        mm16(acc[3][1], xf[3], cat8(wl[1][1], wh[1][1]));
        SBAR();
        held[3] = packT(acc[1][2], acc[1][3]); PIN(held[3]);
        SBAR();
        mm16(acc[3][2], xf[3], cat8(wl[1][2], wh[1][2]));
        mm16(acc[3][3], xf[3], cat8(wl[1][3], wh[1][3]));
        SBAR();
        held[4] = packT(acc[2][0], acc[2][1]); PIN(held[4]);
        SBAR();
        xf[2] = lds128(xr[1] + 38912);
        mm16(acc[4][0], xf[0], cat8(wl[1][0], wh[1][0]));
        mm16(acc[4][1], xf[0], cat8(wl[1][1], wh[1][1]));
        SBAR();
        held[5] = packT(acc[2][2], acc[2][3]); PIN(held[5]);
        SBAR();
        mm16(acc[4][2], xf[0], cat8(wl[1][2], wh[1][2]));
        mm16(acc[4][3], xf[0], cat8(wl[1][3], wh[1][3]));
        SBAR();
        held[6] = packT(acc[3][0], acc[3][1]); PIN(held[6]);
        SBAR();
        xf[3] = lds128(xr[1] + 39936);
        mm16(acc[5][0], xf[1], cat8(wl[1][0], wh[1][0]));
        mm16(acc[5][1], xf[1], cat8(wl[1][1], wh[1][1]));
        SBAR();
        held[7] = packT(acc[3][2], acc[3][3]); PIN(held[7]);
        SBAR();
        mm16(acc[5][2], xf[1], cat8(wl[1][2], wh[1][2]));
        mm16(acc[5][3], xf[1], cat8(wl[1][3], wh[1][3]));
        SBAR();
        held[8] = packT(acc[4][0], acc[4][1]); PIN(held[8]);
        SBAR();
        mm16(acc[6][0], xf[2], cat8(wl[1][0], wh[1][0]));
        mm16(acc[6][1], xf[2], cat8(wl[1][1], wh[1][1]));
        SBAR();
        held[9] = packT(acc[4][2], acc[4][3]); PIN(held[9]);
        SBAR();
        wait_vm_bar<0>();
        mm16(acc[6][2], xf[2], cat8(wl[1][2], wh[1][2]));
        mm16(acc[6][3], xf[2], cat8(wl[1][3], wh[1][3]));
        SBAR();
        held[10] = packT(acc[5][0], acc[5][1]); PIN(held[10]);
        SBAR();
        mm16(acc[7][0], xf[3], cat8(wl[1][0], wh[1][0]));
        mm16(acc[7][1], xf[3], cat8(wl[1][1], wh[1][1]));
        SBAR();
        held[11] = packT(acc[5][2], acc[5][3]); PIN(held[11]);
        SBAR();
        mm16(acc[7][2], xf[3], cat8(wl[1][2], wh[1][2]));
        mm16(acc[7][3], xf[3], cat8(wl[1][3], wh[1][3]));
        SBAR();
        held[12] = packT(acc[6][0], acc[6][1]); PIN(held[12]);
        SBAR();
        asm volatile("s_nop 7" ::: "memory"); SBAR();
        held[13] = packT(acc[6][2], acc[6][3]);
        held[14] = packT(acc[7][0], acc[7][1]);
        held[15] = packT(acc[7][2], acc[7][3]);
        const int gf = (q4 >> 1) | ((g4 & 1) << 1), gb = PJ_G + (8 * g4 + q4) * 128 + p4 * 8;
        PJ_SETUP(gb + gf * 32, gb + (gf ^ 1) * 32, gb + (gf ^ 2) * 32);
        if (j < 2) {
        asm volatile("" : "+s"(kst), "+s"(vst));
        pl[0] = ldstr(pw0[0] + 0); ph[0] = ldstr(pw0[0] + 0 + 512);
        px[0] = lds128(pxr[0] + 0);
        px[1] = lds128(pxr[0] + 256);
        pl[1] = ldstr(pw0[0] + 4096); ph[1] = ldstr(pw0[0] + 4096 + 512);
        px[2] = lds128(pxr[0] + 8192);
        px[3] = lds128(pxr[0] + 8448);
        SBAR();
        pl[2] = ldstr(pw1[0] + 0); ph[2] = ldstr(pw1[0] + 0 + 512);
        mm16z(pacc[0][0], cat8(pl[0], ph[0]), px[0]);
        mm16z(pacc[0][1], cat8(pl[0], ph[0]), px[1]);
        st16kv(wsr, vvo, vst + 0, held[0]);
        SBAR();
        mm16z(pacc[1][0], cat8(pl[1], ph[1]), px[0]);
        mm16z(pacc[1][1], cat8(pl[1], ph[1]), px[1]);
        SBAR();
        pl[3] = ldstr(pw1[0] + 4096); ph[3] = ldstr(pw1[0] + 4096 + 512);
        mm16z(pacc[0][2], cat8(pl[0], ph[0]), px[2]);
        mm16z(pacc[0][3], cat8(pl[0], ph[0]), px[3]);
        st16kv(wsr, vvo, vst + 1024, held[1]);
        SBAR();
        mm16z(pacc[1][2], cat8(pl[1], ph[1]), px[2]);
        mm16z(pacc[1][3], cat8(pl[1], ph[1]), px[3]);
        SBAR();
        pl[0] = ldstr(pw2[0] + 0); ph[0] = ldstr(pw2[0] + 0 + 512);
        mm16z(pacc[2][0], cat8(pl[2], ph[2]), px[0]);
        mm16z(pacc[2][1], cat8(pl[2], ph[2]), px[1]);
        st16kv(wsr, vvo, vst + 4096, held[2]);
        SBAR();
        mm16z(pacc[2][2], cat8(pl[2], ph[2]), px[2]);
        mm16z(pacc[2][3], cat8(pl[2], ph[2]), px[3]);
        SBAR();
        pl[1] = ldstr(pw2[0] + 4096); ph[1] = ldstr(pw2[0] + 4096 + 512);
        mm16z(pacc[3][0], cat8(pl[3], ph[3]), px[0]);
        mm16z(pacc[3][1], cat8(pl[3], ph[3]), px[1]);
        st16kv(wsr, vvo, vst + 5120, held[3]);
        SBAR();
        mm16z(pacc[3][2], cat8(pl[3], ph[3]), px[2]);
        mm16z(pacc[3][3], cat8(pl[3], ph[3]), px[3]);
        SBAR();
        mm16z(pacc[4][0], cat8(pl[0], ph[0]), px[0]);
        mm16z(pacc[4][1], cat8(pl[0], ph[0]), px[1]);
        SBAR();
        wait_vm_bar<63>();
        pl[2] = ldstr(pw0[0] + 32768); ph[2] = ldstr(pw0[0] + 32768 + 512);
        mm16z(pacc[5][0], cat8(pl[1], ph[1]), px[0]);
        mm16z(pacc[5][1], cat8(pl[1], ph[1]), px[1]);
        hb[0] = *(const LAS u32x4*)(hand + 0);
        hb[1] = *(const LAS u32x4*)(hand + 1024);
        SBAR();
        px[0] = lds128(pxr[0] + 32768); px[1] = lds128(pxr[0] + 33024); pl[3] = ldstr(pw0[0] + 36864); ph[3] = ldstr(pw0[0] + 36864 + 512);
        mm16z(pacc[4][2], cat8(pl[0], ph[0]), px[2]);
        mm16z(pacc[4][3], cat8(pl[0], ph[0]), px[3]);
        hb[2] = *(const LAS u32x4*)(hand + 2048);
        hb[3] = *(const LAS u32x4*)(hand + 3072);
        SBAR();
        mm16z(pacc[5][2], cat8(pl[1], ph[1]), px[2]);
        mm16z(pacc[5][3], cat8(pl[1], ph[1]), px[3]);
        SBAR();
        px[2] = lds128(pxr[0] + 40960); px[3] = lds128(pxr[0] + 41216);
        pl[0] = ldstr(pw1[0] + 32768); ph[0] = ldstr(pw1[0] + 32768 + 512);
        mm16(pacc[0][0], cat8(pl[2], ph[2]), px[0]);
        mm16(pacc[0][1], cat8(pl[2], ph[2]), px[1]);
        st16kv(wsr, kvo, kst + 0, hb[0]);
        SBAR();
        mm16(pacc[1][0], cat8(pl[3], ph[3]), px[0]);
        mm16(pacc[1][1], cat8(pl[3], ph[3]), px[1]);
        SBAR();
        pl[1] = ldstr(pw1[0] + 36864); ph[1] = ldstr(pw1[0] + 36864 + 512);
        mm16(pacc[0][2], cat8(pl[2], ph[2]), px[2]);
        mm16(pacc[0][3], cat8(pl[2], ph[2]), px[3]);
        st16kv(wsr, kvo, kst + 2048, hb[1]);
        SBAR();
        mm16(pacc[1][2], cat8(pl[3], ph[3]), px[2]);
        mm16(pacc[1][3], cat8(pl[3], ph[3]), px[3]);
        SBAR();
        pl[2] = ldstr(pw2[0] + 32768); ph[2] = ldstr(pw2[0] + 32768 + 512);
        mm16(pacc[2][0], cat8(pl[0], ph[0]), px[0]);
        mm16(pacc[2][1], cat8(pl[0], ph[0]), px[1]);
        st16kv(wsr, kvo, kst + 256, hb[2]);
        SBAR();
        mm16(pacc[2][2], cat8(pl[0], ph[0]), px[2]);
        mm16(pacc[2][3], cat8(pl[0], ph[0]), px[3]);
        SBAR();
        pl[3] = ldstr(pw2[0] + 36864); ph[3] = ldstr(pw2[0] + 36864 + 512);
        mm16(pacc[3][0], cat8(pl[1], ph[1]), px[0]);
        mm16(pacc[3][1], cat8(pl[1], ph[1]), px[1]);
        st16kv(wsr, kvo, kst + 2304, hb[3]);
        SBAR();
        mm16(pacc[3][2], cat8(pl[1], ph[1]), px[2]);
        mm16(pacc[3][3], cat8(pl[1], ph[1]), px[3]);
        SBAR();
        mm16(pacc[4][0], cat8(pl[2], ph[2]), px[0]);
        mm16(pacc[4][1], cat8(pl[2], ph[2]), px[1]);
        SBAR();
        wait_vm_bar<63>();
        pl[0] = ldstr(pw0[1] + 0); ph[0] = ldstr(pw0[1] + 0 + 512);
        mm16(pacc[5][0], cat8(pl[3], ph[3]), px[0]);
        mm16(pacc[5][1], cat8(pl[3], ph[3]), px[1]);
        hb[0] = *(const LAS u32x4*)(hand + 4096);
        hb[1] = *(const LAS u32x4*)(hand + 5120);
        SBAR();
        px[0] = lds128(pxr[1] + 0); px[1] = lds128(pxr[1] + 256); pl[1] = ldstr(pw0[1] + 4096); ph[1] = ldstr(pw0[1] + 4096 + 512);
        mm16(pacc[4][2], cat8(pl[2], ph[2]), px[2]);
        mm16(pacc[4][3], cat8(pl[2], ph[2]), px[3]);
        hb[2] = *(const LAS u32x4*)(hand + 6144);
        hb[3] = *(const LAS u32x4*)(hand + 7168);
        SBAR();
        mm16(pacc[5][2], cat8(pl[3], ph[3]), px[2]);
        mm16(pacc[5][3], cat8(pl[3], ph[3]), px[3]);
        SBAR();
        px[2] = lds128(pxr[1] + 8192); px[3] = lds128(pxr[1] + 8448);
        pl[2] = ldstr(pw1[1] + 0); ph[2] = ldstr(pw1[1] + 0 + 512);
        mm16(pacc[0][0], cat8(pl[0], ph[0]), px[0]);
        mm16(pacc[0][1], cat8(pl[0], ph[0]), px[1]);
        st16kv(wsr, kvo, kst + 8192, hb[0]);
        SBAR();
        mm16(pacc[1][0], cat8(pl[1], ph[1]), px[0]);
        mm16(pacc[1][1], cat8(pl[1], ph[1]), px[1]);
        SBAR();
        pl[3] = ldstr(pw1[1] + 4096); ph[3] = ldstr(pw1[1] + 4096 + 512);
        mm16(pacc[0][2], cat8(pl[0], ph[0]), px[2]);
        mm16(pacc[0][3], cat8(pl[0], ph[0]), px[3]);
        st16kv(wsr, kvo, kst + 10240, hb[1]);
        SBAR();
        mm16(pacc[1][2], cat8(pl[1], ph[1]), px[2]);
        mm16(pacc[1][3], cat8(pl[1], ph[1]), px[3]);
        SBAR();
        pl[0] = ldstr(pw2[1] + 0); ph[0] = ldstr(pw2[1] + 0 + 512);
        mm16(pacc[2][0], cat8(pl[2], ph[2]), px[0]);
        mm16(pacc[2][1], cat8(pl[2], ph[2]), px[1]);
        st16kv(wsr, kvo, kst + 8448, hb[2]);
        SBAR();
        mm16(pacc[2][2], cat8(pl[2], ph[2]), px[2]);
        mm16(pacc[2][3], cat8(pl[2], ph[2]), px[3]);
        SBAR();
        pl[1] = ldstr(pw2[1] + 4096); ph[1] = ldstr(pw2[1] + 4096 + 512);
        mm16(pacc[3][0], cat8(pl[3], ph[3]), px[0]);
        mm16(pacc[3][1], cat8(pl[3], ph[3]), px[1]);
        st16kv(wsr, kvo, kst + 10496, hb[3]);
        SBAR();
        mm16(pacc[3][2], cat8(pl[3], ph[3]), px[2]);
        mm16(pacc[3][3], cat8(pl[3], ph[3]), px[3]);
        SBAR();
        mm16(pacc[4][0], cat8(pl[0], ph[0]), px[0]);
        mm16(pacc[4][1], cat8(pl[0], ph[0]), px[1]);
        SBAR();
        wait_vm_bar<63>();
        pl[2] = ldstr(pw0[1] + 32768); ph[2] = ldstr(pw0[1] + 32768 + 512);
        mm16(pacc[5][0], cat8(pl[1], ph[1]), px[0]);
        mm16(pacc[5][1], cat8(pl[1], ph[1]), px[1]);
        st16kv(wsr, vvo, vst + 8192, held[4]);
        SBAR();
        px[0] = lds128(pxr[1] + 32768); px[1] = lds128(pxr[1] + 33024); pl[3] = ldstr(pw0[1] + 36864); ph[3] = ldstr(pw0[1] + 36864 + 512);
        mm16(pacc[4][2], cat8(pl[0], ph[0]), px[2]);
        mm16(pacc[4][3], cat8(pl[0], ph[0]), px[3]);
        SBAR();
        mm16(pacc[5][2], cat8(pl[1], ph[1]), px[2]);
        mm16(pacc[5][3], cat8(pl[1], ph[1]), px[3]);
        st16kv(wsr, vvo, vst + 9216, held[5]);
        SBAR();
        px[2] = lds128(pxr[1] + 40960); px[3] = lds128(pxr[1] + 41216);
        pl[0] = ldstr(pw1[1] + 32768); ph[0] = ldstr(pw1[1] + 32768 + 512);
        mm16(pacc[0][0], cat8(pl[2], ph[2]), px[0]);
        mm16(pacc[0][1], cat8(pl[2], ph[2]), px[1]);
        SBAR();
        mm16(pacc[1][0], cat8(pl[3], ph[3]), px[0]);
        mm16(pacc[1][1], cat8(pl[3], ph[3]), px[1]);
        st16kv(wsr, vvo, vst + 12288, held[6]);
        SBAR();
        pl[1] = ldstr(pw1[1] + 36864); ph[1] = ldstr(pw1[1] + 36864 + 512);
        mm16(pacc[0][2], cat8(pl[2], ph[2]), px[2]);
        mm16(pacc[0][3], cat8(pl[2], ph[2]), px[3]);
        SBAR();
        mm16(pacc[1][2], cat8(pl[3], ph[3]), px[2]);
        mm16(pacc[1][3], cat8(pl[3], ph[3]), px[3]);
        st16kv(wsr, vvo, vst + 13312, held[7]);
        SBAR();
        pl[2] = ldstr(pw2[1] + 32768); ph[2] = ldstr(pw2[1] + 32768 + 512);
        mm16(pacc[2][0], cat8(pl[0], ph[0]), px[0]);
        mm16(pacc[2][1], cat8(pl[0], ph[0]), px[1]);
        SBAR();
        mm16(pacc[2][2], cat8(pl[0], ph[0]), px[2]);
        mm16(pacc[2][3], cat8(pl[0], ph[0]), px[3]);
        SBAR();
        pl[3] = ldstr(pw2[1] + 36864); ph[3] = ldstr(pw2[1] + 36864 + 512);
        mm16(pacc[3][0], cat8(pl[1], ph[1]), px[0]);
        mm16(pacc[3][1], cat8(pl[1], ph[1]), px[1]);
        SBAR();
        mm16(pacc[3][2], cat8(pl[1], ph[1]), px[2]);
        mm16(pacc[3][3], cat8(pl[1], ph[1]), px[3]);
        SBAR();
        mm16(pacc[4][0], cat8(pl[2], ph[2]), px[0]);
        mm16(pacc[4][1], cat8(pl[2], ph[2]), px[1]);
        SBAR();
        wait_vm_bar<63>();
        pl[0] = ldstr(pw0[0] + 0); ph[0] = ldstr(pw0[0] + 0 + 512);
        mm16(pacc[5][0], cat8(pl[3], ph[3]), px[0]);
        mm16(pacc[5][1], cat8(pl[3], ph[3]), px[1]);
        st16kv(wsr, vvo, vst + 16384, held[8]);
        SBAR();
        px[0] = lds128(pxr[0] + 0); px[1] = lds128(pxr[0] + 256); pl[1] = ldstr(pw0[0] + 4096); ph[1] = ldstr(pw0[0] + 4096 + 512);
        mm16(pacc[4][2], cat8(pl[2], ph[2]), px[2]);
        mm16(pacc[4][3], cat8(pl[2], ph[2]), px[3]);
        SBAR();
        mm16(pacc[5][2], cat8(pl[3], ph[3]), px[2]);
        mm16(pacc[5][3], cat8(pl[3], ph[3]), px[3]);
        st16kv(wsr, vvo, vst + 17408, held[9]);
        SBAR();
        px[2] = lds128(pxr[0] + 8192); px[3] = lds128(pxr[0] + 8448);
        pl[2] = ldstr(pw1[0] + 0); ph[2] = ldstr(pw1[0] + 0 + 512);
        mm16(pacc[0][0], cat8(pl[0], ph[0]), px[0]);
        mm16(pacc[0][1], cat8(pl[0], ph[0]), px[1]);
        SBAR();
        mm16(pacc[1][0], cat8(pl[1], ph[1]), px[0]);
        mm16(pacc[1][1], cat8(pl[1], ph[1]), px[1]);
        st16kv(wsr, vvo, vst + 20480, held[10]);
        SBAR();
        pl[3] = ldstr(pw1[0] + 4096); ph[3] = ldstr(pw1[0] + 4096 + 512);
        mm16(pacc[0][2], cat8(pl[0], ph[0]), px[2]);
        mm16(pacc[0][3], cat8(pl[0], ph[0]), px[3]);
        SBAR();
        mm16(pacc[1][2], cat8(pl[1], ph[1]), px[2]);
        mm16(pacc[1][3], cat8(pl[1], ph[1]), px[3]);
        st16kv(wsr, vvo, vst + 21504, held[11]);
        SBAR();
        pl[0] = ldstr(pw2[0] + 0); ph[0] = ldstr(pw2[0] + 0 + 512);
        mm16(pacc[2][0], cat8(pl[2], ph[2]), px[0]);
        mm16(pacc[2][1], cat8(pl[2], ph[2]), px[1]);
        SBAR();
        mm16(pacc[2][2], cat8(pl[2], ph[2]), px[2]);
        mm16(pacc[2][3], cat8(pl[2], ph[2]), px[3]);
        SBAR();
        pl[1] = ldstr(pw2[0] + 4096); ph[1] = ldstr(pw2[0] + 4096 + 512);
        mm16(pacc[3][0], cat8(pl[3], ph[3]), px[0]);
        mm16(pacc[3][1], cat8(pl[3], ph[3]), px[1]);
        SBAR();
        mm16(pacc[3][2], cat8(pl[3], ph[3]), px[2]);
        mm16(pacc[3][3], cat8(pl[3], ph[3]), px[3]);
        SBAR();
        mm16(pacc[4][0], cat8(pl[0], ph[0]), px[0]);
        mm16(pacc[4][1], cat8(pl[0], ph[0]), px[1]);
        SBAR();
        wait_vm_bar<63>();
        pl[2] = ldstr(pw0[0] + 32768); ph[2] = ldstr(pw0[0] + 32768 + 512);
        mm16(pacc[5][0], cat8(pl[1], ph[1]), px[0]);
        mm16(pacc[5][1], cat8(pl[1], ph[1]), px[1]);
        warmv(wjunk, lane * 128, wp2, j == 0 ? 8192u : 0u);
        SBAR();
        px[0] = lds128(pxr[0] + 32768); px[1] = lds128(pxr[0] + 33024); pl[3] = ldstr(pw0[0] + 36864); ph[3] = ldstr(pw0[0] + 36864 + 512);
        mm16(pacc[4][2], cat8(pl[0], ph[0]), px[2]);
        mm16(pacc[4][3], cat8(pl[0], ph[0]), px[3]);
        st16kv(wsr, vvo, vst + 24576, held[12]);
        SBAR();
        mm16(pacc[5][2], cat8(pl[1], ph[1]), px[2]);
        mm16(pacc[5][3], cat8(pl[1], ph[1]), px[3]);
        SBAR();
        px[2] = lds128(pxr[0] + 40960); px[3] = lds128(pxr[0] + 41216);
        pl[0] = ldstr(pw1[0] + 32768); ph[0] = ldstr(pw1[0] + 32768 + 512);
        mm16(pacc[0][0], cat8(pl[2], ph[2]), px[0]);
        mm16(pacc[0][1], cat8(pl[2], ph[2]), px[1]);
        st16kv(wsr, vvo, vst + 25600, held[13]);
        SBAR();
        mm16(pacc[1][0], cat8(pl[3], ph[3]), px[0]);
        mm16(pacc[1][1], cat8(pl[3], ph[3]), px[1]);
        SBAR();
        pl[1] = ldstr(pw1[0] + 36864); ph[1] = ldstr(pw1[0] + 36864 + 512);
        mm16(pacc[0][2], cat8(pl[2], ph[2]), px[2]);
        mm16(pacc[0][3], cat8(pl[2], ph[2]), px[3]);
        st16kv(wsr, vvo, vst + 28672, held[14]);
        SBAR();
        mm16(pacc[1][2], cat8(pl[3], ph[3]), px[2]);
        mm16(pacc[1][3], cat8(pl[3], ph[3]), px[3]);
        SBAR();
        pl[2] = ldstr(pw2[0] + 32768); ph[2] = ldstr(pw2[0] + 32768 + 512);
        mm16(pacc[2][0], cat8(pl[0], ph[0]), px[0]);
        mm16(pacc[2][1], cat8(pl[0], ph[0]), px[1]);
        st16kv(wsr, vvo, vst + 29696, held[15]);
        SBAR();
        mm16(pacc[2][2], cat8(pl[0], ph[0]), px[2]);
        mm16(pacc[2][3], cat8(pl[0], ph[0]), px[3]);
        SBAR();
        pl[3] = ldstr(pw2[0] + 36864); ph[3] = ldstr(pw2[0] + 36864 + 512);
        mm16(pacc[3][0], cat8(pl[1], ph[1]), px[0]);
        mm16(pacc[3][1], cat8(pl[1], ph[1]), px[1]);
        SBAR();
        mm16(pacc[3][2], cat8(pl[1], ph[1]), px[2]);
        mm16(pacc[3][3], cat8(pl[1], ph[1]), px[3]);
        SBAR();
        mm16(pacc[4][0], cat8(pl[2], ph[2]), px[0]);
        mm16(pacc[4][1], cat8(pl[2], ph[2]), px[1]);
        SBAR();
        wait_vm_bar<63>();
        pl[0] = ldstr(pw0[1] + 0); ph[0] = ldstr(pw0[1] + 0 + 512);
        mm16(pacc[5][0], cat8(pl[3], ph[3]), px[0]);
        mm16(pacc[5][1], cat8(pl[3], ph[3]), px[1]);
        warmv(wjunk, lane * 128, wp2, j == 1 ? 8192u : 0u);
        SBAR();
        px[0] = lds128(pxr[1] + 0); px[1] = lds128(pxr[1] + 256); pl[1] = ldstr(pw0[1] + 4096); ph[1] = ldstr(pw0[1] + 4096 + 512);
        mm16(pacc[4][2], cat8(pl[2], ph[2]), px[2]);
        mm16(pacc[4][3], cat8(pl[2], ph[2]), px[3]);
        hb[0] = *(const LAS u32x4*)(hand + 0);
        hb[1] = *(const LAS u32x4*)(hand + 1024);
        SBAR();
        mm16(pacc[5][2], cat8(pl[3], ph[3]), px[2]);
        mm16(pacc[5][3], cat8(pl[3], ph[3]), px[3]);
        hb[2] = *(const LAS u32x4*)(hand + 2048);
        hb[3] = *(const LAS u32x4*)(hand + 3072);
        SBAR();
        px[2] = lds128(pxr[1] + 8192); px[3] = lds128(pxr[1] + 8448);
        pl[2] = ldstr(pw1[1] + 0); ph[2] = ldstr(pw1[1] + 0 + 512);
        mm16(pacc[0][0], cat8(pl[0], ph[0]), px[0]);
        mm16(pacc[0][1], cat8(pl[0], ph[0]), px[1]);
        SBAR();
        mm16(pacc[1][0], cat8(pl[1], ph[1]), px[0]);
        mm16(pacc[1][1], cat8(pl[1], ph[1]), px[1]);
        st16kv(wsr, kvo, kst + 16384, hb[0]);
        SBAR();
        pl[3] = ldstr(pw1[1] + 4096); ph[3] = ldstr(pw1[1] + 4096 + 512);
        mm16(pacc[0][2], cat8(pl[0], ph[0]), px[2]);
        mm16(pacc[0][3], cat8(pl[0], ph[0]), px[3]);
        SBAR();
        mm16(pacc[1][2], cat8(pl[1], ph[1]), px[2]);
        mm16(pacc[1][3], cat8(pl[1], ph[1]), px[3]);
        st16kv(wsr, kvo, kst + 18432, hb[1]);
        SBAR();
        pl[0] = ldstr(pw2[1] + 0); ph[0] = ldstr(pw2[1] + 0 + 512);
        mm16(pacc[2][0], cat8(pl[2], ph[2]), px[0]);
        mm16(pacc[2][1], cat8(pl[2], ph[2]), px[1]);
        SBAR();
        mm16(pacc[2][2], cat8(pl[2], ph[2]), px[2]);
        mm16(pacc[2][3], cat8(pl[2], ph[2]), px[3]);
        st16kv(wsr, kvo, kst + 16640, hb[2]);
        SBAR();
        pl[1] = ldstr(pw2[1] + 4096); ph[1] = ldstr(pw2[1] + 4096 + 512);
        mm16(pacc[3][0], cat8(pl[3], ph[3]), px[0]);
        mm16(pacc[3][1], cat8(pl[3], ph[3]), px[1]);
        SBAR();
        mm16(pacc[3][2], cat8(pl[3], ph[3]), px[2]);
        mm16(pacc[3][3], cat8(pl[3], ph[3]), px[3]);
        st16kv(wsr, kvo, kst + 18688, hb[3]);
        SBAR();
        mm16(pacc[4][0], cat8(pl[0], ph[0]), px[0]);
        mm16(pacc[4][1], cat8(pl[0], ph[0]), px[1]);
        SBAR();
        wait_vm_bar<63>();
        pl[2] = ldstr(pw0[1] + 32768); ph[2] = ldstr(pw0[1] + 32768 + 512);
        mm16(pacc[5][0], cat8(pl[1], ph[1]), px[0]);
        mm16(pacc[5][1], cat8(pl[1], ph[1]), px[1]);
        warmv(wjunk, lane * 128, wp2, j == 2 ? 8192u : 0u);
        SBAR();
        px[0] = lds128(pxr[1] + 32768); px[1] = lds128(pxr[1] + 33024); pl[3] = ldstr(pw0[1] + 36864); ph[3] = ldstr(pw0[1] + 36864 + 512);
        mm16(pacc[4][2], cat8(pl[0], ph[0]), px[2]);
        mm16(pacc[4][3], cat8(pl[0], ph[0]), px[3]);
        hb[0] = *(const LAS u32x4*)(hand + 4096);
        hb[1] = *(const LAS u32x4*)(hand + 5120);
        SBAR();
        mm16(pacc[5][2], cat8(pl[1], ph[1]), px[2]);
        mm16(pacc[5][3], cat8(pl[1], ph[1]), px[3]);
        hb[2] = *(const LAS u32x4*)(hand + 6144);
        hb[3] = *(const LAS u32x4*)(hand + 7168);
        SBAR();
        px[2] = lds128(pxr[1] + 40960); px[3] = lds128(pxr[1] + 41216);
        pl[0] = ldstr(pw1[1] + 32768); ph[0] = ldstr(pw1[1] + 32768 + 512);
        mm16(pacc[0][0], cat8(pl[2], ph[2]), px[0]);
        mm16(pacc[0][1], cat8(pl[2], ph[2]), px[1]);
        SBAR();
        mm16(pacc[1][0], cat8(pl[3], ph[3]), px[0]);
        mm16(pacc[1][1], cat8(pl[3], ph[3]), px[1]);
        st16kv(wsr, kvo, kst + 24576, hb[0]);
        SBAR();
        pl[1] = ldstr(pw1[1] + 36864); ph[1] = ldstr(pw1[1] + 36864 + 512);
        mm16(pacc[0][2], cat8(pl[2], ph[2]), px[2]);
        mm16(pacc[0][3], cat8(pl[2], ph[2]), px[3]);
        SBAR();
        mm16(pacc[1][2], cat8(pl[3], ph[3]), px[2]);
        mm16(pacc[1][3], cat8(pl[3], ph[3]), px[3]);
        st16kv(wsr, kvo, kst + 26624, hb[1]);
        SBAR();
        pl[2] = ldstr(pw2[1] + 32768); ph[2] = ldstr(pw2[1] + 32768 + 512);
        mm16(pacc[2][0], cat8(pl[0], ph[0]), px[0]);
        mm16(pacc[2][1], cat8(pl[0], ph[0]), px[1]);
        SBAR();
        mm16(pacc[2][2], cat8(pl[0], ph[0]), px[2]);
        mm16(pacc[2][3], cat8(pl[0], ph[0]), px[3]);
        st16kv(wsr, kvo, kst + 24832, hb[2]);
        SBAR();
        pl[3] = ldstr(pw2[1] + 36864); ph[3] = ldstr(pw2[1] + 36864 + 512);
        mm16(pacc[3][0], cat8(pl[1], ph[1]), px[0]);
        mm16(pacc[3][1], cat8(pl[1], ph[1]), px[1]);
        SBAR();
        mm16(pacc[3][2], cat8(pl[1], ph[1]), px[2]);
        mm16(pacc[3][3], cat8(pl[1], ph[1]), px[3]);
        st16kv(wsr, kvo, kst + 26880, hb[3]);
        SBAR();
        mm16(pacc[4][0], cat8(pl[2], ph[2]), px[0]);
        mm16(pacc[4][1], cat8(pl[2], ph[2]), px[1]);
        SBAR();
        wait_vm_bar<63>();
        pl[0] = ldstr(pw0[0] + 0); ph[0] = ldstr(pw0[0] + 0 + 512);
        mm16(pacc[5][0], cat8(pl[3], ph[3]), px[0]);
        mm16(pacc[5][1], cat8(pl[3], ph[3]), px[1]);
        warmv(wjunk, lane * 128, wp2, j == 3 ? 8192u : 0u);
        SBAR();
        px[0] = lds128(pxr[0] + 0); px[1] = lds128(pxr[0] + 256); pl[1] = ldstr(pw0[0] + 4096); ph[1] = ldstr(pw0[0] + 4096 + 512);
        mm16(pacc[4][2], cat8(pl[2], ph[2]), px[2]);
        mm16(pacc[4][3], cat8(pl[2], ph[2]), px[3]);
        SBAR();
        mm16(pacc[5][2], cat8(pl[3], ph[3]), px[2]);
        mm16(pacc[5][3], cat8(pl[3], ph[3]), px[3]);
        SBAR();
        px[2] = lds128(pxr[0] + 8192); px[3] = lds128(pxr[0] + 8448);
        pl[2] = ldstr(pw1[0] + 0); ph[2] = ldstr(pw1[0] + 0 + 512);
        mm16(pacc[0][0], cat8(pl[0], ph[0]), px[0]);
        mm16(pacc[0][1], cat8(pl[0], ph[0]), px[1]);
        SBAR();
        mm16(pacc[1][0], cat8(pl[1], ph[1]), px[0]);
        mm16(pacc[1][1], cat8(pl[1], ph[1]), px[1]);
        SBAR();
        pl[3] = ldstr(pw1[0] + 4096); ph[3] = ldstr(pw1[0] + 4096 + 512);
        mm16(pacc[0][2], cat8(pl[0], ph[0]), px[2]);
        mm16(pacc[0][3], cat8(pl[0], ph[0]), px[3]);
        SBAR();
        mm16(pacc[1][2], cat8(pl[1], ph[1]), px[2]);
        mm16(pacc[1][3], cat8(pl[1], ph[1]), px[3]);
        SBAR();
        pl[0] = ldstr(pw2[0] + 0); ph[0] = ldstr(pw2[0] + 0 + 512);
        mm16(pacc[2][0], cat8(pl[2], ph[2]), px[0]);
        mm16(pacc[2][1], cat8(pl[2], ph[2]), px[1]);
        SBAR();
        mm16(pacc[2][2], cat8(pl[2], ph[2]), px[2]);
        mm16(pacc[2][3], cat8(pl[2], ph[2]), px[3]);
        SBAR();
        pl[1] = ldstr(pw2[0] + 4096); ph[1] = ldstr(pw2[0] + 4096 + 512);
        mm16(pacc[3][0], cat8(pl[3], ph[3]), px[0]);
        mm16(pacc[3][1], cat8(pl[3], ph[3]), px[1]);
        SBAR();
        mm16(pacc[3][2], cat8(pl[3], ph[3]), px[2]);
        mm16(pacc[3][3], cat8(pl[3], ph[3]), px[3]);
        SBAR();
        mm16(pacc[4][0], cat8(pl[0], ph[0]), px[0]);
        mm16(pacc[4][1], cat8(pl[0], ph[0]), px[1]);
        SBAR();
        wait_vm_bar<63>();
        pl[2] = ldstr(pw0[0] + 32768); ph[2] = ldstr(pw0[0] + 32768 + 512);
        mm16(pacc[5][0], cat8(pl[1], ph[1]), px[0]);
        mm16(pacc[5][1], cat8(pl[1], ph[1]), px[1]);
        warmv(wjunk, lane * 128, wp2, j == 4 ? 8192u : 0u);
        SBAR();
        px[0] = lds128(pxr[0] + 32768); px[1] = lds128(pxr[0] + 33024); pl[3] = ldstr(pw0[0] + 36864); ph[3] = ldstr(pw0[0] + 36864 + 512);
        mm16(pacc[4][2], cat8(pl[0], ph[0]), px[2]);
        mm16(pacc[4][3], cat8(pl[0], ph[0]), px[3]);
        SBAR();
        mm16(pacc[5][2], cat8(pl[1], ph[1]), px[2]);
        mm16(pacc[5][3], cat8(pl[1], ph[1]), px[3]);
        SBAR();
        px[2] = lds128(pxr[0] + 40960); px[3] = lds128(pxr[0] + 41216);
        pl[0] = ldstr(pw1[0] + 32768); ph[0] = ldstr(pw1[0] + 32768 + 512);
        mm16(pacc[0][0], cat8(pl[2], ph[2]), px[0]);
        mm16(pacc[0][1], cat8(pl[2], ph[2]), px[1]);
        SBAR();
        mm16(pacc[1][0], cat8(pl[3], ph[3]), px[0]);
        mm16(pacc[1][1], cat8(pl[3], ph[3]), px[1]);
        SBAR();
        pl[1] = ldstr(pw1[0] + 36864); ph[1] = ldstr(pw1[0] + 36864 + 512);
        mm16(pacc[0][2], cat8(pl[2], ph[2]), px[2]);
        mm16(pacc[0][3], cat8(pl[2], ph[2]), px[3]);
        SBAR();
        mm16(pacc[1][2], cat8(pl[3], ph[3]), px[2]);
        mm16(pacc[1][3], cat8(pl[3], ph[3]), px[3]);
        SBAR();
        pl[2] = ldstr(pw2[0] + 32768); ph[2] = ldstr(pw2[0] + 32768 + 512);
        mm16(pacc[2][0], cat8(pl[0], ph[0]), px[0]);
        mm16(pacc[2][1], cat8(pl[0], ph[0]), px[1]);
        SBAR();
        mm16(pacc[2][2], cat8(pl[0], ph[0]), px[2]);
        mm16(pacc[2][3], cat8(pl[0], ph[0]), px[3]);
        SBAR();
        pl[3] = ldstr(pw2[0] + 36864); ph[3] = ldstr(pw2[0] + 36864 + 512);
        mm16(pacc[3][0], cat8(pl[1], ph[1]), px[0]);
        mm16(pacc[3][1], cat8(pl[1], ph[1]), px[1]);
        SBAR();
        mm16(pacc[3][2], cat8(pl[1], ph[1]), px[2]);
        mm16(pacc[3][3], cat8(pl[1], ph[1]), px[3]);
        SBAR();
        mm16(pacc[4][0], cat8(pl[2], ph[2]), px[0]);
        mm16(pacc[4][1], cat8(pl[2], ph[2]), px[1]);
        SBAR();
        wait_vm_bar<63>();
        pl[0] = ldstr(pw0[1] + 0); ph[0] = ldstr(pw0[1] + 0 + 512);
        mm16(pacc[5][0], cat8(pl[3], ph[3]), px[0]);
        mm16(pacc[5][1], cat8(pl[3], ph[3]), px[1]);
        warmv(wjunk, lane * 128, wp2, j == 5 ? 8192u : 0u);
        SBAR();
        px[0] = lds128(pxr[1] + 0); px[1] = lds128(pxr[1] + 256); pl[1] = ldstr(pw0[1] + 4096); ph[1] = ldstr(pw0[1] + 4096 + 512);
        mm16(pacc[4][2], cat8(pl[2], ph[2]), px[2]);
        mm16(pacc[4][3], cat8(pl[2], ph[2]), px[3]);
        SBAR();
        mm16(pacc[5][2], cat8(pl[3], ph[3]), px[2]);
        mm16(pacc[5][3], cat8(pl[3], ph[3]), px[3]);
        SBAR();
        px[2] = lds128(pxr[1] + 8192); px[3] = lds128(pxr[1] + 8448);
        pl[2] = ldstr(pw1[1] + 0); ph[2] = ldstr(pw1[1] + 0 + 512);
        mm16(pacc[0][0], cat8(pl[0], ph[0]), px[0]);
        mm16(pacc[0][1], cat8(pl[0], ph[0]), px[1]);
        SBAR();
        mm16(pacc[1][0], cat8(pl[1], ph[1]), px[0]);
        mm16(pacc[1][1], cat8(pl[1], ph[1]), px[1]);
        SBAR();
        pl[3] = ldstr(pw1[1] + 4096); ph[3] = ldstr(pw1[1] + 4096 + 512);
        mm16(pacc[0][2], cat8(pl[0], ph[0]), px[2]);
        mm16(pacc[0][3], cat8(pl[0], ph[0]), px[3]);
        SBAR();
        mm16(pacc[1][2], cat8(pl[1], ph[1]), px[2]);
        mm16(pacc[1][3], cat8(pl[1], ph[1]), px[3]);
        SBAR();
        pl[0] = ldstr(pw2[1] + 0); ph[0] = ldstr(pw2[1] + 0 + 512);
        mm16(pacc[2][0], cat8(pl[2], ph[2]), px[0]);
        mm16(pacc[2][1], cat8(pl[2], ph[2]), px[1]);
        SBAR();
        mm16(pacc[2][2], cat8(pl[2], ph[2]), px[2]);
        mm16(pacc[2][3], cat8(pl[2], ph[2]), px[3]);
        SBAR();
        pl[1] = ldstr(pw2[1] + 4096); ph[1] = ldstr(pw2[1] + 4096 + 512);
        mm16(pacc[3][0], cat8(pl[3], ph[3]), px[0]);
        mm16(pacc[3][1], cat8(pl[3], ph[3]), px[1]);
        SBAR();
        mm16(pacc[3][2], cat8(pl[3], ph[3]), px[2]);
        mm16(pacc[3][3], cat8(pl[3], ph[3]), px[3]);
        SBAR();
        mm16(pacc[4][0], cat8(pl[0], ph[0]), px[0]);
        mm16(pacc[4][1], cat8(pl[0], ph[0]), px[1]);
        SBAR();
        wait_vm_bar<0>();
        pl[2] = ldstr(pw0[1] + 32768); ph[2] = ldstr(pw0[1] + 32768 + 512);
        mm16(pacc[5][0], cat8(pl[1], ph[1]), px[0]);
        mm16(pacc[5][1], cat8(pl[1], ph[1]), px[1]);
        warmv(wjunk, lane * 128, wp2, j == 6 ? 8192u : 0u);
        SBAR();
        px[0] = lds128(pxr[1] + 32768); px[1] = lds128(pxr[1] + 33024); pl[3] = ldstr(pw0[1] + 36864); ph[3] = ldstr(pw0[1] + 36864 + 512);
        mm16(pacc[4][2], cat8(pl[0], ph[0]), px[2]);
        mm16(pacc[4][3], cat8(pl[0], ph[0]), px[3]);
        st_flag(wsr4, WS_FLAG + F_KV * 32768 + b * 128, tag);
        SBAR();
        mm16(pacc[5][2], cat8(pl[1], ph[1]), px[2]);
        mm16(pacc[5][3], cat8(pl[1], ph[1]), px[3]);
        SBAR();
        px[2] = lds128(pxr[1] + 40960); px[3] = lds128(pxr[1] + 41216);
        pl[0] = ldstr(pw1[1] + 32768); ph[0] = ldstr(pw1[1] + 32768 + 512);
        mm16(pacc[0][0], cat8(pl[2], ph[2]), px[0]);
        mm16(pacc[0][1], cat8(pl[2], ph[2]), px[1]);
        SBAR();
        mm16(pacc[1][0], cat8(pl[3], ph[3]), px[0]);
        mm16(pacc[1][1], cat8(pl[3], ph[3]), px[1]);
        SBAR();
        pl[1] = ldstr(pw1[1] + 36864); ph[1] = ldstr(pw1[1] + 36864 + 512);
        mm16(pacc[0][2], cat8(pl[2], ph[2]), px[2]);
        mm16(pacc[0][3], cat8(pl[2], ph[2]), px[3]);
        SBAR();
        mm16(pacc[1][2], cat8(pl[3], ph[3]), px[2]);
        mm16(pacc[1][3], cat8(pl[3], ph[3]), px[3]);
        SBAR();
        pl[2] = ldstr(pw2[1] + 32768); ph[2] = ldstr(pw2[1] + 32768 + 512);
        mm16(pacc[2][0], cat8(pl[0], ph[0]), px[0]);
        mm16(pacc[2][1], cat8(pl[0], ph[0]), px[1]);
        SBAR();
        mm16(pacc[2][2], cat8(pl[0], ph[0]), px[2]);
        mm16(pacc[2][3], cat8(pl[0], ph[0]), px[3]);
        SBAR();
        pl[3] = ldstr(pw2[1] + 36864); ph[3] = ldstr(pw2[1] + 36864 + 512);
        mm16(pacc[3][0], cat8(pl[1], ph[1]), px[0]);
        mm16(pacc[3][1], cat8(pl[1], ph[1]), px[1]);
        SBAR();
        mm16(pacc[3][2], cat8(pl[1], ph[1]), px[2]);
        mm16(pacc[3][3], cat8(pl[1], ph[1]), px[3]);
        SBAR();
        mm16(pacc[4][0], cat8(pl[2], ph[2]), px[0]);
        mm16(pacc[4][1], cat8(pl[2], ph[2]), px[1]);
        SBAR();
        wait_vm_bar<63>();
        pl[0] = ldstr(pw0[0] + 0); ph[0] = ldstr(pw0[0] + 0 + 512);
        mm16(pacc[5][0], cat8(pl[3], ph[3]), px[0]);
        mm16(pacc[5][1], cat8(pl[3], ph[3]), px[1]);
        warmv(wjunk, lane * 128, wp2, j == 7 ? 8192u : 0u);
        SBAR();
        px[0] = lds128(pxr[0] + 0); px[1] = lds128(pxr[0] + 256); pl[1] = ldstr(pw0[0] + 4096); ph[1] = ldstr(pw0[0] + 4096 + 512);
        mm16(pacc[4][2], cat8(pl[2], ph[2]), px[2]);
        mm16(pacc[4][3], cat8(pl[2], ph[2]), px[3]);
        SBAR();
        mm16(pacc[5][2], cat8(pl[3], ph[3]), px[2]);
        mm16(pacc[5][3], cat8(pl[3], ph[3]), px[3]);
        SBAR();
        px[2] = lds128(pxr[0] + 8192); px[3] = lds128(pxr[0] + 8448);
        pl[2] = ldstr(pw1[0] + 0); ph[2] = ldstr(pw1[0] + 0 + 512);
        mm16(pacc[0][0], cat8(pl[0], ph[0]), px[0]);
        mm16(pacc[0][1], cat8(pl[0], ph[0]), px[1]);
        SBAR();
        mm16(pacc[1][0], cat8(pl[1], ph[1]), px[0]);
        mm16(pacc[1][1], cat8(pl[1], ph[1]), px[1]);
        SBAR();
        pl[3] = ldstr(pw1[0] + 4096); ph[3] = ldstr(pw1[0] + 4096 + 512);
        mm16(pacc[0][2], cat8(pl[0], ph[0]), px[2]);
        mm16(pacc[0][3], cat8(pl[0], ph[0]), px[3]);
        SBAR();
        mm16(pacc[1][2], cat8(pl[1], ph[1]), px[2]);
        mm16(pacc[1][3], cat8(pl[1], ph[1]), px[3]);
        SBAR();
        pl[0] = ldstr(pw2[0] + 0); ph[0] = ldstr(pw2[0] + 0 + 512);
        mm16(pacc[2][0], cat8(pl[2], ph[2]), px[0]);
        mm16(pacc[2][1], cat8(pl[2], ph[2]), px[1]);
        SBAR();
        mm16(pacc[2][2], cat8(pl[2], ph[2]), px[2]);
        mm16(pacc[2][3], cat8(pl[2], ph[2]), px[3]);
        SBAR();
        pl[1] = ldstr(pw2[0] + 4096); ph[1] = ldstr(pw2[0] + 4096 + 512);
        mm16(pacc[3][0], cat8(pl[3], ph[3]), px[0]);
        mm16(pacc[3][1], cat8(pl[3], ph[3]), px[1]);
        SBAR();
        mm16(pacc[3][2], cat8(pl[3], ph[3]), px[2]);
        mm16(pacc[3][3], cat8(pl[3], ph[3]), px[3]);
        SBAR();
        mm16(pacc[4][0], cat8(pl[0], ph[0]), px[0]);
        mm16(pacc[4][1], cat8(pl[0], ph[0]), px[1]);
        SBAR();
        wait_vm_bar<63>();
        pl[2] = ldstr(pw0[0] + 32768); ph[2] = ldstr(pw0[0] + 32768 + 512);
        mm16(pacc[5][0], cat8(pl[1], ph[1]), px[0]);
        mm16(pacc[5][1], cat8(pl[1], ph[1]), px[1]);
        SBAR();
        px[0] = lds128(pxr[0] + 32768); px[1] = lds128(pxr[0] + 33024); pl[3] = ldstr(pw0[0] + 36864); ph[3] = ldstr(pw0[0] + 36864 + 512);
        mm16(pacc[4][2], cat8(pl[0], ph[0]), px[2]);
        mm16(pacc[4][3], cat8(pl[0], ph[0]), px[3]);
        SBAR();
        mm16(pacc[5][2], cat8(pl[1], ph[1]), px[2]);
        mm16(pacc[5][3], cat8(pl[1], ph[1]), px[3]);
        SBAR();
        px[2] = lds128(pxr[0] + 40960); px[3] = lds128(pxr[0] + 41216);
        pl[0] = ldstr(pw1[0] + 32768); ph[0] = ldstr(pw1[0] + 32768 + 512);
        mm16(pacc[0][0], cat8(pl[2], ph[2]), px[0]);
        mm16(pacc[0][1], cat8(pl[2], ph[2]), px[1]);
        SBAR();
        mm16(pacc[1][0], cat8(pl[3], ph[3]), px[0]);
        mm16(pacc[1][1], cat8(pl[3], ph[3]), px[1]);
        SBAR();
        pl[1] = ldstr(pw1[0] + 36864); ph[1] = ldstr(pw1[0] + 36864 + 512);
        mm16(pacc[0][2], cat8(pl[2], ph[2]), px[2]);
        mm16(pacc[0][3], cat8(pl[2], ph[2]), px[3]);
        SBAR();
        mm16(pacc[1][2], cat8(pl[3], ph[3]), px[2]);
        mm16(pacc[1][3], cat8(pl[3], ph[3]), px[3]);
        SBAR();
        pl[2] = ldstr(pw2[0] + 32768); ph[2] = ldstr(pw2[0] + 32768 + 512);
        mm16(pacc[2][0], cat8(pl[0], ph[0]), px[0]);
        mm16(pacc[2][1], cat8(pl[0], ph[0]), px[1]);
        SBAR();
        mm16(pacc[2][2], cat8(pl[0], ph[0]), px[2]);
        mm16(pacc[2][3], cat8(pl[0], ph[0]), px[3]);
        SBAR();
        pl[3] = ldstr(pw2[0] + 36864); ph[3] = ldstr(pw2[0] + 36864 + 512);
        mm16(pacc[3][0], cat8(pl[1], ph[1]), px[0]);
        mm16(pacc[3][1], cat8(pl[1], ph[1]), px[1]);
        SBAR();
        mm16(pacc[3][2], cat8(pl[1], ph[1]), px[2]);
        mm16(pacc[3][3], cat8(pl[1], ph[1]), px[3]);
        SBAR();
        mm16(pacc[4][0], cat8(pl[2], ph[2]), px[0]);
        mm16(pacc[4][1], cat8(pl[2], ph[2]), px[1]);
        SBAR();
        wait_vm_bar<63>();
        pl[0] = ldstr(pw0[1] + 0); ph[0] = ldstr(pw0[1] + 0 + 512);
        mm16(pacc[5][0], cat8(pl[3], ph[3]), px[0]);
        mm16(pacc[5][1], cat8(pl[3], ph[3]), px[1]);
        SBAR();
        px[0] = lds128(pxr[1] + 0); px[1] = lds128(pxr[1] + 256); pl[1] = ldstr(pw0[1] + 4096); ph[1] = ldstr(pw0[1] + 4096 + 512);
        mm16(pacc[4][2], cat8(pl[2], ph[2]), px[2]);
        mm16(pacc[4][3], cat8(pl[2], ph[2]), px[3]);
        SBAR();
        mm16(pacc[5][2], cat8(pl[3], ph[3]), px[2]);
        mm16(pacc[5][3], cat8(pl[3], ph[3]), px[3]);
        SBAR();
        px[2] = lds128(pxr[1] + 8192); px[3] = lds128(pxr[1] + 8448);
        pl[2] = ldstr(pw1[1] + 0); ph[2] = ldstr(pw1[1] + 0 + 512);
        mm16(pacc[0][0], cat8(pl[0], ph[0]), px[0]);
        mm16(pacc[0][1], cat8(pl[0], ph[0]), px[1]);
        SBAR();
        mm16(pacc[1][0], cat8(pl[1], ph[1]), px[0]);
        mm16(pacc[1][1], cat8(pl[1], ph[1]), px[1]);
        SBAR();
        pl[3] = ldstr(pw1[1] + 4096); ph[3] = ldstr(pw1[1] + 4096 + 512);
        mm16(pacc[0][2], cat8(pl[0], ph[0]), px[2]);
        mm16(pacc[0][3], cat8(pl[0], ph[0]), px[3]);
        SBAR();
        mm16(pacc[1][2], cat8(pl[1], ph[1]), px[2]);
        mm16(pacc[1][3], cat8(pl[1], ph[1]), px[3]);
        SBAR();
        pl[0] = ldstr(pw2[1] + 0); ph[0] = ldstr(pw2[1] + 0 + 512);
        mm16(pacc[2][0], cat8(pl[2], ph[2]), px[0]);
        mm16(pacc[2][1], cat8(pl[2], ph[2]), px[1]);
        SBAR();
        mm16(pacc[2][2], cat8(pl[2], ph[2]), px[2]);
        mm16(pacc[2][3], cat8(pl[2], ph[2]), px[3]);
        SBAR();
        pl[1] = ldstr(pw2[1] + 4096); ph[1] = ldstr(pw2[1] + 4096 + 512);
        mm16(pacc[3][0], cat8(pl[3], ph[3]), px[0]);
        mm16(pacc[3][1], cat8(pl[3], ph[3]), px[1]);
        SBAR();
        mm16(pacc[3][2], cat8(pl[3], ph[3]), px[2]);
        mm16(pacc[3][3], cat8(pl[3], ph[3]), px[3]);
        SBAR();
        mm16(pacc[4][0], cat8(pl[0], ph[0]), px[0]);
        mm16(pacc[4][1], cat8(pl[0], ph[0]), px[1]);
        SBAR();
        wait_vm_bar<63>();
        pl[2] = ldstr(pw0[1] + 32768); ph[2] = ldstr(pw0[1] + 32768 + 512);
        mm16(pacc[5][0], cat8(pl[1], ph[1]), px[0]);
        mm16(pacc[5][1], cat8(pl[1], ph[1]), px[1]);
        kad += 16384u; ksrd[0] = (unsigned)kad; ksrd[1] = (unsigned)(kad >> 32);
        SBAR();
        px[0] = lds128(pxr[1] + 32768); px[1] = lds128(pxr[1] + 33024); pl[3] = ldstr(pw0[1] + 36864); ph[3] = ldstr(pw0[1] + 36864 + 512);
        mm16(pacc[4][2], cat8(pl[0], ph[0]), px[2]);
        mm16(pacc[4][3], cat8(pl[0], ph[0]), px[3]);
        SBAR();
        mm16(pacc[5][2], cat8(pl[1], ph[1]), px[2]);
        mm16(pacc[5][3], cat8(pl[1], ph[1]), px[3]);
        SBAR();
        px[2] = lds128(pxr[1] + 40960); px[3] = lds128(pxr[1] + 41216);
        pl[0] = ldstr(pw1[1] + 32768); ph[0] = ldstr(pw1[1] + 32768 + 512);
        mm16(pacc[0][0], cat8(pl[2], ph[2]), px[0]);
        mm16(pacc[0][1], cat8(pl[2], ph[2]), px[1]);
        SBAR();
        mm16(pacc[1][0], cat8(pl[3], ph[3]), px[0]);
        mm16(pacc[1][1], cat8(pl[3], ph[3]), px[1]);
        SBAR();
        pl[1] = ldstr(pw1[1] + 36864); ph[1] = ldstr(pw1[1] + 36864 + 512);
        mm16(pacc[0][2], cat8(pl[2], ph[2]), px[2]);
        mm16(pacc[0][3], cat8(pl[2], ph[2]), px[3]);
        SBAR();
        mm16(pacc[1][2], cat8(pl[3], ph[3]), px[2]);
        mm16(pacc[1][3], cat8(pl[3], ph[3]), px[3]);
        SBAR();
        pl[2] = ldstr(pw2[1] + 32768); ph[2] = ldstr(pw2[1] + 32768 + 512);
        mm16(pacc[2][0], cat8(pl[0], ph[0]), px[0]);
        mm16(pacc[2][1], cat8(pl[0], ph[0]), px[1]);
        SBAR();
        mm16(pacc[2][2], cat8(pl[0], ph[0]), px[2]);
        mm16(pacc[2][3], cat8(pl[0], ph[0]), px[3]);
        SBAR();
        pl[3] = ldstr(pw2[1] + 36864); ph[3] = ldstr(pw2[1] + 36864 + 512);
        mm16(pacc[3][0], cat8(pl[1], ph[1]), px[0]);
        mm16(pacc[3][1], cat8(pl[1], ph[1]), px[1]);
        SBAR();
        mm16(pacc[3][2], cat8(pl[1], ph[1]), px[2]);
        mm16(pacc[3][3], cat8(pl[1], ph[1]), px[3]);
        SBAR();
        mm16(pacc[4][0], cat8(pl[2], ph[2]), px[0]);
        mm16(pacc[4][1], cat8(pl[2], ph[2]), px[1]);
        SBAR();
        mm16(pacc[5][0], cat8(pl[3], ph[3]), px[0]);
        mm16(pacc[5][1], cat8(pl[3], ph[3]), px[1]);
        SBAR();
        mm16(pacc[4][2], cat8(pl[2], ph[2]), px[2]);
        mm16(pacc[4][3], cat8(pl[2], ph[2]), px[3]);
        SBAR();
        mm16(pacc[5][2], cat8(pl[3], ph[3]), px[2]);
        mm16(pacc[5][3], cat8(pl[3], ph[3]), px[3]);
        SBAR();
        asm volatile("s_nop 7" ::: "memory"); SBAR();
        } else {
        asm volatile("" : "+s"(kst), "+s"(vst));
        pl[0] = ldstr(pw0[0] + 0); ph[0] = ldstr(pw0[0] + 0 + 512);
        px[0] = lds128(pxr[0] + 0);
        px[1] = lds128(pxr[0] + 256);
        pl[1] = ldstr(pw0[0] + 4096); ph[1] = ldstr(pw0[0] + 4096 + 512);
        px[2] = lds128(pxr[0] + 8192);
        px[3] = lds128(pxr[0] + 8448);
        SBAR();
        pl[2] = ldstr(pw1[0] + 0); ph[2] = ldstr(pw1[0] + 0 + 512);
        mm16z(pacc[0][0], cat8(pl[0], ph[0]), px[0]);
        mm16z(pacc[0][1], cat8(pl[0], ph[0]), px[1]);
        st16kv(wsr, vvo, vst + 0, held[0]);
        SBAR();
        mm16z(pacc[1][0], cat8(pl[1], ph[1]), px[0]);
        mm16z(pacc[1][1], cat8(pl[1], ph[1]), px[1]);
        SBAR();
        pl[3] = ldstr(pw1[0] + 4096); ph[3] = ldstr(pw1[0] + 4096 + 512);
        mm16z(pacc[0][2], cat8(pl[0], ph[0]), px[2]);
        mm16z(pacc[0][3], cat8(pl[0], ph[0]), px[3]);
        st16kv(wsr, vvo, vst + 1024, held[1]);
        SBAR();
        mm16z(pacc[1][2], cat8(pl[1], ph[1]), px[2]);
        mm16z(pacc[1][3], cat8(pl[1], ph[1]), px[3]);
        SBAR();
        pl[0] = ldstr(pw2[0] + 0); ph[0] = ldstr(pw2[0] + 0 + 512);
        mm16z(pacc[2][0], cat8(pl[2], ph[2]), px[0]);
        mm16z(pacc[2][1], cat8(pl[2], ph[2]), px[1]);
        SBAR();
        mm16z(pacc[2][2], cat8(pl[2], ph[2]), px[2]);
        mm16z(pacc[2][3], cat8(pl[2], ph[2]), px[3]);
        SBAR();
        pl[1] = ldstr(pw2[0] + 4096); ph[1] = ldstr(pw2[0] + 4096 + 512);
        mm16z(pacc[3][0], cat8(pl[3], ph[3]), px[0]);
        mm16z(pacc[3][1], cat8(pl[3], ph[3]), px[1]);
        SBAR();
        mm16z(pacc[3][2], cat8(pl[3], ph[3]), px[2]);
        mm16z(pacc[3][3], cat8(pl[3], ph[3]), px[3]);
        SBAR();
        mm16z(pacc[4][0], cat8(pl[0], ph[0]), px[0]);
        mm16z(pacc[4][1], cat8(pl[0], ph[0]), px[1]);
        SBAR();
        wait_vm_bar<63>();
        pl[2] = ldstr(pw0[0] + 32768); ph[2] = ldstr(pw0[0] + 32768 + 512);
        mm16z(pacc[5][0], cat8(pl[1], ph[1]), px[0]);
        mm16z(pacc[5][1], cat8(pl[1], ph[1]), px[1]);
        hb[0] = *(const LAS u32x4*)(hand + 0);
        hb[1] = *(const LAS u32x4*)(hand + 1024);
        SBAR();
        px[0] = lds128(pxr[0] + 32768); px[1] = lds128(pxr[0] + 33024); pl[3] = ldstr(pw0[0] + 36864); ph[3] = ldstr(pw0[0] + 36864 + 512);
        mm16z(pacc[4][2], cat8(pl[0], ph[0]), px[2]);
        mm16z(pacc[4][3], cat8(pl[0], ph[0]), px[3]);
        SBAR();
        mm16z(pacc[5][2], cat8(pl[1], ph[1]), px[2]);
        mm16z(pacc[5][3], cat8(pl[1], ph[1]), px[3]);
        st16kv(wsr, kvo, kst + 0, hb[0]);
        SBAR();
        px[2] = lds128(pxr[0] + 40960); px[3] = lds128(pxr[0] + 41216);
        pl[0] = ldstr(pw1[0] + 32768); ph[0] = ldstr(pw1[0] + 32768 + 512);
        mm16(pacc[0][0], cat8(pl[2], ph[2]), px[0]);
        mm16(pacc[0][1], cat8(pl[2], ph[2]), px[1]);
        SBAR();
        mm16(pacc[1][0], cat8(pl[3], ph[3]), px[0]);
        mm16(pacc[1][1], cat8(pl[3], ph[3]), px[1]);
        st16kv(wsr, kvo, kst + 2048, hb[1]);
        SBAR();
        pl[1] = ldstr(pw1[0] + 36864); ph[1] = ldstr(pw1[0] + 36864 + 512);
        mm16(pacc[0][2], cat8(pl[2], ph[2]), px[2]);
        mm16(pacc[0][3], cat8(pl[2], ph[2]), px[3]);
        SBAR();
        mm16(pacc[1][2], cat8(pl[3], ph[3]), px[2]);
        mm16(pacc[1][3], cat8(pl[3], ph[3]), px[3]);
        SBAR();
        pl[2] = ldstr(pw2[0] + 32768); ph[2] = ldstr(pw2[0] + 32768 + 512);
        mm16(pacc[2][0], cat8(pl[0], ph[0]), px[0]);
        mm16(pacc[2][1], cat8(pl[0], ph[0]), px[1]);
        SBAR();
        mm16(pacc[2][2], cat8(pl[0], ph[0]), px[2]);
        mm16(pacc[2][3], cat8(pl[0], ph[0]), px[3]);
        SBAR();
        pl[3] = ldstr(pw2[0] + 36864); ph[3] = ldstr(pw2[0] + 36864 + 512);
        mm16(pacc[3][0], cat8(pl[1], ph[1]), px[0]);
        mm16(pacc[3][1], cat8(pl[1], ph[1]), px[1]);
        SBAR();
        mm16(pacc[3][2], cat8(pl[1], ph[1]), px[2]);
        mm16(pacc[3][3], cat8(pl[1], ph[1]), px[3]);
        SBAR();
        mm16(pacc[4][0], cat8(pl[2], ph[2]), px[0]);
        mm16(pacc[4][1], cat8(pl[2], ph[2]), px[1]);
        SBAR();
        wait_vm_bar<63>();
        pl[0] = ldstr(pw0[1] + 0); ph[0] = ldstr(pw0[1] + 0 + 512);
        mm16(pacc[5][0], cat8(pl[3], ph[3]), px[0]);
        mm16(pacc[5][1], cat8(pl[3], ph[3]), px[1]);
        hb[2] = *(const LAS u32x4*)(hand + 2048);
        hb[3] = *(const LAS u32x4*)(hand + 3072);
        SBAR();
        px[0] = lds128(pxr[1] + 0); px[1] = lds128(pxr[1] + 256); pl[1] = ldstr(pw0[1] + 4096); ph[1] = ldstr(pw0[1] + 4096 + 512);
        mm16(pacc[4][2], cat8(pl[2], ph[2]), px[2]);
        mm16(pacc[4][3], cat8(pl[2], ph[2]), px[3]);
        SBAR();
        mm16(pacc[5][2], cat8(pl[3], ph[3]), px[2]);
        mm16(pacc[5][3], cat8(pl[3], ph[3]), px[3]);
        st16kv(wsr, kvo, kst + 256, hb[2]);
        SBAR();
        px[2] = lds128(pxr[1] + 8192); px[3] = lds128(pxr[1] + 8448);
        pl[2] = ldstr(pw1[1] + 0); ph[2] = ldstr(pw1[1] + 0 + 512);
        mm16(pacc[0][0], cat8(pl[0], ph[0]), px[0]);
        mm16(pacc[0][1], cat8(pl[0], ph[0]), px[1]);
        SBAR();
        mm16(pacc[1][0], cat8(pl[1], ph[1]), px[0]);
        mm16(pacc[1][1], cat8(pl[1], ph[1]), px[1]);
        st16kv(wsr, kvo, kst + 2304, hb[3]);
        SBAR();
        pl[3] = ldstr(pw1[1] + 4096); ph[3] = ldstr(pw1[1] + 4096 + 512);
        mm16(pacc[0][2], cat8(pl[0], ph[0]), px[2]);
        mm16(pacc[0][3], cat8(pl[0], ph[0]), px[3]);
        SBAR();
        mm16(pacc[1][2], cat8(pl[1], ph[1]), px[2]);
        mm16(pacc[1][3], cat8(pl[1], ph[1]), px[3]);
        SBAR();
        pl[0] = ldstr(pw2[1] + 0); ph[0] = ldstr(pw2[1] + 0 + 512);
        mm16(pacc[2][0], cat8(pl[2], ph[2]), px[0]);
        mm16(pacc[2][1], cat8(pl[2], ph[2]), px[1]);
        SBAR();
        mm16(pacc[2][2], cat8(pl[2], ph[2]), px[2]);
        mm16(pacc[2][3], cat8(pl[2], ph[2]), px[3]);
        SBAR();
        pl[1] = ldstr(pw2[1] + 4096); ph[1] = ldstr(pw2[1] + 4096 + 512);
        mm16(pacc[3][0], cat8(pl[3], ph[3]), px[0]);
        mm16(pacc[3][1], cat8(pl[3], ph[3]), px[1]);
        SBAR();
        mm16(pacc[3][2], cat8(pl[3], ph[3]), px[2]);
        mm16(pacc[3][3], cat8(pl[3], ph[3]), px[3]);
        SBAR();
        mm16(pacc[4][0], cat8(pl[0], ph[0]), px[0]);
        mm16(pacc[4][1], cat8(pl[0], ph[0]), px[1]);
        SBAR();
        wait_vm_bar<63>();
        pl[2] = ldstr(pw0[1] + 32768); ph[2] = ldstr(pw0[1] + 32768 + 512);
        mm16(pacc[5][0], cat8(pl[1], ph[1]), px[0]);
        mm16(pacc[5][1], cat8(pl[1], ph[1]), px[1]);
        hb[0] = *(const LAS u32x4*)(hand + 4096);
        hb[1] = *(const LAS u32x4*)(hand + 5120);
        SBAR();
        px[0] = lds128(pxr[1] + 32768); px[1] = lds128(pxr[1] + 33024); pl[3] = ldstr(pw0[1] + 36864); ph[3] = ldstr(pw0[1] + 36864 + 512);
        mm16(pacc[4][2], cat8(pl[0], ph[0]), px[2]);
        mm16(pacc[4][3], cat8(pl[0], ph[0]), px[3]);
        SBAR();
        mm16(pacc[5][2], cat8(pl[1], ph[1]), px[2]);
        mm16(pacc[5][3], cat8(pl[1], ph[1]), px[3]);
        st16kv(wsr, kvo, kst + 8192, hb[0]);
        SBAR();
        px[2] = lds128(pxr[1] + 40960); px[3] = lds128(pxr[1] + 41216);
        pl[0] = ldstr(pw1[1] + 32768); ph[0] = ldstr(pw1[1] + 32768 + 512);
        mm16(pacc[0][0], cat8(pl[2], ph[2]), px[0]);
        mm16(pacc[0][1], cat8(pl[2], ph[2]), px[1]);
        SBAR();
        mm16(pacc[1][0], cat8(pl[3], ph[3]), px[0]);
        mm16(pacc[1][1], cat8(pl[3], ph[3]), px[1]);
        st16kv(wsr, kvo, kst + 10240, hb[1]);
        SBAR();
        pl[1] = ldstr(pw1[1] + 36864); ph[1] = ldstr(pw1[1] + 36864 + 512);
        mm16(pacc[0][2], cat8(pl[2], ph[2]), px[2]);
        mm16(pacc[0][3], cat8(pl[2], ph[2]), px[3]);
        SBAR();
        mm16(pacc[1][2], cat8(pl[3], ph[3]), px[2]);
        mm16(pacc[1][3], cat8(pl[3], ph[3]), px[3]);
        SBAR();
        pl[2] = ldstr(pw2[1] + 32768); ph[2] = ldstr(pw2[1] + 32768 + 512);
        mm16(pacc[2][0], cat8(pl[0], ph[0]), px[0]);
        mm16(pacc[2][1], cat8(pl[0], ph[0]), px[1]);
        SBAR();
        mm16(pacc[2][2], cat8(pl[0], ph[0]), px[2]);
        mm16(pacc[2][3], cat8(pl[0], ph[0]), px[3]);
        SBAR();
        pl[3] = ldstr(pw2[1] + 36864); ph[3] = ldstr(pw2[1] + 36864 + 512);
        mm16(pacc[3][0], cat8(pl[1], ph[1]), px[0]);
        mm16(pacc[3][1], cat8(pl[1], ph[1]), px[1]);
        SBAR();
        mm16(pacc[3][2], cat8(pl[1], ph[1]), px[2]);
        mm16(pacc[3][3], cat8(pl[1], ph[1]), px[3]);
        SBAR();
        mm16(pacc[4][0], cat8(pl[2], ph[2]), px[0]);
        mm16(pacc[4][1], cat8(pl[2], ph[2]), px[1]);
        SBAR();
        wait_vm_bar<63>();
        pl[0] = ldstr(pw0[0] + 0); ph[0] = ldstr(pw0[0] + 0 + 512);
        mm16(pacc[5][0], cat8(pl[3], ph[3]), px[0]);
        mm16(pacc[5][1], cat8(pl[3], ph[3]), px[1]);
        hb[2] = *(const LAS u32x4*)(hand + 6144);
        hb[3] = *(const LAS u32x4*)(hand + 7168);
        SBAR();
        px[0] = lds128(pxr[0] + 0); px[1] = lds128(pxr[0] + 256); pl[1] = ldstr(pw0[0] + 4096); ph[1] = ldstr(pw0[0] + 4096 + 512);
        mm16(pacc[4][2], cat8(pl[2], ph[2]), px[2]);
        mm16(pacc[4][3], cat8(pl[2], ph[2]), px[3]);
        SBAR();
        mm16(pacc[5][2], cat8(pl[3], ph[3]), px[2]);
        mm16(pacc[5][3], cat8(pl[3], ph[3]), px[3]);
        st16kv(wsr, kvo, kst + 8448, hb[2]);
        SBAR();
        px[2] = lds128(pxr[0] + 8192); px[3] = lds128(pxr[0] + 8448);
        pl[2] = ldstr(pw1[0] + 0); ph[2] = ldstr(pw1[0] + 0 + 512);
        mm16(pacc[0][0], cat8(pl[0], ph[0]), px[0]);
        mm16(pacc[0][1], cat8(pl[0], ph[0]), px[1]);
        SBAR();
        mm16(pacc[1][0], cat8(pl[1], ph[1]), px[0]);
        mm16(pacc[1][1], cat8(pl[1], ph[1]), px[1]);
        st16kv(wsr, kvo, kst + 10496, hb[3]);
        SBAR();
        pl[3] = ldstr(pw1[0] + 4096); ph[3] = ldstr(pw1[0] + 4096 + 512);
        mm16(pacc[0][2], cat8(pl[0], ph[0]), px[2]);
        mm16(pacc[0][3], cat8(pl[0], ph[0]), px[3]);
        SBAR();
        mm16(pacc[1][2], cat8(pl[1], ph[1]), px[2]);
        mm16(pacc[1][3], cat8(pl[1], ph[1]), px[3]);
        SBAR();
        pl[0] = ldstr(pw2[0] + 0); ph[0] = ldstr(pw2[0] + 0 + 512);
        mm16(pacc[2][0], cat8(pl[2], ph[2]), px[0]);
        mm16(pacc[2][1], cat8(pl[2], ph[2]), px[1]);
        SBAR();
        mm16(pacc[2][2], cat8(pl[2], ph[2]), px[2]);
        mm16(pacc[2][3], cat8(pl[2], ph[2]), px[3]);
        SBAR();
        pl[1] = ldstr(pw2[0] + 4096); ph[1] = ldstr(pw2[0] + 4096 + 512);
        mm16(pacc[3][0], cat8(pl[3], ph[3]), px[0]);
        mm16(pacc[3][1], cat8(pl[3], ph[3]), px[1]);
        SBAR();
        mm16(pacc[3][2], cat8(pl[3], ph[3]), px[2]);
        mm16(pacc[3][3], cat8(pl[3], ph[3]), px[3]);
        SBAR();
        mm16(pacc[4][0], cat8(pl[0], ph[0]), px[0]);
        mm16(pacc[4][1], cat8(pl[0], ph[0]), px[1]);
        SBAR();
        wait_vm_bar<63>();
        pl[2] = ldstr(pw0[0] + 32768); ph[2] = ldstr(pw0[0] + 32768 + 512);
        mm16(pacc[5][0], cat8(pl[1], ph[1]), px[0]);
        mm16(pacc[5][1], cat8(pl[1], ph[1]), px[1]);
        warmv(wjunk, lane * 128, wp2, j == 0 ? 8192u : 0u);
        SBAR();
        px[0] = lds128(pxr[0] + 32768); px[1] = lds128(pxr[0] + 33024); pl[3] = ldstr(pw0[0] + 36864); ph[3] = ldstr(pw0[0] + 36864 + 512);
        mm16(pacc[4][2], cat8(pl[0], ph[0]), px[2]);
        mm16(pacc[4][3], cat8(pl[0], ph[0]), px[3]);
        st16kv(wsr, vvo, vst + 4096, held[2]);
        SBAR();
        mm16(pacc[5][2], cat8(pl[1], ph[1]), px[2]);
        mm16(pacc[5][3], cat8(pl[1], ph[1]), px[3]);
        SBAR();
        px[2] = lds128(pxr[0] + 40960); px[3] = lds128(pxr[0] + 41216);
        pl[0] = ldstr(pw1[0] + 32768); ph[0] = ldstr(pw1[0] + 32768 + 512);
        mm16(pacc[0][0], cat8(pl[2], ph[2]), px[0]);
        mm16(pacc[0][1], cat8(pl[2], ph[2]), px[1]);
        st16kv(wsr, vvo, vst + 5120, held[3]);
        SBAR();
        mm16(pacc[1][0], cat8(pl[3], ph[3]), px[0]);
        mm16(pacc[1][1], cat8(pl[3], ph[3]), px[1]);
        SBAR();
        pl[1] = ldstr(pw1[0] + 36864); ph[1] = ldstr(pw1[0] + 36864 + 512);
        mm16(pacc[0][2], cat8(pl[2], ph[2]), px[2]);
        mm16(pacc[0][3], cat8(pl[2], ph[2]), px[3]);
        SBAR();
        mm16(pacc[1][2], cat8(pl[3], ph[3]), px[2]);
        mm16(pacc[1][3], cat8(pl[3], ph[3]), px[3]);
        SBAR();
        pl[2] = ldstr(pw2[0] + 32768); ph[2] = ldstr(pw2[0] + 32768 + 512);
        mm16(pacc[2][0], cat8(pl[0], ph[0]), px[0]);
        mm16(pacc[2][1], cat8(pl[0], ph[0]), px[1]);
        SBAR();
        mm16(pacc[2][2], cat8(pl[0], ph[0]), px[2]);
        mm16(pacc[2][3], cat8(pl[0], ph[0]), px[3]);
        SBAR();
        pl[3] = ldstr(pw2[0] + 36864); ph[3] = ldstr(pw2[0] + 36864 + 512);
        mm16(pacc[3][0], cat8(pl[1], ph[1]), px[0]);
        mm16(pacc[3][1], cat8(pl[1], ph[1]), px[1]);
        SBAR();
        mm16(pacc[3][2], cat8(pl[1], ph[1]), px[2]);
        mm16(pacc[3][3], cat8(pl[1], ph[1]), px[3]);
        SBAR();
        mm16(pacc[4][0], cat8(pl[2], ph[2]), px[0]);
        mm16(pacc[4][1], cat8(pl[2], ph[2]), px[1]);
        SBAR();
        wait_vm_bar<63>();
        pl[0] = ldstr(pw0[1] + 0); ph[0] = ldstr(pw0[1] + 0 + 512);
        mm16(pacc[5][0], cat8(pl[3], ph[3]), px[0]);
        mm16(pacc[5][1], cat8(pl[3], ph[3]), px[1]);
        warmv(wjunk, lane * 128, wp2, j == 1 ? 8192u : 0u);
        SBAR();
        px[0] = lds128(pxr[1] + 0); px[1] = lds128(pxr[1] + 256); pl[1] = ldstr(pw0[1] + 4096); ph[1] = ldstr(pw0[1] + 4096 + 512);
        mm16(pacc[4][2], cat8(pl[2], ph[2]), px[2]);
        mm16(pacc[4][3], cat8(pl[2], ph[2]), px[3]);
        hb[0] = *(const LAS u32x4*)(hand + 0);
        hb[1] = *(const LAS u32x4*)(hand + 1024);
        SBAR();
        mm16(pacc[5][2], cat8(pl[3], ph[3]), px[2]);
        mm16(pacc[5][3], cat8(pl[3], ph[3]), px[3]);
        SBAR();
        px[2] = lds128(pxr[1] + 8192); px[3] = lds128(pxr[1] + 8448);
        pl[2] = ldstr(pw1[1] + 0); ph[2] = ldstr(pw1[1] + 0 + 512);
        mm16(pacc[0][0], cat8(pl[0], ph[0]), px[0]);
        mm16(pacc[0][1], cat8(pl[0], ph[0]), px[1]);
        st16kv(wsr, kvo, kst + 16384, hb[0]);
        SBAR();
        mm16(pacc[1][0], cat8(pl[1], ph[1]), px[0]);
        mm16(pacc[1][1], cat8(pl[1], ph[1]), px[1]);
        SBAR();
        pl[3] = ldstr(pw1[1] + 4096); ph[3] = ldstr(pw1[1] + 4096 + 512);
        mm16(pacc[0][2], cat8(pl[0], ph[0]), px[2]);
        mm16(pacc[0][3], cat8(pl[0], ph[0]), px[3]);
        st16kv(wsr, kvo, kst + 18432, hb[1]);
        SBAR();
        mm16(pacc[1][2], cat8(pl[1], ph[1]), px[2]);
        mm16(pacc[1][3], cat8(pl[1], ph[1]), px[3]);
        SBAR();
        pl[0] = ldstr(pw2[1] + 0); ph[0] = ldstr(pw2[1] + 0 + 512);
        mm16(pacc[2][0], cat8(pl[2], ph[2]), px[0]);
        mm16(pacc[2][1], cat8(pl[2], ph[2]), px[1]);
        SBAR();
        mm16(pacc[2][2], cat8(pl[2], ph[2]), px[2]);
        mm16(pacc[2][3], cat8(pl[2], ph[2]), px[3]);
        SBAR();
        pl[1] = ldstr(pw2[1] + 4096); ph[1] = ldstr(pw2[1] + 4096 + 512);
        mm16(pacc[3][0], cat8(pl[3], ph[3]), px[0]);
        mm16(pacc[3][1], cat8(pl[3], ph[3]), px[1]);
        SBAR();
        mm16(pacc[3][2], cat8(pl[3], ph[3]), px[2]);
        mm16(pacc[3][3], cat8(pl[3], ph[3]), px[3]);
        SBAR();
        mm16(pacc[4][0], cat8(pl[0], ph[0]), px[0]);
        mm16(pacc[4][1], cat8(pl[0], ph[0]), px[1]);
        SBAR();
        wait_vm_bar<63>();
        pl[2] = ldstr(pw0[1] + 32768); ph[2] = ldstr(pw0[1] + 32768 + 512);
        mm16(pacc[5][0], cat8(pl[1], ph[1]), px[0]);
        mm16(pacc[5][1], cat8(pl[1], ph[1]), px[1]);
        warmv(wjunk, lane * 128, wp2, j == 2 ? 8192u : 0u);
        SBAR();
        px[0] = lds128(pxr[1] + 32768); px[1] = lds128(pxr[1] + 33024); pl[3] = ldstr(pw0[1] + 36864); ph[3] = ldstr(pw0[1] + 36864 + 512);
        mm16(pacc[4][2], cat8(pl[0], ph[0]), px[2]);
        mm16(pacc[4][3], cat8(pl[0], ph[0]), px[3]);
        hb[2] = *(const LAS u32x4*)(hand + 2048);
        hb[3] = *(const LAS u32x4*)(hand + 3072);
        SBAR();
        mm16(pacc[5][2], cat8(pl[1], ph[1]), px[2]);
        mm16(pacc[5][3], cat8(pl[1], ph[1]), px[3]);
        SBAR();
        px[2] = lds128(pxr[1] + 40960); px[3] = lds128(pxr[1] + 41216);
        pl[0] = ldstr(pw1[1] + 32768); ph[0] = ldstr(pw1[1] + 32768 + 512);
        mm16(pacc[0][0], cat8(pl[2], ph[2]), px[0]);
        mm16(pacc[0][1], cat8(pl[2], ph[2]), px[1]);
        st16kv(wsr, kvo, kst + 16640, hb[2]);
        SBAR();
        mm16(pacc[1][0], cat8(pl[3], ph[3]), px[0]);
        mm16(pacc[1][1], cat8(pl[3], ph[3]), px[1]);
        SBAR();
        pl[1] = ldstr(pw1[1] + 36864); ph[1] = ldstr(pw1[1] + 36864 + 512);
        mm16(pacc[0][2], cat8(pl[2], ph[2]), px[2]);
        mm16(pacc[0][3], cat8(pl[2], ph[2]), px[3]);
        st16kv(wsr, kvo, kst + 18688, hb[3]);
        SBAR();
        mm16(pacc[1][2], cat8(pl[3], ph[3]), px[2]);
        mm16(pacc[1][3], cat8(pl[3], ph[3]), px[3]);
        SBAR();
        pl[2] = ldstr(pw2[1] + 32768); ph[2] = ldstr(pw2[1] + 32768 + 512);
        mm16(pacc[2][0], cat8(pl[0], ph[0]), px[0]);
        mm16(pacc[2][1], cat8(pl[0], ph[0]), px[1]);
        SBAR();
        mm16(pacc[2][2], cat8(pl[0], ph[0]), px[2]);
        mm16(pacc[2][3], cat8(pl[0], ph[0]), px[3]);
        SBAR();
        pl[3] = ldstr(pw2[1] + 36864); ph[3] = ldstr(pw2[1] + 36864 + 512);
        mm16(pacc[3][0], cat8(pl[1], ph[1]), px[0]);
        mm16(pacc[3][1], cat8(pl[1], ph[1]), px[1]);
        SBAR();
        mm16(pacc[3][2], cat8(pl[1], ph[1]), px[2]);
        mm16(pacc[3][3], cat8(pl[1], ph[1]), px[3]);
        SBAR();
        mm16(pacc[4][0], cat8(pl[2], ph[2]), px[0]);
        mm16(pacc[4][1], cat8(pl[2], ph[2]), px[1]);
        SBAR();
        wait_vm_bar<63>();
        pl[0] = ldstr(pw0[0] + 0); ph[0] = ldstr(pw0[0] + 0 + 512);
        mm16(pacc[5][0], cat8(pl[3], ph[3]), px[0]);
        mm16(pacc[5][1], cat8(pl[3], ph[3]), px[1]);
        warmv(wjunk, lane * 128, wp2, j == 3 ? 8192u : 0u);
        SBAR();
        px[0] = lds128(pxr[0] + 0); px[1] = lds128(pxr[0] + 256); pl[1] = ldstr(pw0[0] + 4096); ph[1] = ldstr(pw0[0] + 4096 + 512);
        mm16(pacc[4][2], cat8(pl[2], ph[2]), px[2]);
        mm16(pacc[4][3], cat8(pl[2], ph[2]), px[3]);
        hb[0] = *(const LAS u32x4*)(hand + 4096);
        hb[1] = *(const LAS u32x4*)(hand + 5120);
        SBAR();
        mm16(pacc[5][2], cat8(pl[3], ph[3]), px[2]);
        mm16(pacc[5][3], cat8(pl[3], ph[3]), px[3]);
        SBAR();
        px[2] = lds128(pxr[0] + 8192); px[3] = lds128(pxr[0] + 8448);
        pl[2] = ldstr(pw1[0] + 0); ph[2] = ldstr(pw1[0] + 0 + 512);
        mm16(pacc[0][0], cat8(pl[0], ph[0]), px[0]);
        mm16(pacc[0][1], cat8(pl[0], ph[0]), px[1]);
        st16kv(wsr, kvo, kst + 24576, hb[0]);
        SBAR();
        mm16(pacc[1][0], cat8(pl[1], ph[1]), px[0]);
        mm16(pacc[1][1], cat8(pl[1], ph[1]), px[1]);
        SBAR();
        pl[3] = ldstr(pw1[0] + 4096); ph[3] = ldstr(pw1[0] + 4096 + 512);
        mm16(pacc[0][2], cat8(pl[0], ph[0]), px[2]);
        mm16(pacc[0][3], cat8(pl[0], ph[0]), px[3]);
        st16kv(wsr, kvo, kst + 26624, hb[1]);
        SBAR();
        mm16(pacc[1][2], cat8(pl[1], ph[1]), px[2]);
        mm16(pacc[1][3], cat8(pl[1], ph[1]), px[3]);
        SBAR();
        pl[0] = ldstr(pw2[0] + 0); ph[0] = ldstr(pw2[0] + 0 + 512);
        mm16(pacc[2][0], cat8(pl[2], ph[2]), px[0]);
        mm16(pacc[2][1], cat8(pl[2], ph[2]), px[1]);
        SBAR();
        mm16(pacc[2][2], cat8(pl[2], ph[2]), px[2]);
        mm16(pacc[2][3], cat8(pl[2], ph[2]), px[3]);
        SBAR();
        pl[1] = ldstr(pw2[0] + 4096); ph[1] = ldstr(pw2[0] + 4096 + 512);
        mm16(pacc[3][0], cat8(pl[3], ph[3]), px[0]);
        mm16(pacc[3][1], cat8(pl[3], ph[3]), px[1]);
        SBAR();
        mm16(pacc[3][2], cat8(pl[3], ph[3]), px[2]);
        mm16(pacc[3][3], cat8(pl[3], ph[3]), px[3]);
        SBAR();
        mm16(pacc[4][0], cat8(pl[0], ph[0]), px[0]);
        mm16(pacc[4][1], cat8(pl[0], ph[0]), px[1]);
        SBAR();
        wait_vm_bar<63>();
        pl[2] = ldstr(pw0[0] + 32768); ph[2] = ldstr(pw0[0] + 32768 + 512);
        mm16(pacc[5][0], cat8(pl[1], ph[1]), px[0]);
        mm16(pacc[5][1], cat8(pl[1], ph[1]), px[1]);
        warmv(wjunk, lane * 128, wp2, j == 4 ? 8192u : 0u);
        SBAR();
        px[0] = lds128(pxr[0] + 32768); px[1] = lds128(pxr[0] + 33024); pl[3] = ldstr(pw0[0] + 36864); ph[3] = ldstr(pw0[0] + 36864 + 512);
        mm16(pacc[4][2], cat8(pl[0], ph[0]), px[2]);
        mm16(pacc[4][3], cat8(pl[0], ph[0]), px[3]);
        hb[2] = *(const LAS u32x4*)(hand + 6144);
        hb[3] = *(const LAS u32x4*)(hand + 7168);
        SBAR();
        mm16(pacc[5][2], cat8(pl[1], ph[1]), px[2]);
        mm16(pacc[5][3], cat8(pl[1], ph[1]), px[3]);
        SBAR();
        px[2] = lds128(pxr[0] + 40960); px[3] = lds128(pxr[0] + 41216);
        pl[0] = ldstr(pw1[0] + 32768); ph[0] = ldstr(pw1[0] + 32768 + 512);
        mm16(pacc[0][0], cat8(pl[2], ph[2]), px[0]);
        mm16(pacc[0][1], cat8(pl[2], ph[2]), px[1]);
        st16kv(wsr, kvo, kst + 24832, hb[2]);
        SBAR();
        mm16(pacc[1][0], cat8(pl[3], ph[3]), px[0]);
        mm16(pacc[1][1], cat8(pl[3], ph[3]), px[1]);
        SBAR();
        pl[1] = ldstr(pw1[0] + 36864); ph[1] = ldstr(pw1[0] + 36864 + 512);
        mm16(pacc[0][2], cat8(pl[2], ph[2]), px[2]);
        mm16(pacc[0][3], cat8(pl[2], ph[2]), px[3]);
        st16kv(wsr, kvo, kst + 26880, hb[3]);
        SBAR();
        mm16(pacc[1][2], cat8(pl[3], ph[3]), px[2]);
        mm16(pacc[1][3], cat8(pl[3], ph[3]), px[3]);
        SBAR();
        pl[2] = ldstr(pw2[0] + 32768); ph[2] = ldstr(pw2[0] + 32768 + 512);
        mm16(pacc[2][0], cat8(pl[0], ph[0]), px[0]);
        mm16(pacc[2][1], cat8(pl[0], ph[0]), px[1]);
        SBAR();
        mm16(pacc[2][2], cat8(pl[0], ph[0]), px[2]);
        mm16(pacc[2][3], cat8(pl[0], ph[0]), px[3]);
        SBAR();
        pl[3] = ldstr(pw2[0] + 36864); ph[3] = ldstr(pw2[0] + 36864 + 512);
        mm16(pacc[3][0], cat8(pl[1], ph[1]), px[0]);
        mm16(pacc[3][1], cat8(pl[1], ph[1]), px[1]);
        SBAR();
        mm16(pacc[3][2], cat8(pl[1], ph[1]), px[2]);
        mm16(pacc[3][3], cat8(pl[1], ph[1]), px[3]);
        SBAR();
        mm16(pacc[4][0], cat8(pl[2], ph[2]), px[0]);
        mm16(pacc[4][1], cat8(pl[2], ph[2]), px[1]);
        SBAR();
        wait_vm_bar<63>();
        pl[0] = ldstr(pw0[1] + 0); ph[0] = ldstr(pw0[1] + 0 + 512);
        mm16(pacc[5][0], cat8(pl[3], ph[3]), px[0]);
        mm16(pacc[5][1], cat8(pl[3], ph[3]), px[1]);
        warmv(wjunk, lane * 128, wp2, j == 5 ? 8192u : 0u);
        SBAR();
        px[0] = lds128(pxr[1] + 0); px[1] = lds128(pxr[1] + 256); pl[1] = ldstr(pw0[1] + 4096); ph[1] = ldstr(pw0[1] + 4096 + 512);
        mm16(pacc[4][2], cat8(pl[2], ph[2]), px[2]);
        mm16(pacc[4][3], cat8(pl[2], ph[2]), px[3]);
        st16kv(wsr, vvo, vst + 8192, held[4]);
        SBAR();
        mm16(pacc[5][2], cat8(pl[3], ph[3]), px[2]);
        mm16(pacc[5][3], cat8(pl[3], ph[3]), px[3]);
        SBAR();
        px[2] = lds128(pxr[1] + 8192); px[3] = lds128(pxr[1] + 8448);
        pl[2] = ldstr(pw1[1] + 0); ph[2] = ldstr(pw1[1] + 0 + 512);
        mm16(pacc[0][0], cat8(pl[0], ph[0]), px[0]);
        mm16(pacc[0][1], cat8(pl[0], ph[0]), px[1]);
        st16kv(wsr, vvo, vst + 9216, held[5]);
        SBAR();
        mm16(pacc[1][0], cat8(pl[1], ph[1]), px[0]);
        mm16(pacc[1][1], cat8(pl[1], ph[1]), px[1]);
        SBAR();
        pl[3] = ldstr(pw1[1] + 4096); ph[3] = ldstr(pw1[1] + 4096 + 512);
        mm16(pacc[0][2], cat8(pl[0], ph[0]), px[2]);
        mm16(pacc[0][3], cat8(pl[0], ph[0]), px[3]);
        SBAR();
        mm16(pacc[1][2], cat8(pl[1], ph[1]), px[2]);
        mm16(pacc[1][3], cat8(pl[1], ph[1]), px[3]);
        SBAR();
        pl[0] = ldstr(pw2[1] + 0); ph[0] = ldstr(pw2[1] + 0 + 512);
        mm16(pacc[2][0], cat8(pl[2], ph[2]), px[0]);
        mm16(pacc[2][1], cat8(pl[2], ph[2]), px[1]);
        SBAR();
        mm16(pacc[2][2], cat8(pl[2], ph[2]), px[2]);
        mm16(pacc[2][3], cat8(pl[2], ph[2]), px[3]);
        SBAR();
        pl[1] = ldstr(pw2[1] + 4096); ph[1] = ldstr(pw2[1] + 4096 + 512);
        mm16(pacc[3][0], cat8(pl[3], ph[3]), px[0]);
        mm16(pacc[3][1], cat8(pl[3], ph[3]), px[1]);
        SBAR();
        mm16(pacc[3][2], cat8(pl[3], ph[3]), px[2]);
        mm16(pacc[3][3], cat8(pl[3], ph[3]), px[3]);
        SBAR();
        mm16(pacc[4][0], cat8(pl[0], ph[0]), px[0]);
        mm16(pacc[4][1], cat8(pl[0], ph[0]), px[1]);
        SBAR();
        wait_vm_bar<63>();
        pl[2] = ldstr(pw0[1] + 32768); ph[2] = ldstr(pw0[1] + 32768 + 512);
        mm16(pacc[5][0], cat8(pl[1], ph[1]), px[0]);
        mm16(pacc[5][1], cat8(pl[1], ph[1]), px[1]);
        warmv(wjunk, lane * 128, wp2, j == 6 ? 8192u : 0u);
        SBAR();
        px[0] = lds128(pxr[1] + 32768); px[1] = lds128(pxr[1] + 33024); pl[3] = ldstr(pw0[1] + 36864); ph[3] = ldstr(pw0[1] + 36864 + 512);
        mm16(pacc[4][2], cat8(pl[0], ph[0]), px[2]);
        mm16(pacc[4][3], cat8(pl[0], ph[0]), px[3]);
        st16kv(wsr, vvo, vst + 12288, held[6]);
        SBAR();
        mm16(pacc[5][2], cat8(pl[1], ph[1]), px[2]);
        mm16(pacc[5][3], cat8(pl[1], ph[1]), px[3]);
        SBAR();
        px[2] = lds128(pxr[1] + 40960); px[3] = lds128(pxr[1] + 41216);
        pl[0] = ldstr(pw1[1] + 32768); ph[0] = ldstr(pw1[1] + 32768 + 512);
        mm16(pacc[0][0], cat8(pl[2], ph[2]), px[0]);
        mm16(pacc[0][1], cat8(pl[2], ph[2]), px[1]);
        st16kv(wsr, vvo, vst + 13312, held[7]);
        SBAR();
        mm16(pacc[1][0], cat8(pl[3], ph[3]), px[0]);
        mm16(pacc[1][1], cat8(pl[3], ph[3]), px[1]);
        SBAR();
        pl[1] = ldstr(pw1[1] + 36864); ph[1] = ldstr(pw1[1] + 36864 + 512);
        mm16(pacc[0][2], cat8(pl[2], ph[2]), px[2]);
        mm16(pacc[0][3], cat8(pl[2], ph[2]), px[3]);
        SBAR();
        mm16(pacc[1][2], cat8(pl[3], ph[3]), px[2]);
        mm16(pacc[1][3], cat8(pl[3], ph[3]), px[3]);
        SBAR();
        pl[2] = ldstr(pw2[1] + 32768); ph[2] = ldstr(pw2[1] + 32768 + 512);
        mm16(pacc[2][0], cat8(pl[0], ph[0]), px[0]);
        mm16(pacc[2][1], cat8(pl[0], ph[0]), px[1]);
        SBAR();
        mm16(pacc[2][2], cat8(pl[0], ph[0]), px[2]);
        mm16(pacc[2][3], cat8(pl[0], ph[0]), px[3]);
        SBAR();
        pl[3] = ldstr(pw2[1] + 36864); ph[3] = ldstr(pw2[1] + 36864 + 512);
        mm16(pacc[3][0], cat8(pl[1], ph[1]), px[0]);
        mm16(pacc[3][1], cat8(pl[1], ph[1]), px[1]);
        SBAR();
        mm16(pacc[3][2], cat8(pl[1], ph[1]), px[2]);
        mm16(pacc[3][3], cat8(pl[1], ph[1]), px[3]);
        SBAR();
        mm16(pacc[4][0], cat8(pl[2], ph[2]), px[0]);
        mm16(pacc[4][1], cat8(pl[2], ph[2]), px[1]);
        SBAR();
        wait_vm_bar<63>();
        pl[0] = ldstr(pw0[0] + 0); ph[0] = ldstr(pw0[0] + 0 + 512);
        mm16(pacc[5][0], cat8(pl[3], ph[3]), px[0]);
        mm16(pacc[5][1], cat8(pl[3], ph[3]), px[1]);
        warmv(wjunk, lane * 128, wp2, j == 7 ? 8192u : 0u);
        SBAR();
        px[0] = lds128(pxr[0] + 0); px[1] = lds128(pxr[0] + 256); pl[1] = ldstr(pw0[0] + 4096); ph[1] = ldstr(pw0[0] + 4096 + 512);
        mm16(pacc[4][2], cat8(pl[2], ph[2]), px[2]);
        mm16(pacc[4][3], cat8(pl[2], ph[2]), px[3]);
        st16kv(wsr, vvo, vst + 16384, held[8]);
        SBAR();
        mm16(pacc[5][2], cat8(pl[3], ph[3]), px[2]);
        mm16(pacc[5][3], cat8(pl[3], ph[3]), px[3]);
        SBAR();
        px[2] = lds128(pxr[0] + 8192); px[3] = lds128(pxr[0] + 8448);
        pl[2] = ldstr(pw1[0] + 0); ph[2] = ldstr(pw1[0] + 0 + 512);
        mm16(pacc[0][0], cat8(pl[0], ph[0]), px[0]);
        mm16(pacc[0][1], cat8(pl[0], ph[0]), px[1]);
        st16kv(wsr, vvo, vst + 17408, held[9]);
        SBAR();
        mm16(pacc[1][0], cat8(pl[1], ph[1]), px[0]);
        mm16(pacc[1][1], cat8(pl[1], ph[1]), px[1]);
        SBAR();
        pl[3] = ldstr(pw1[0] + 4096); ph[3] = ldstr(pw1[0] + 4096 + 512);
        mm16(pacc[0][2], cat8(pl[0], ph[0]), px[2]);
        mm16(pacc[0][3], cat8(pl[0], ph[0]), px[3]);
        SBAR();
        mm16(pacc[1][2], cat8(pl[1], ph[1]), px[2]);
        mm16(pacc[1][3], cat8(pl[1], ph[1]), px[3]);
        SBAR();
        pl[0] = ldstr(pw2[0] + 0); ph[0] = ldstr(pw2[0] + 0 + 512);
        mm16(pacc[2][0], cat8(pl[2], ph[2]), px[0]);
        mm16(pacc[2][1], cat8(pl[2], ph[2]), px[1]);
        SBAR();
        mm16(pacc[2][2], cat8(pl[2], ph[2]), px[2]);
        mm16(pacc[2][3], cat8(pl[2], ph[2]), px[3]);
        SBAR();
        pl[1] = ldstr(pw2[0] + 4096); ph[1] = ldstr(pw2[0] + 4096 + 512);
        mm16(pacc[3][0], cat8(pl[3], ph[3]), px[0]);
        mm16(pacc[3][1], cat8(pl[3], ph[3]), px[1]);
        SBAR();
        mm16(pacc[3][2], cat8(pl[3], ph[3]), px[2]);
        mm16(pacc[3][3], cat8(pl[3], ph[3]), px[3]);
        SBAR();
        mm16(pacc[4][0], cat8(pl[0], ph[0]), px[0]);
        mm16(pacc[4][1], cat8(pl[0], ph[0]), px[1]);
        SBAR();
        wait_vm_bar<63>();
        pl[2] = ldstr(pw0[0] + 32768); ph[2] = ldstr(pw0[0] + 32768 + 512);
        mm16(pacc[5][0], cat8(pl[1], ph[1]), px[0]);
        mm16(pacc[5][1], cat8(pl[1], ph[1]), px[1]);
        st16kv(wsr, vvo, vst + 20480, held[10]);
        SBAR();
        px[0] = lds128(pxr[0] + 32768); px[1] = lds128(pxr[0] + 33024); pl[3] = ldstr(pw0[0] + 36864); ph[3] = ldstr(pw0[0] + 36864 + 512);
        mm16(pacc[4][2], cat8(pl[0], ph[0]), px[2]);
        mm16(pacc[4][3], cat8(pl[0], ph[0]), px[3]);
        SBAR();
        mm16(pacc[5][2], cat8(pl[1], ph[1]), px[2]);
        mm16(pacc[5][3], cat8(pl[1], ph[1]), px[3]);
        st16kv(wsr, vvo, vst + 21504, held[11]);
        SBAR();
        px[2] = lds128(pxr[0] + 40960); px[3] = lds128(pxr[0] + 41216);
        pl[0] = ldstr(pw1[0] + 32768); ph[0] = ldstr(pw1[0] + 32768 + 512);
        mm16(pacc[0][0], cat8(pl[2], ph[2]), px[0]);
        mm16(pacc[0][1], cat8(pl[2], ph[2]), px[1]);
        SBAR();
        mm16(pacc[1][0], cat8(pl[3], ph[3]), px[0]);
        mm16(pacc[1][1], cat8(pl[3], ph[3]), px[1]);
        SBAR();
        pl[1] = ldstr(pw1[0] + 36864); ph[1] = ldstr(pw1[0] + 36864 + 512);
        mm16(pacc[0][2], cat8(pl[2], ph[2]), px[2]);
        mm16(pacc[0][3], cat8(pl[2], ph[2]), px[3]);
        SBAR();
        mm16(pacc[1][2], cat8(pl[3], ph[3]), px[2]);
        mm16(pacc[1][3], cat8(pl[3], ph[3]), px[3]);
        SBAR();
        pl[2] = ldstr(pw2[0] + 32768); ph[2] = ldstr(pw2[0] + 32768 + 512);
        mm16(pacc[2][0], cat8(pl[0], ph[0]), px[0]);
        mm16(pacc[2][1], cat8(pl[0], ph[0]), px[1]);
        SBAR();
        mm16(pacc[2][2], cat8(pl[0], ph[0]), px[2]);
        mm16(pacc[2][3], cat8(pl[0], ph[0]), px[3]);
        SBAR();
        pl[3] = ldstr(pw2[0] + 36864); ph[3] = ldstr(pw2[0] + 36864 + 512);
        mm16(pacc[3][0], cat8(pl[1], ph[1]), px[0]);
        mm16(pacc[3][1], cat8(pl[1], ph[1]), px[1]);
        SBAR();
        mm16(pacc[3][2], cat8(pl[1], ph[1]), px[2]);
        mm16(pacc[3][3], cat8(pl[1], ph[1]), px[3]);
        SBAR();
        mm16(pacc[4][0], cat8(pl[2], ph[2]), px[0]);
        mm16(pacc[4][1], cat8(pl[2], ph[2]), px[1]);
        SBAR();
        wait_vm_bar<63>();
        pl[0] = ldstr(pw0[1] + 0); ph[0] = ldstr(pw0[1] + 0 + 512);
        mm16(pacc[5][0], cat8(pl[3], ph[3]), px[0]);
        mm16(pacc[5][1], cat8(pl[3], ph[3]), px[1]);
        st16kv(wsr, vvo, vst + 24576, held[12]);
        SBAR();
        px[0] = lds128(pxr[1] + 0); px[1] = lds128(pxr[1] + 256); pl[1] = ldstr(pw0[1] + 4096); ph[1] = ldstr(pw0[1] + 4096 + 512);
        mm16(pacc[4][2], cat8(pl[2], ph[2]), px[2]);
        mm16(pacc[4][3], cat8(pl[2], ph[2]), px[3]);
        SBAR();
        mm16(pacc[5][2], cat8(pl[3], ph[3]), px[2]);
        mm16(pacc[5][3], cat8(pl[3], ph[3]), px[3]);
        st16kv(wsr, vvo, vst + 25600, held[13]);
        SBAR();
        px[2] = lds128(pxr[1] + 8192); px[3] = lds128(pxr[1] + 8448);
        pl[2] = ldstr(pw1[1] + 0); ph[2] = ldstr(pw1[1] + 0 + 512);
        mm16(pacc[0][0], cat8(pl[0], ph[0]), px[0]);
        mm16(pacc[0][1], cat8(pl[0], ph[0]), px[1]);
        SBAR();
        mm16(pacc[1][0], cat8(pl[1], ph[1]), px[0]);
        mm16(pacc[1][1], cat8(pl[1], ph[1]), px[1]);
        SBAR();
        pl[3] = ldstr(pw1[1] + 4096); ph[3] = ldstr(pw1[1] + 4096 + 512);
        mm16(pacc[0][2], cat8(pl[0], ph[0]), px[2]);
        mm16(pacc[0][3], cat8(pl[0], ph[0]), px[3]);
        SBAR();
        mm16(pacc[1][2], cat8(pl[1], ph[1]), px[2]);
        mm16(pacc[1][3], cat8(pl[1], ph[1]), px[3]);
        SBAR();
        pl[0] = ldstr(pw2[1] + 0); ph[0] = ldstr(pw2[1] + 0 + 512);
        mm16(pacc[2][0], cat8(pl[2], ph[2]), px[0]);
        mm16(pacc[2][1], cat8(pl[2], ph[2]), px[1]);
        SBAR();
        mm16(pacc[2][2], cat8(pl[2], ph[2]), px[2]);
        mm16(pacc[2][3], cat8(pl[2], ph[2]), px[3]);
        SBAR();
        pl[1] = ldstr(pw2[1] + 4096); ph[1] = ldstr(pw2[1] + 4096 + 512);
        mm16(pacc[3][0], cat8(pl[3], ph[3]), px[0]);
        mm16(pacc[3][1], cat8(pl[3], ph[3]), px[1]);
        SBAR();
        mm16(pacc[3][2], cat8(pl[3], ph[3]), px[2]);
        mm16(pacc[3][3], cat8(pl[3], ph[3]), px[3]);
        SBAR();
        mm16(pacc[4][0], cat8(pl[0], ph[0]), px[0]);
        mm16(pacc[4][1], cat8(pl[0], ph[0]), px[1]);
        SBAR();
        wait_vm_bar<63>();
        pl[2] = ldstr(pw0[1] + 32768); ph[2] = ldstr(pw0[1] + 32768 + 512);
        mm16(pacc[5][0], cat8(pl[1], ph[1]), px[0]);
        mm16(pacc[5][1], cat8(pl[1], ph[1]), px[1]);
        st16kv(wsr, vvo, vst + 28672, held[14]);
        SBAR();
        px[0] = lds128(pxr[1] + 32768); px[1] = lds128(pxr[1] + 33024); pl[3] = ldstr(pw0[1] + 36864); ph[3] = ldstr(pw0[1] + 36864 + 512);
        mm16(pacc[4][2], cat8(pl[0], ph[0]), px[2]);
        mm16(pacc[4][3], cat8(pl[0], ph[0]), px[3]);
        SBAR();
        mm16(pacc[5][2], cat8(pl[1], ph[1]), px[2]);
        mm16(pacc[5][3], cat8(pl[1], ph[1]), px[3]);
        st16kv(wsr, vvo, vst + 29696, held[15]);
        SBAR();
        px[2] = lds128(pxr[1] + 40960); px[3] = lds128(pxr[1] + 41216);
        pl[0] = ldstr(pw1[1] + 32768); ph[0] = ldstr(pw1[1] + 32768 + 512);
        mm16(pacc[0][0], cat8(pl[2], ph[2]), px[0]);
        mm16(pacc[0][1], cat8(pl[2], ph[2]), px[1]);
        kad += 16384u; ksrd[0] = (unsigned)kad; ksrd[1] = (unsigned)(kad >> 32);
        SBAR();
        mm16(pacc[1][0], cat8(pl[3], ph[3]), px[0]);
        mm16(pacc[1][1], cat8(pl[3], ph[3]), px[1]);
        SBAR();
        pl[1] = ldstr(pw1[1] + 36864); ph[1] = ldstr(pw1[1] + 36864 + 512);
        mm16(pacc[0][2], cat8(pl[2], ph[2]), px[2]);
        mm16(pacc[0][3], cat8(pl[2], ph[2]), px[3]);
        SBAR();
        mm16(pacc[1][2], cat8(pl[3], ph[3]), px[2]);
        mm16(pacc[1][3], cat8(pl[3], ph[3]), px[3]);
        SBAR();
        pl[2] = ldstr(pw2[1] + 32768); ph[2] = ldstr(pw2[1] + 32768 + 512);
        mm16(pacc[2][0], cat8(pl[0], ph[0]), px[0]);
        mm16(pacc[2][1], cat8(pl[0], ph[0]), px[1]);
        SBAR();
        mm16(pacc[2][2], cat8(pl[0], ph[0]), px[2]);
        mm16(pacc[2][3], cat8(pl[0], ph[0]), px[3]);
        SBAR();
        pl[3] = ldstr(pw2[1] + 36864); ph[3] = ldstr(pw2[1] + 36864 + 512);
        mm16(pacc[3][0], cat8(pl[1], ph[1]), px[0]);
        mm16(pacc[3][1], cat8(pl[1], ph[1]), px[1]);
        SBAR();
        mm16(pacc[3][2], cat8(pl[1], ph[1]), px[2]);
        mm16(pacc[3][3], cat8(pl[1], ph[1]), px[3]);
        SBAR();
        mm16(pacc[4][0], cat8(pl[2], ph[2]), px[0]);
        mm16(pacc[4][1], cat8(pl[2], ph[2]), px[1]);
        SBAR();
        mm16(pacc[5][0], cat8(pl[3], ph[3]), px[0]);
        mm16(pacc[5][1], cat8(pl[3], ph[3]), px[1]);
        SBAR();
        mm16(pacc[4][2], cat8(pl[2], ph[2]), px[2]);
        mm16(pacc[4][3], cat8(pl[2], ph[2]), px[3]);
        SBAR();
        mm16(pacc[5][2], cat8(pl[3], ph[3]), px[2]);
        mm16(pacc[5][3], cat8(pl[3], ph[3]), px[3]);
        SBAR();
        asm volatile("s_nop 7" ::: "memory"); SBAR();
        }
        wait_vm_bar<32>();
        ldsp kb0 = (ldsp)lds + opq(l16);
        ldsp qsrc = (ldsp)hand;
        u32x4 hw;
        qr[0] = lds128(qsrc);
        kf[0] = lds128(kb0 + 0);
        kf[1] = lds128(kb0 + 8192);
        kf[2] = lds128(kb0 + 1024);
        kf[3] = lds128(kb0 + 9216);
        qr[1] = lds128(qsrc + 1024);
        qr[2] = lds128(qsrc + 2048);
        qr[3] = lds128(qsrc + 3072);
        qr[4] = lds128(qsrc + 4096);
        qr[5] = lds128(qsrc + 5120);
        qr[6] = lds128(qsrc + 6144);
        qr[7] = lds128(qsrc + 7168);
        SBAR();
        hw[0] = cvtpk(gelu_mul(pacc[0][0][0], pacc[1][0][0]), gelu_mul(pacc[0][0][1], pacc[1][0][1])); PIN(hw);
        hw[1] = cvtpk(gelu_mul(pacc[0][0][2], pacc[1][0][2]), gelu_mul(pacc[0][0][3], pacc[1][0][3])); PIN(hw);
        hw[2] = cvtpk(gelu_mul(pacc[0][1][0], pacc[1][1][0]), gelu_mul(pacc[0][1][1], pacc[1][1][1])); PIN(hw);
        SBAR();
        kf[4] = lds128(kb0 + 2048);
        sA0 = MFMA(kf[0], qr[0], f32x16{}); PIN(sA0);
        hw[3] = cvtpk(gelu_mul(pacc[0][1][2], pacc[1][1][2]), gelu_mul(pacc[0][1][3], pacc[1][1][3])); PIN(hw); st16(wsr, fcol / 16 + 0 >= 3 * h ? vvo : 0xf0000000u, WS_H + (rbA * NFU + fcol / 16 + 0) * 1024, swapT(hw));
        SBAR();
        kf[5] = lds128(kb0 + 10240);
        sA1 = MFMA(kf[1], qr[0], f32x16{}); PIN(sA1);
        hw[0] = cvtpk(gelu_mul(pacc[2][0][0], pacc[3][0][0]), gelu_mul(pacc[2][0][1], pacc[3][0][1])); PIN(hw);
        SBAR();
        kf[6] = lds128(kb0 + 3072);
        sA0 = MFMA(kf[2], qr[1], sA0); PIN(sA0);
        hw[1] = cvtpk(gelu_mul(pacc[2][0][2], pacc[3][0][2]), gelu_mul(pacc[2][0][3], pacc[3][0][3])); PIN(hw);
        SBAR();
        kf[7] = lds128(kb0 + 11264);
        sA1 = MFMA(kf[3], qr[1], sA1); PIN(sA1);
        hw[2] = cvtpk(gelu_mul(pacc[2][1][0], pacc[3][1][0]), gelu_mul(pacc[2][1][1], pacc[3][1][1])); PIN(hw);
        SBAR();
        kf[8] = lds128(kb0 + 4096);
        sA0 = MFMA(kf[4], qr[2], sA0); PIN(sA0);
        hw[3] = cvtpk(gelu_mul(pacc[2][1][2], pacc[3][1][2]), gelu_mul(pacc[2][1][3], pacc[3][1][3])); PIN(hw); st16(wsr, fcol / 16 + 1 >= 3 * h ? vvo : 0xf0000000u, WS_H + (rbA * NFU + fcol / 16 + 1) * 1024, swapT(hw));
        SBAR();
        kf[9] = lds128(kb0 + 12288);
        sA1 = MFMA(kf[5], qr[2], sA1); PIN(sA1);
        hw[0] = cvtpk(gelu_mul(pacc[4][0][0], pacc[5][0][0]), gelu_mul(pacc[4][0][1], pacc[5][0][1])); PIN(hw);
        SBAR();
        kf[10] = lds128(kb0 + 5120);
        sA0 = MFMA(kf[6], qr[3], sA0); PIN(sA0);
        hw[1] = cvtpk(gelu_mul(pacc[4][0][2], pacc[5][0][2]), gelu_mul(pacc[4][0][3], pacc[5][0][3])); PIN(hw);
        SBAR();
        kf[11] = lds128(kb0 + 13312);
        sA1 = MFMA(kf[7], qr[3], sA1); PIN(sA1);
        hw[2] = cvtpk(gelu_mul(pacc[4][1][0], pacc[5][1][0]), gelu_mul(pacc[4][1][1], pacc[5][1][1])); PIN(hw);
        SBAR();
        kf[12] = lds128(kb0 + 6144);
        sA0 = MFMA(kf[8], qr[4], sA0); PIN(sA0);
        hw[3] = cvtpk(gelu_mul(pacc[4][1][2], pacc[5][1][2]), gelu_mul(pacc[4][1][3], pacc[5][1][3])); PIN(hw); st16(wsr, fcol / 16 + 2 >= 3 * h ? vvo : 0xf0000000u, WS_H + (rbA * NFU + fcol / 16 + 2) * 1024, swapT(hw));
        SBAR();
        kf[13] = lds128(kb0 + 14336);
        sA1 = MFMA(kf[9], qr[4], sA1); PIN(sA1);
        hw[0] = cvtpk(gelu_mul(pacc[0][2][0], pacc[1][2][0]), gelu_mul(pacc[0][2][1], pacc[1][2][1])); PIN(hw);
        SBAR();
        kf[14] = lds128(kb0 + 7168);
        sA0 = MFMA(kf[10], qr[5], sA0); PIN(sA0);
        hw[1] = cvtpk(gelu_mul(pacc[0][2][2], pacc[1][2][2]), gelu_mul(pacc[0][2][3], pacc[1][2][3])); PIN(hw);
        SBAR();
        kf[15] = lds128(kb0 + 15360);
        sA1 = MFMA(kf[11], qr[5], sA1); PIN(sA1);
        hw[2] = cvtpk(gelu_mul(pacc[0][3][0], pacc[1][3][0]), gelu_mul(pacc[0][3][1], pacc[1][3][1])); PIN(hw);
        SBAR();
        sA0 = MFMA(kf[12], qr[6], sA0); PIN(sA0);
        hw[3] = cvtpk(gelu_mul(pacc[0][3][2], pacc[1][3][2]), gelu_mul(pacc[0][3][3], pacc[1][3][3])); PIN(hw); st16(wsr, fcol / 16 + 0 >= 3 * h ? vvo : 0xf0000000u, WS_H + (rbB * NFU + fcol / 16 + 0) * 1024, swapT(hw));
        SBAR();
        sA1 = MFMA(kf[13], qr[6], sA1); PIN(sA1);
        hw[0] = cvtpk(gelu_mul(pacc[2][2][0], pacc[3][2][0]), gelu_mul(pacc[2][2][1], pacc[3][2][1])); PIN(hw);
        SBAR();
        sA0 = MFMA(kf[14], qr[7], sA0); PIN(sA0);
        hw[1] = cvtpk(gelu_mul(pacc[2][2][2], pacc[3][2][2]), gelu_mul(pacc[2][2][3], pacc[3][2][3])); PIN(hw);
        SBAR();
        sA1 = MFMA(kf[15], qr[7], sA1); PIN(sA1);
        hw[2] = cvtpk(gelu_mul(pacc[2][3][0], pacc[3][3][0]), gelu_mul(pacc[2][3][1], pacc[3][3][1])); PIN(hw);
        SBAR();
        hw[3] = cvtpk(gelu_mul(pacc[2][3][2], pacc[3][3][2]), gelu_mul(pacc[2][3][3], pacc[3][3][3])); PIN(hw); st16(wsr, fcol / 16 + 1 >= 3 * h ? vvo : 0xf0000000u, WS_H + (rbB * NFU + fcol / 16 + 1) * 1024, swapT(hw));
        SBAR();
        hw[0] = cvtpk(gelu_mul(pacc[4][2][0], pacc[5][2][0]), gelu_mul(pacc[4][2][1], pacc[5][2][1])); PIN(hw);
        SBAR();
        hw[1] = cvtpk(gelu_mul(pacc[4][2][2], pacc[5][2][2]), gelu_mul(pacc[4][2][3], pacc[5][2][3])); PIN(hw);
        SBAR();
        hw[2] = cvtpk(gelu_mul(pacc[4][3][0], pacc[5][3][0]), gelu_mul(pacc[4][3][1], pacc[5][3][1])); PIN(hw);
        SBAR();
        hw[3] = cvtpk(gelu_mul(pacc[4][3][2], pacc[5][3][2]), gelu_mul(pacc[4][3][3], pacc[5][3][3])); PIN(hw); st16(wsr, fcol / 16 + 2 >= 3 * h ? vvo : 0xf0000000u, WS_H + (rbB * NFU + fcol / 16 + 2) * 1024, swapT(hw));
        SBAR();
        }
    }
    const int sw = 2 * j + 2;
    const int swA = isA ? sw : -100;
    const unsigned mskb = (isA ? (wid < 2 ? 3u : 2u) << (sw - 2) : 0u) | (wid < 6 ? 1u << 16 : 0u), flgb = 10u | (2u << sw);
    f32x16 o[4], negm, sB0, sB1;
#pragma unroll
    for (int d = 0; d < 4; ++d) o[d] = f32x16{};
    float m = 0.f, l0 = 0.f, l1 = 0.f, l2 = 0.f, fsc = 1.f, lfin = 0.f; bool pend = false;
    u32x4 pk0, pk1, pk2, pk3;
#define LSUM() lfin = (l0 + l1) + l2
#define LSCALE(f) do { l0 *= (f); l1 *= (f); l2 *= (f); } while (0)
    bf16x8 vf[16];
    ldsp kbase = (ldsp)lds + l16, vbase = (ldsp)lds + 65536 + l16;
    const unsigned ctxA = WS_CTX + h * 8192 + rbA * 262144, ctxB = WS_CTX + h * 8192 + rbB * 262144;
#define SETUP_KV(t) do { \
        kad += ((t) + 1 >= sw) ? 32768u : 16384u; ksrd[0] = (unsigned)kad; ksrd[1] = (unsigned)(kad >> 32); ksrd[2] = (t) + 2 > 16 ? 0u : (((t) + 2 >= sw) ? 32768u : 16384u); \
        vad += ((t) >= sw) ? 32768u : 16384u; vsrd[0] = (unsigned)vad; vsrd[1] = (unsigned)(vad >> 32); vsrd[2] = ((t) + 1 >= sw) ? 32768u : 16384u; \
        asm volatile("" : "+s"(ksrd), "+s"(vsrd)); } while (0)
#define PIECE(p, PAR) do { if ((p) == 0) dmab<0>(kvoff, ksrd, lds0w + (PAR) * 32768); else if ((p) == 4) dmab<0>(kvoff, vsrd, lds0w + 65536 + (1 - (PAR)) * 32768); \
        else dmab_m<((p) & 3) * 1024>(kvoff, (p) < 4 ? ksrd : vsrd); } while (0)
#define STEPBAR() wait_vm_bar<0>()
#define EXL(v) EX2(v)
#define ROWMAX(C0, C1) ({ float a_ = MX3(C0[0], C0[1], C1[0]), b_ = MX3(C0[2], C0[3], C1[1]); a_ = MX3(a_, C1[2], C1[3]); \
        _Pragma("unroll") for (int e = 4; e < 16; e += 4) { a_ = MX3(a_, C0[e], C0[e + 1]); b_ = MX3(b_, C0[e + 2], C0[e + 3]); a_ = MX3(a_, C1[e], C1[e + 1]); b_ = MX3(b_, C1[e + 2], C1[e + 3]); } \
        half_max(__builtin_fmaxf(a_, b_)); })
    {
        if (swA == 2 && wid < 2) cmask(sA0, sA1, rb * 32, l16);
        m = __builtin_fmaxf(ROWMAX(sA0, sA1), -1e30f);
#pragma unroll
        for (int e = 0; e < 16; ++e) { sA0[e] = EX2(sA0[e] - m); sA1[e] -= m; negm[e] = -m; }
#pragma unroll
        for (int e = 0; e < 8; e += 2) { l0 += sA0[e]; l1 += sA0[e + 1]; }
        SETUP_KV(0);
        SBAR();
        if (isA) wait_vm<0>();
        wait_vm_bar<6>();
        asm volatile("; WARMV_END %0" :: "v"(wjunk));
        if (j >= 2 && wid == 4) st_flag(wsr, WS_FLAG + F_KV * 32768 + b * 128, tag);
        PIECE(0, 0); PIECE(1, 0);
#pragma unroll
        for (int i = 0; i < 4; ++i) kf[i] = lds128(kbase + 32768 + ((i & 1) * 8 + (i >> 1)) * 1024);
        SBAR();
    }
#define PKF(i) __builtin_bit_cast(bf16x8, ((i) < 4) ? pk0 : ((i) < 8) ? pk1 : ((i) < 12) ? pk2 : pk3)
    for (int t = 1; t < 16; t += 2) {
        {
        vbase += (t == swA + 1) ? 16384 : 0; PIN(vbase);
        SBAR();
        kf[5] = lds128(kbase + 43008); MEMF(); kf[4] = lds128(kbase + 34816);
        asm volatile("v_mfma_f32_32x32x16_bf16 %0, %1, %2, %3" : "=&v"(sB0) : "v"(kf[0]), "v"(qr[0]), "v"(negm));
        SBAR();
        PIECE(2, 0);
        pk0[0] = cvtpk(sA0[0], sA0[1]); PIN(pk0);
        sA1[0] = EXL(sA1[0]); sA1[1] = EXL(sA1[1]); PIN(sA1);
        SBAR();
        asm volatile("v_mfma_f32_32x32x16_bf16 %0, %1, %2, %3" : "=&v"(sB1) : "v"(kf[1]), "v"(qr[0]), "v"(negm));
        SBAR();
        PIECE(3, 0);
        pk0[1] = cvtpk(sA0[2], sA0[3]); PIN(pk0);
        sA1[2] = EXL(sA1[2]); sA1[3] = EXL(sA1[3]); PIN(sA1);
        SBAR();
        kf[7] = lds128(kbase + 44032); MEMF(); kf[6] = lds128(kbase + 35840);
        sB0 = MFMA(kf[2], qr[1], sB0); PIN(sB0);
        SBAR();
        PIECE(4, 0);
        pk0[2] = cvtpk(sA0[4], sA0[5]); PIN(pk0);
        sA1[4] = EXL(sA1[4]); sA1[5] = EXL(sA1[5]); PIN(sA1);
        SBAR();
        sB1 = MFMA(kf[3], qr[1], sB1); PIN(sB1);
        SBAR();
        PIECE(5, 0);
        pk0[3] = cvtpk(sA0[6], sA0[7]); PIN(pk0);
        sA1[6] = EXL(sA1[6]); sA1[7] = EXL(sA1[7]); PIN(sA1);
        SBAR();
        kf[9] = lds128(kbase + 45056); MEMF(); kf[8] = lds128(kbase + 36864);
        sB0 = MFMA(kf[4], qr[2], sB0); PIN(sB0);
        SBAR();
        PIECE(6, 0);
        pk1[0] = cvtpk(sA0[8], sA0[9]); PIN(pk1);
        sA1[8] = EXL(sA1[8]); sA1[9] = EXL(sA1[9]); PIN(sA1);
        SBAR();
        sB1 = MFMA(kf[5], qr[2], sB1); PIN(sB1);
        SBAR();
        PIECE(7, 0);
        pk1[1] = cvtpk(sA0[10], sA0[11]); PIN(pk1);
        sA1[10] = EXL(sA1[10]); sA1[11] = EXL(sA1[11]); PIN(sA1);
        SBAR();
        kf[11] = lds128(kbase + 46080); MEMF(); kf[10] = lds128(kbase + 37888);
        sB0 = MFMA(kf[6], qr[3], sB0); PIN(sB0);
        SBAR();
        pk1[2] = cvtpk(sA0[12], sA0[13]); PIN(pk1);
        sA1[12] = EXL(sA1[12]); sA1[13] = EXL(sA1[13]); PIN(sA1);
        SBAR();
        sB1 = MFMA(kf[7], qr[3], sB1); PIN(sB1);
        SBAR();
        pk1[3] = cvtpk(sA0[14], sA0[15]); PIN(pk1);
        sA1[14] = EXL(sA1[14]); sA1[15] = EXL(sA1[15]); PIN(sA1);
        SBAR();
        kf[13] = lds128(kbase + 47104); MEMF(); kf[12] = lds128(kbase + 38912);
        sB0 = MFMA(kf[8], qr[4], sB0); PIN(sB0);
        SBAR();
        l0 += sA1[0]; l1 += sA1[1]; l2 += sA0[8]; PIN(l0); PIN(l1); PIN(l2);
        pk2[0] = cvtpk(sA1[0], sA1[1]); PIN(pk2);
        SBAR();
        sB1 = MFMA(kf[9], qr[4], sB1); PIN(sB1);
        SBAR();
        l0 += sA1[2]; l1 += sA1[3]; l2 += sA0[9]; PIN(l0); PIN(l1); PIN(l2);
        pk2[1] = cvtpk(sA1[2], sA1[3]); PIN(pk2);
        SBAR();
        kf[15] = lds128(kbase + 48128); MEMF(); kf[14] = lds128(kbase + 39936);
        sB0 = MFMA(kf[10], qr[5], sB0); PIN(sB0);
        SBAR();
        l0 += sA1[4]; l1 += sA1[5]; l2 += sA0[10]; PIN(l0); PIN(l1); PIN(l2);
        pk2[2] = cvtpk(sA1[4], sA1[5]); PIN(pk2);
        SBAR();
        sB1 = MFMA(kf[11], qr[5], sB1); PIN(sB1);
        SBAR();
        l0 += sA1[6]; l1 += sA1[7]; l2 += sA0[11]; PIN(l0); PIN(l1); PIN(l2);
        pk2[3] = cvtpk(sA1[6], sA1[7]); PIN(pk2);
        SBAR();
        vf[1] = lds128(vbase + 1024); MEMF(); vf[0] = lds128(vbase + 0);
        sB0 = MFMA(kf[12], qr[6], sB0); PIN(sB0);
        SBAR();
        l0 += sA1[8]; l1 += sA1[9]; l2 += sA0[12]; PIN(l0); PIN(l1); PIN(l2);
        pk3[0] = cvtpk(sA1[8], sA1[9]); PIN(pk3);
        SBAR();
        sB1 = MFMA(kf[13], qr[6], sB1); PIN(sB1);
        SBAR();
        l0 += sA1[10]; l1 += sA1[11]; l2 += sA0[13]; PIN(l0); PIN(l1); PIN(l2);
        pk3[1] = cvtpk(sA1[10], sA1[11]); PIN(pk3);
        SBAR();
        vf[3] = lds128(vbase + 3072); MEMF(); vf[2] = lds128(vbase + 2048);
        sB0 = MFMA(kf[14], qr[7], sB0); PIN(sB0);
        SBAR();
        l0 += sA1[12]; l1 += sA1[13]; l2 += sA0[14]; PIN(l0); PIN(l1); PIN(l2);
        pk3[2] = cvtpk(sA1[12], sA1[13]); PIN(pk3);
        SBAR();
        sB1 = MFMA(kf[15], qr[7], sB1); PIN(sB1);
        SBAR();
        l0 += sA1[14]; l1 += sA1[15]; l2 += sA0[15]; PIN(l0); PIN(l1); PIN(l2);
        pk3[3] = cvtpk(sA1[14], sA1[15]); PIN(pk3);
        SBAR();
        vf[5] = lds128(vbase + 5120); MEMF(); vf[4] = lds128(vbase + 4096);
        o[0] = MFMA(vf[0], __builtin_bit_cast(bf16x8, pk0), o[0]); PIN(o[0]);
        SBAR();
        SBAR();
        o[1] = MFMA(vf[1], __builtin_bit_cast(bf16x8, pk0), o[1]); PIN(o[1]);
        SBAR();
        SBAR();
        if (__builtin_expect((mskb >> t) & 1u, 0)) {
            const int tile_ = (t >= sw) ? 2 * t - sw + (isA ? 1 : 0) : t;
            cmask(sB0, sB1, ((t >= sw) ? rbB : rb) * 32 - 64 * tile_, l16); }
        float a_ = max3(sB0[0], sB0[1], sB0[2]), b_ = max3(sB0[3], sB0[4], sB0[5]);
        a_ = max3(a_, sB0[6], sB0[7]); b_ = max3(b_, sB0[8], sB0[9]);
        SBAR();
        vf[7] = lds128(vbase + 7168); MEMF(); vf[6] = lds128(vbase + 6144);
        o[2] = MFMA(vf[2], __builtin_bit_cast(bf16x8, pk0), o[2]); PIN(o[2]);
        SBAR();
        a_ = max3(a_, sB0[10], sB0[11]); b_ = max3(b_, sB0[12], sB0[13]);
        a_ = max3(a_, sB0[14], sB0[15]); b_ = max3(b_, sB1[0], sB1[1]);
        a_ = max3(a_, sB1[2], sB1[3]); b_ = max3(b_, sB1[4], sB1[5]);
        SBAR();
        o[3] = MFMA(vf[3], __builtin_bit_cast(bf16x8, pk0), o[3]); PIN(o[3]);
        SBAR();
        a_ = max3(a_, sB1[6], sB1[7]); b_ = max3(b_, sB1[8], sB1[9]);
        a_ = max3(a_, sB1[10], sB1[11]); b_ = max3(b_, sB1[12], sB1[13]);
        a_ = max3(a_, sB1[14], sB1[15]);
        SBAR();
        vf[9] = lds128(vbase + 9216); MEMF(); vf[8] = lds128(vbase + 8192);
        o[0] = MFMA(vf[4], __builtin_bit_cast(bf16x8, pk1), o[0]); PIN(o[0]);
        SBAR();
        const float rm_ = half_max2(a_, b_);
        if (__builtin_expect(false || __any(rm_ > THR), 0)) {
            const float dl_ = __builtin_fmaxf(rm_, false ? -1e30f : 0.f); m += dl_; fsc = false ? 0.f : EX2(-dl_); LSUM(); LSCALE(fsc); pend = true;
            _Pragma("unroll") for (int e = 0; e < 16; ++e) { sB0[e] -= dl_; sB1[e] -= dl_; }
            bcast16(negm, -m, l16); }
        SBAR();
        o[1] = MFMA(vf[5], __builtin_bit_cast(bf16x8, pk1), o[1]); PIN(o[1]);
        SBAR();
        sB0[0] = EXL(sB0[0]);
        sB0[1] = EXL(sB0[1]);
        PIN(sB0); SBAR();
        vf[11] = lds128(vbase + 11264); MEMF(); vf[10] = lds128(vbase + 10240);
        o[2] = MFMA(vf[6], __builtin_bit_cast(bf16x8, pk1), o[2]); PIN(o[2]);
        SBAR();
        l0 += sB0[0]; PIN(l0);
        sB0[2] = EXL(sB0[2]);
        PIN(sB0); SBAR();
        o[3] = MFMA(vf[7], __builtin_bit_cast(bf16x8, pk1), o[3]); PIN(o[3]);
        SBAR();
        sB0[3] = EXL(sB0[3]);
        sB0[4] = EXL(sB0[4]);
        PIN(sB0); SBAR();
        vf[13] = lds128(vbase + 13312); MEMF(); vf[12] = lds128(vbase + 12288);
        o[0] = MFMA(vf[8], __builtin_bit_cast(bf16x8, pk2), o[0]); PIN(o[0]);
        SBAR();
        SETUP_KV(t);
        l1 += sB0[1]; PIN(l1);
        sB0[5] = EXL(sB0[5]);
        PIN(sB0); SBAR();
        o[1] = MFMA(vf[9], __builtin_bit_cast(bf16x8, pk2), o[1]); PIN(o[1]);
        SBAR();
        sB0[6] = EXL(sB0[6]);
        sB0[7] = EXL(sB0[7]);
        PIN(sB0); SBAR();
        vf[15] = lds128(vbase + 15360); MEMF(); vf[14] = lds128(vbase + 14336);
        o[2] = MFMA(vf[10], __builtin_bit_cast(bf16x8, pk2), o[2]); PIN(o[2]);
        SBAR();
        l0 += sB0[2]; PIN(l0);
        sB0[8] = EXL(sB0[8]);
        PIN(sB0); SBAR();
        o[3] = MFMA(vf[11], __builtin_bit_cast(bf16x8, pk2), o[3]); PIN(o[3]);
        SBAR();
        sB0[9] = EXL(sB0[9]);
        sB0[10] = EXL(sB0[10]);
        PIN(sB0); SBAR();
        o[0] = MFMA(vf[12], __builtin_bit_cast(bf16x8, pk3), o[0]); PIN(o[0]);
        SBAR();
        l1 += sB0[3]; PIN(l1);
        sB0[11] = EXL(sB0[11]);
        PIN(sB0); SBAR();
        o[1] = MFMA(vf[13], __builtin_bit_cast(bf16x8, pk3), o[1]); PIN(o[1]);
        SBAR();
        sB0[12] = EXL(sB0[12]);
        sB0[13] = EXL(sB0[13]);
        PIN(sB0); SBAR();
        STEPBAR();
        if (__builtin_expect((flgb >> t) & 1u, 0)) {
            if (t == sw + 1 && wid == 4) st_flag(wsr, WS_FLAG + F_CA * 32768 + b * 128, tag);
            if (t == 1) { if (wid == 4) st_flag(wsr, WS_FLAG + F_H * 32768 + b * 128, tag); poll_reissue(pv, fbase + F_KV * 32768, (unsigned)((b & 31) + 32 * ((opq(l16) >> 4) & 7)) * 128u); }
            if (t == 3) poll_check(pv, fbase + F_KV * 32768, (unsigned)((b & 31) + 32 * ((opq(l16) >> 4) & 7)) * 128u, tag); }
        kbase += (t + 1 == swA) ? 16384 : 0; PIN(kbase);
        { kf[1] = lds128(kbase + 8192); MEMF(); kf[0] = lds128(kbase + 0); MEMF(); kf[3] = lds128(kbase + 9216); MEMF(); kf[2] = lds128(kbase + 1024); PIECE(0, 1); }
        SBAR();
        o[2] = MFMA(vf[14], __builtin_bit_cast(bf16x8, pk3), o[2]); PIN(o[2]);
        SBAR();
        PIECE(1, 1);
        l0 += sB0[4]; PIN(l0);
        l1 += sB0[5]; PIN(l1);
        l0 += sB0[6]; PIN(l0);
        sB0[14] = EXL(sB0[14]);
        PIN(sB0); SBAR();
        o[3] = MFMA(vf[15], __builtin_bit_cast(bf16x8, pk3), o[3]); PIN(o[3]);
        SBAR();
        l1 += sB0[7]; PIN(l1);
        sB0[15] = EXL(sB0[15]);
        PIN(sB0); SBAR();
        if (__builtin_expect(pend, 0)) {
            _Pragma("unroll") for (int d = 0; d < 4; ++d) o[d] *= fsc;
            pend = false; }
        SBAR();
        }
        {
        const bool dosw = (t + 1) == swA;
        if (__builtin_expect(dosw, 0)) { const unsigned lq_ = opq(l16);
            qr[0] = lds128((ldsp)lds + LDS_QB + wid * 8192 + 0 + lq_);
            qr[1] = lds128((ldsp)lds + LDS_QB + wid * 8192 + 1024 + lq_);
            qr[2] = lds128((ldsp)lds + LDS_QB + wid * 8192 + 2048 + lq_);
            qr[3] = lds128((ldsp)lds + LDS_QB + wid * 8192 + 3072 + lq_);
            qr[4] = lds128((ldsp)lds + LDS_QB + wid * 8192 + 4096 + lq_);
            qr[5] = lds128((ldsp)lds + LDS_QB + wid * 8192 + 5120 + lq_);
            qr[6] = lds128((ldsp)lds + LDS_QB + wid * 8192 + 6144 + lq_);
            qr[7] = lds128((ldsp)lds + LDS_QB + wid * 8192 + 7168 + lq_);
            m = 0.f; bcast16(negm, 0.f, l16); }
        vbase += ((t + 1) == swA + 1) ? 16384 : 0; PIN(vbase);
        SBAR();
        kf[5] = lds128(kbase + 10240); MEMF(); kf[4] = lds128(kbase + 2048);
        asm volatile("v_mfma_f32_32x32x16_bf16 %0, %1, %2, %3" : "=&v"(sA0) : "v"(kf[0]), "v"(qr[0]), "v"(negm));
        SBAR();
        PIECE(2, 1);
        pk0[0] = cvtpk(sB0[0], sB0[1]); PIN(pk0);
        sB1[0] = EXL(sB1[0]); sB1[1] = EXL(sB1[1]); PIN(sB1);
        SBAR();
        asm volatile("v_mfma_f32_32x32x16_bf16 %0, %1, %2, %3" : "=&v"(sA1) : "v"(kf[1]), "v"(qr[0]), "v"(negm));
        SBAR();
        PIECE(3, 1);
        pk0[1] = cvtpk(sB0[2], sB0[3]); PIN(pk0);
        sB1[2] = EXL(sB1[2]); sB1[3] = EXL(sB1[3]); PIN(sB1);
        SBAR();
        kf[7] = lds128(kbase + 11264); MEMF(); kf[6] = lds128(kbase + 3072);
        sA0 = MFMA(kf[2], qr[1], sA0); PIN(sA0);
        SBAR();
        PIECE(4, 1);
        pk0[2] = cvtpk(sB0[4], sB0[5]); PIN(pk0);
        sB1[4] = EXL(sB1[4]); sB1[5] = EXL(sB1[5]); PIN(sB1);
        SBAR();
        sA1 = MFMA(kf[3], qr[1], sA1); PIN(sA1);
        SBAR();
        PIECE(5, 1);
        pk0[3] = cvtpk(sB0[6], sB0[7]); PIN(pk0);
        sB1[6] = EXL(sB1[6]); sB1[7] = EXL(sB1[7]); PIN(sB1);
        SBAR();
        kf[9] = lds128(kbase + 12288); MEMF(); kf[8] = lds128(kbase + 4096);
        sA0 = MFMA(kf[4], qr[2], sA0); PIN(sA0);
        SBAR();
        PIECE(6, 1);
        pk1[0] = cvtpk(sB0[8], sB0[9]); PIN(pk1);
        sB1[8] = EXL(sB1[8]); sB1[9] = EXL(sB1[9]); PIN(sB1);
        SBAR();
        sA1 = MFMA(kf[5], qr[2], sA1); PIN(sA1);
        SBAR();
        PIECE(7, 1);
        pk1[1] = cvtpk(sB0[10], sB0[11]); PIN(pk1);
        sB1[10] = EXL(sB1[10]); sB1[11] = EXL(sB1[11]); PIN(sB1);
        SBAR();
        kf[11] = lds128(kbase + 13312); MEMF(); kf[10] = lds128(kbase + 5120);
        sA0 = MFMA(kf[6], qr[3], sA0); PIN(sA0);
        SBAR();
        pk1[2] = cvtpk(sB0[12], sB0[13]); PIN(pk1);
        sB1[12] = EXL(sB1[12]); sB1[13] = EXL(sB1[13]); PIN(sB1);
        SBAR();
        sA1 = MFMA(kf[7], qr[3], sA1); PIN(sA1);
        SBAR();
        pk1[3] = cvtpk(sB0[14], sB0[15]); PIN(pk1);
        sB1[14] = EXL(sB1[14]); sB1[15] = EXL(sB1[15]); PIN(sB1);
        SBAR();
        kf[13] = lds128(kbase + 14336); MEMF(); kf[12] = lds128(kbase + 6144);
        sA0 = MFMA(kf[8], qr[4], sA0); PIN(sA0);
        SBAR();
        l0 += sB1[0]; l1 += sB1[1]; l2 += sB0[8]; PIN(l0); PIN(l1); PIN(l2);
        pk2[0] = cvtpk(sB1[0], sB1[1]); PIN(pk2);
        SBAR();
        sA1 = MFMA(kf[9], qr[4], sA1); PIN(sA1);
        SBAR();
        l0 += sB1[2]; l1 += sB1[3]; l2 += sB0[9]; PIN(l0); PIN(l1); PIN(l2);
        pk2[1] = cvtpk(sB1[2], sB1[3]); PIN(pk2);
        SBAR();
        kf[15] = lds128(kbase + 15360); MEMF(); kf[14] = lds128(kbase + 7168);
        sA0 = MFMA(kf[10], qr[5], sA0); PIN(sA0);
        SBAR();
        l0 += sB1[4]; l1 += sB1[5]; l2 += sB0[10]; PIN(l0); PIN(l1); PIN(l2);
        pk2[2] = cvtpk(sB1[4], sB1[5]); PIN(pk2);
        SBAR();
        sA1 = MFMA(kf[11], qr[5], sA1); PIN(sA1);
        SBAR();
        l0 += sB1[6]; l1 += sB1[7]; l2 += sB0[11]; PIN(l0); PIN(l1); PIN(l2);
        pk2[3] = cvtpk(sB1[6], sB1[7]); PIN(pk2);
        SBAR();
        vf[1] = lds128(vbase + 33792); MEMF(); vf[0] = lds128(vbase + 32768);
        sA0 = MFMA(kf[12], qr[6], sA0); PIN(sA0);
        SBAR();
        l0 += sB1[8]; l1 += sB1[9]; l2 += sB0[12]; PIN(l0); PIN(l1); PIN(l2);
        pk3[0] = cvtpk(sB1[8], sB1[9]); PIN(pk3);
        SBAR();
        sA1 = MFMA(kf[13], qr[6], sA1); PIN(sA1);
        SBAR();
        l0 += sB1[10]; l1 += sB1[11]; l2 += sB0[13]; PIN(l0); PIN(l1); PIN(l2);
        pk3[1] = cvtpk(sB1[10], sB1[11]); PIN(pk3);
        SBAR();
        vf[3] = lds128(vbase + 35840); MEMF(); vf[2] = lds128(vbase + 34816);
        sA0 = MFMA(kf[14], qr[7], sA0); PIN(sA0);
        SBAR();
        l0 += sB1[12]; l1 += sB1[13]; l2 += sB0[14]; PIN(l0); PIN(l1); PIN(l2);
        pk3[2] = cvtpk(sB1[12], sB1[13]); PIN(pk3);
        SBAR();
        sA1 = MFMA(kf[15], qr[7], sA1); PIN(sA1);
        SBAR();
        l0 += sB1[14]; l1 += sB1[15]; l2 += sB0[15]; PIN(l0); PIN(l1); PIN(l2);
        pk3[3] = cvtpk(sB1[14], sB1[15]); PIN(pk3);
        SBAR();
        vf[5] = lds128(vbase + 37888); MEMF(); vf[4] = lds128(vbase + 36864);
        o[0] = MFMA(vf[0], __builtin_bit_cast(bf16x8, pk0), o[0]); PIN(o[0]);
        SBAR();
        SBAR();
        o[1] = MFMA(vf[1], __builtin_bit_cast(bf16x8, pk0), o[1]); PIN(o[1]);
        SBAR();
        SBAR();
        if (__builtin_expect((mskb >> (t + 1)) & 1u, 0)) {
            const int tile_ = ((t + 1) >= sw) ? 2 * (t + 1) - sw + (isA ? 1 : 0) : (t + 1);
            cmask(sA0, sA1, (((t + 1) >= sw) ? rbB : rb) * 32 - 64 * tile_, l16); }
        float a_ = max3(sA0[0], sA0[1], sA0[2]), b_ = max3(sA0[3], sA0[4], sA0[5]);
        a_ = max3(a_, sA0[6], sA0[7]); b_ = max3(b_, sA0[8], sA0[9]);
        SBAR();
        vf[7] = lds128(vbase + 39936); MEMF(); vf[6] = lds128(vbase + 38912);
        o[2] = MFMA(vf[2], __builtin_bit_cast(bf16x8, pk0), o[2]); PIN(o[2]);
        SBAR();
        a_ = max3(a_, sA0[10], sA0[11]); b_ = max3(b_, sA0[12], sA0[13]);
        a_ = max3(a_, sA0[14], sA0[15]); b_ = max3(b_, sA1[0], sA1[1]);
        a_ = max3(a_, sA1[2], sA1[3]); b_ = max3(b_, sA1[4], sA1[5]);
        SBAR();
        o[3] = MFMA(vf[3], __builtin_bit_cast(bf16x8, pk0), o[3]); PIN(o[3]);
        SBAR();
        a_ = max3(a_, sA1[6], sA1[7]); b_ = max3(b_, sA1[8], sA1[9]);
        a_ = max3(a_, sA1[10], sA1[11]); b_ = max3(b_, sA1[12], sA1[13]);
        a_ = max3(a_, sA1[14], sA1[15]);
        SBAR();
        vf[9] = lds128(vbase + 41984); MEMF(); vf[8] = lds128(vbase + 40960);
        o[0] = MFMA(vf[4], __builtin_bit_cast(bf16x8, pk1), o[0]); PIN(o[0]);
        SBAR();
        const float rm_ = half_max2(a_, b_);
        if (__builtin_expect(dosw || __any(rm_ > THR), 0)) {
            const float dl_ = __builtin_fmaxf(rm_, dosw ? -1e30f : 0.f); m += dl_; fsc = dosw ? 0.f : EX2(-dl_); LSUM(); LSCALE(fsc); pend = true;
            _Pragma("unroll") for (int e = 0; e < 16; ++e) { sA0[e] -= dl_; sA1[e] -= dl_; }
            bcast16(negm, -m, l16); }
        SBAR();
        o[1] = MFMA(vf[5], __builtin_bit_cast(bf16x8, pk1), o[1]); PIN(o[1]);
        SBAR();
        sA0[0] = EXL(sA0[0]);
        sA0[1] = EXL(sA0[1]);
        PIN(sA0); SBAR();
        vf[11] = lds128(vbase + 44032); MEMF(); vf[10] = lds128(vbase + 43008);
        o[2] = MFMA(vf[6], __builtin_bit_cast(bf16x8, pk1), o[2]); PIN(o[2]);
        SBAR();
        l0 += sA0[0]; PIN(l0);
        sA0[2] = EXL(sA0[2]);
        PIN(sA0); SBAR();
        o[3] = MFMA(vf[7], __builtin_bit_cast(bf16x8, pk1), o[3]); PIN(o[3]);
        SBAR();
        sA0[3] = EXL(sA0[3]);
        sA0[4] = EXL(sA0[4]);
        PIN(sA0); SBAR();
        vf[13] = lds128(vbase + 46080); MEMF(); vf[12] = lds128(vbase + 45056);
        o[0] = MFMA(vf[8], __builtin_bit_cast(bf16x8, pk2), o[0]); PIN(o[0]);
        SBAR();
        SETUP_KV((t + 1));
        l1 += sA0[1]; PIN(l1);
        sA0[5] = EXL(sA0[5]);
        PIN(sA0); SBAR();
        o[1] = MFMA(vf[9], __builtin_bit_cast(bf16x8, pk2), o[1]); PIN(o[1]);
        SBAR();
        sA0[6] = EXL(sA0[6]);
        sA0[7] = EXL(sA0[7]);
        PIN(sA0); SBAR();
        vf[15] = lds128(vbase + 48128); MEMF(); vf[14] = lds128(vbase + 47104);
        o[2] = MFMA(vf[10], __builtin_bit_cast(bf16x8, pk2), o[2]); PIN(o[2]);
        SBAR();
        l0 += sA0[2]; PIN(l0);
        sA0[8] = EXL(sA0[8]);
        PIN(sA0); SBAR();
        o[3] = MFMA(vf[11], __builtin_bit_cast(bf16x8, pk2), o[3]); PIN(o[3]);
        SBAR();
        sA0[9] = EXL(sA0[9]);
        sA0[10] = EXL(sA0[10]);
        PIN(sA0); SBAR();
        o[0] = MFMA(vf[12], __builtin_bit_cast(bf16x8, pk3), o[0]); PIN(o[0]);
        SBAR();
        l1 += sA0[3]; PIN(l1);
        sA0[11] = EXL(sA0[11]);
        PIN(sA0); SBAR();
        o[1] = MFMA(vf[13], __builtin_bit_cast(bf16x8, pk3), o[1]); PIN(o[1]);
        SBAR();
        sA0[12] = EXL(sA0[12]);
        sA0[13] = EXL(sA0[13]);
        PIN(sA0); SBAR();
        STEPBAR();
        kbase += ((t + 1) + 1 == swA) ? 16384 : 0; PIN(kbase);
        { kf[1] = lds128(kbase + 40960); MEMF(); kf[0] = lds128(kbase + 32768); MEMF(); kf[3] = lds128(kbase + 41984); MEMF(); kf[2] = lds128(kbase + 33792); PIECE(0, 0); }
        SBAR();
        o[2] = MFMA(vf[14], __builtin_bit_cast(bf16x8, pk3), o[2]); PIN(o[2]);
        SBAR();
        PIECE(1, 0);
        l0 += sA0[4]; PIN(l0);
        l1 += sA0[5]; PIN(l1);
        l0 += sA0[6]; PIN(l0);
        sA0[14] = EXL(sA0[14]);
        PIN(sA0); SBAR();
        o[3] = MFMA(vf[15], __builtin_bit_cast(bf16x8, pk3), o[3]); PIN(o[3]);
        SBAR();
        l1 += sA0[7]; PIN(l1);
        sA0[15] = EXL(sA0[15]);
        PIN(sA0); SBAR();
        if (__builtin_expect(pend, 0)) {
            if (dosw) { const float il_ = __builtin_amdgcn_rcpf(half_sum(lfin)); const unsigned lo_ = opq(l16);
                _Pragma("unroll") for (int d = 0; d < 4; ++d) { const f32x16 t_ = o[d] * il_;
                    st16(wsr, lo_, ctxA + (2 * d) * 1024, pack8(t_, 0)); st16(wsr, lo_, ctxA + (2 * d + 1) * 1024, pack8(t_, 1)); } }
            _Pragma("unroll") for (int d = 0; d < 4; ++d) o[d] *= fsc;
            pend = false; }
        SBAR();
        }
    }
    const int fq4 = (b & 7) >> 1, g128 = (b & 1) * 8 + ((b >> 3) & 7), fcg = b >> 6;
    const int jp = g128 < 8 ? g128 : 15 - g128;
    const int fk0 = fq4 == 3 ? 0 : (fq4 == 0 ? 0 : 1 + 85 * fq4), fkn = (fq4 == 0 || fq4 == 3) ? 86 : 85;
    unsigned pvoff_c;
    { const int lq = (int)(opq(l16) >> 4); int hp = fq4 == 3 ? (lq & 31) : (fk0 >> 3) + (lq & 15); const int hmax = fq4 == 3 ? 28 : (fk0 + fkn - 1) >> 3; hp = hp > hmax ? hmax : hp;
      pvoff_c = (unsigned)((hp >> 2) + 8 * ((hp & 3) + 4 * jp)) * 128u; }
    const unsigned cflag = (fq4 == 3 ? F_H : g128 < 8 ? F_CA : F_CB) * 32768;
    {
        ldsp vp = vbase + ((17 == swA + 1) ? 16384 : 0);
#pragma unroll
        for (int i = 0; i < 16; ++i) vf[i] = lds128(vp + i * 1024);
#pragma unroll
        for (int e = 0; e < 16; ++e) sA1[e] = EX2(sA1[e]);
#pragma unroll
        for (int e = 0; e < 16; e += 2) { l0 += sA1[e]; l1 += sA1[e + 1]; l2 += sA0[8 + (e >> 1)]; }
        pk0 = pack8(sA0, 0); pk1 = pack8(sA0, 1); pk2 = pack8(sA1, 0); pk3 = pack8(sA1, 1);
#pragma unroll
        for (int i = 0; i < 16; ++i) o[i & 3] = MFMA(vf[i], PKF(i), o[i & 3]);
    }
    {
        const float l = half_sum((l0 + l1) + l2);
        LAS char* xo = (LAS char*)lds + (isA ? 32768 : 65536) + wid * 8192 + l16;
        LAS float* xs = (LAS float*)((LAS char*)lds + LDS_QB) + wid * 128;
#pragma unroll
        for (int d = 0; d < 2; ++d)
#pragma unroll
            for (int g = 0; g < 4; ++g) { const f32x16& s = isA ? o[d] : o[2 + d]; *(LAS f32x4*)(xo + (d * 4 + g) * 1024) = (f32x4){s[4 * g], s[4 * g + 1], s[4 * g + 2], s[4 * g + 3]}; }
        xs[lane] = m; xs[64 + lane] = l;
        if (tid == 0) *(LAS unsigned*)((LAS char*)lds + LDS_QB + 4096) = 0u;
        asm volatile("s_waitcnt lgkmcnt(0)\n\ts_barrier" ::: "memory");
        const int pw = wid ^ 4;
        LAS const char* po = (LAS const char*)lds + (isA ? 65536 : 32768) + pw * 8192 + l16;
        LAS const float* ps = (LAS const float*)((LAS char*)lds + LDS_QB) + pw * 128;
        const float m2 = ps[lane], l2 = ps[64 + lane];
        const float mm = __builtin_fmaxf(m, m2), f1 = EX2(m - mm), f2 = EX2(m2 - mm);
        const float il = __builtin_amdgcn_rcpf(l * f1 + l2 * f2), g1 = f1 * il, g2 = f2 * il;
        const unsigned cb = ctxB + (isA ? 4096 : 0);
#pragma unroll
        for (int d = 0; d < 2; ++d) {
            f32x16 s = isA ? o[2 + d] : o[d];
#pragma unroll
            for (int g = 0; g < 4; ++g) { const f32x4 v = *(LAS const f32x4*)(po + (d * 4 + g) * 1024);
#pragma unroll
                for (int e = 0; e < 4; ++e) s[4 * g + e] = s[4 * g + e] * g1 + v[e] * g2; }
            st16(wsr, l16, cb + (2 * d) * 1024, pack8(s, 0));
            st16(wsr, l16, cb + (2 * d + 1) * 1024, pack8(s, 1));
        }
    }
    SBAR();
    wait_vm_bar<4>();
    {
    const u64 fab = (u64)(uintptr_t)(ws + (fq4 == 3 ? WS_H + (g128 * 4 + (wid >> 1)) * (NFU * 1024) : WS_CTX + (g128 * 4 + (wid >> 1)) * 262144 + fk0 * 1024));
    const u32x4 fasrd = {(unsigned)fab, (unsigned)(fab >> 32), (unsigned)fkn * 1024u, 0x00020000u};
    const u64 fwb = (u64)(uintptr_t)(fq4 == 3 ? wd : wo) + fk0 * 16384;
    const u32x4 fwsrd = {(unsigned)fwb, (unsigned)(fwb >> 32), (unsigned)((fq4 == 3 ? FF : NH * HD) - fk0 * 16) * 1024u, 0x00020000u};
    unsigned favo = l16 + (wid & 1) * 2048;
    unsigned fwvo = (8 * wid + ((lane >> 2) & 7)) * 1024 + fcg * 256 + hh * 64 + ((lane & 3) ^ ((lane >> 3) & 2)) * 16;
    const unsigned fla = lds0 + wid * 2048, flw = lds0 + 16384 + wid * 2048;
    const int fwr_ = wid & 1, fwc = (wid >> 1) & 1, fwk = wid >> 2;
    const int g4 = (int)(opq(l16) >> 8);
    ldsp far[2], fwr0[2], fwr1[2];
    far[0] = (ldsp)lds + fwr_ * 8192 + fwk * 2048 + (g4 >> 1) * 1024 + (g4 & 1) * 512 + (lane & 15) * 16;
    fwr0[0] = (ldsp)lds + 16384 + fwk * 8192 + (g4 >> 1) * 4096 + fwc * 1024 + (4 * (g4 & 1) + ((lane >> 2) & 3)) * 64 + (g4 & 1) * 32 + (lane & 3) * 8;
    fwr1[0] = (ldsp)lds + 16384 + fwk * 8192 + (g4 >> 1) * 4096 + fwc * 1024 + (4 * (g4 & 1) + ((lane >> 2) & 3)) * 64 + (1 - (g4 & 1)) * 32 + (lane & 3) * 8;
    far[1] = far[0] + 65536; fwr0[1] = fwr0[0] + 65536; fwr1[1] = fwr1[0] + 65536; PIN(far[1]); PIN(fwr0[1]); PIN(fwr1[1]);
    bf16x8 fq[2][4]; s16x4 fml[2][4], fmh[2][4];
    f32x4 facc[4][4];
#pragma unroll
    for (int i = 0; i < 16; ++i) facc[i >> 2][i & 3] = f32x4{};
    LAS unsigned* cnt = (LAS unsigned*)((LAS char*)lds + LDS_QB + 4096);
    pv = poll_issue(fbase + cflag, pvoff_c);
    dmaw<0>(fwvo, fwsrd, flw + 0);
    dmaw<0>(fwvo + 128u, fwsrd, flw + 1024); fwvo += 65536u;
    dmaw<0>(fwvo, fwsrd, flw + 32768);
    dmaw<0>(fwvo + 128u, fwsrd, flw + 33792); fwvo += 65536u;
    dmaw<0>(fwvo, fwsrd, flw + 65536);
    dmaw<0>(fwvo + 128u, fwsrd, flw + 66560); fwvo += 65536u;
    dmaw<0>(fwvo, fwsrd, flw + 98304);
    dmaw<0>(fwvo + 128u, fwsrd, flw + 99328); fwvo += 65536u;
    SBAR();
    wait_vm<8>();
    { unsigned old = 0;
      if (lane == 0) old = __hip_atomic_fetch_add(cnt, 1u, __ATOMIC_RELAXED, __HIP_MEMORY_SCOPE_WORKGROUP);
      if (__builtin_amdgcn_readfirstlane(old) == 7u) {
          st_flag(wsr, WS_FLAG + F_CB * 32768 + b * 128, tag);
          if (sw == 16) st_flag(wsr, WS_FLAG + F_CA * 32768 + b * 128, tag);
      } }
    poll_check(pv, fbase + cflag, pvoff_c, tag);
    dmab<0>(favo, fasrd, fla + 0);
    dmab<1024>(favo, fasrd, fla + 0); favo += 4096u;
    dmab<0>(favo, fasrd, fla + 32768);
    dmab<1024>(favo, fasrd, fla + 32768); favo += 4096u;
    dmab<0>(favo, fasrd, fla + 65536);
    dmab<1024>(favo, fasrd, fla + 65536); favo += 4096u;
    dmab<0>(favo, fasrd, fla + 98304);
    dmab<1024>(favo, fasrd, fla + 98304); favo += 4096u;
    SBAR();
    wait_vm_bar<6>();
    fml[0][0] = ldstr(fwr0[0] + 0 + 0);
    fmh[0][0] = ldstr(fwr0[0] + 0 + 2048);
    fq[0][0] = lds128(far[0] + 0 + 0);
    fml[0][1] = ldstr(fwr1[0] + 0 + 0);
    fmh[0][1] = ldstr(fwr1[0] + 0 + 2048);
    fq[0][1] = lds128(far[0] + 0 + 256);
    fml[0][2] = ldstr(fwr0[0] + 0 + 512);
    fmh[0][2] = ldstr(fwr0[0] + 0 + 2560);
    fq[0][2] = lds128(far[0] + 0 + 4096);
    fml[0][3] = ldstr(fwr1[0] + 0 + 512);
    fmh[0][3] = ldstr(fwr1[0] + 0 + 2560);
    fq[0][3] = lds128(far[0] + 0 + 4352);
    SBAR();
    wait_vm_bar<4>();
    fml[1][0] = ldstr(fwr0[0] + 32768 + 0); fmh[1][0] = ldstr(fwr0[0] + 32768 + 2048);
    facc[0][0] = MFMA16(cat8(fml[0][0], fmh[0][0]), fq[0][0], facc[0][0]); PIN(facc[0][0]);
    facc[0][1] = MFMA16(cat8(fml[0][1], fmh[0][1]), fq[0][0], facc[0][1]); PIN(facc[0][1]);
    dmaw<0>(fwvo, fwsrd, flw + 0);
    SBAR();
    fq[1][0] = lds128(far[0] + 32768 + 0); fml[1][1] = ldstr(fwr1[0] + 32768 + 0);
    facc[1][0] = MFMA16(cat8(fml[0][0], fmh[0][0]), fq[0][1], facc[1][0]); PIN(facc[1][0]);
    facc[1][1] = MFMA16(cat8(fml[0][1], fmh[0][1]), fq[0][1], facc[1][1]); PIN(facc[1][1]);
    dmaw<0>(fwvo + 128u, fwsrd, flw + 1024); fwvo += 65536u;
    SBAR();
    fmh[1][1] = ldstr(fwr1[0] + 32768 + 2048); fq[1][1] = lds128(far[0] + 32768 + 256);
    facc[0][2] = MFMA16(cat8(fml[0][2], fmh[0][2]), fq[0][0], facc[0][2]); PIN(facc[0][2]);
    facc[1][2] = MFMA16(cat8(fml[0][2], fmh[0][2]), fq[0][1], facc[1][2]); PIN(facc[1][2]);
    dmab<0>(favo, fasrd, fla + 0);
    SBAR();
    fml[1][2] = ldstr(fwr0[0] + 32768 + 512); fmh[1][2] = ldstr(fwr0[0] + 32768 + 2560);
    facc[2][0] = MFMA16(cat8(fml[0][0], fmh[0][0]), fq[0][2], facc[2][0]); PIN(facc[2][0]);
    facc[2][1] = MFMA16(cat8(fml[0][1], fmh[0][1]), fq[0][2], facc[2][1]); PIN(facc[2][1]);
    dmab<1024>(favo, fasrd, fla + 0); favo += 4096u;
    SBAR();
    fq[1][2] = lds128(far[0] + 32768 + 4096); fml[1][3] = ldstr(fwr1[0] + 32768 + 512);
    facc[2][2] = MFMA16(cat8(fml[0][2], fmh[0][2]), fq[0][2], facc[2][2]); PIN(facc[2][2]);
    facc[0][3] = MFMA16(cat8(fml[0][3], fmh[0][3]), fq[0][0], facc[0][3]); PIN(facc[0][3]);
    SBAR();
    fmh[1][3] = ldstr(fwr1[0] + 32768 + 2560); fq[1][3] = lds128(far[0] + 32768 + 4352);
    facc[1][3] = MFMA16(cat8(fml[0][3], fmh[0][3]), fq[0][1], facc[1][3]); PIN(facc[1][3]);
    facc[2][3] = MFMA16(cat8(fml[0][3], fmh[0][3]), fq[0][2], facc[2][3]); PIN(facc[2][3]);
    SBAR();
    facc[3][0] = MFMA16(cat8(fml[0][0], fmh[0][0]), fq[0][3], facc[3][0]); PIN(facc[3][0]);
    facc[3][1] = MFMA16(cat8(fml[0][1], fmh[0][1]), fq[0][3], facc[3][1]); PIN(facc[3][1]);
    SBAR();
    facc[3][2] = MFMA16(cat8(fml[0][2], fmh[0][2]), fq[0][3], facc[3][2]); PIN(facc[3][2]);
    facc[3][3] = MFMA16(cat8(fml[0][3], fmh[0][3]), fq[0][3], facc[3][3]); PIN(facc[3][3]);
    SBAR();
    wait_vm_bar<6>();
    fml[0][0] = ldstr(fwr0[1] + 0 + 0); fmh[0][0] = ldstr(fwr0[1] + 0 + 2048);
    facc[0][0] = MFMA16(cat8(fml[1][0], fmh[1][0]), fq[1][0], facc[0][0]); PIN(facc[0][0]);
    facc[0][1] = MFMA16(cat8(fml[1][1], fmh[1][1]), fq[1][0], facc[0][1]); PIN(facc[0][1]);
    dmaw<0>(fwvo, fwsrd, flw + 32768);
    SBAR();
    fq[0][0] = lds128(far[1] + 0 + 0); fml[0][1] = ldstr(fwr1[1] + 0 + 0);
    facc[1][0] = MFMA16(cat8(fml[1][0], fmh[1][0]), fq[1][1], facc[1][0]); PIN(facc[1][0]);
    facc[1][1] = MFMA16(cat8(fml[1][1], fmh[1][1]), fq[1][1], facc[1][1]); PIN(facc[1][1]);
    dmaw<0>(fwvo + 128u, fwsrd, flw + 33792); fwvo += 65536u;
    SBAR();
    fmh[0][1] = ldstr(fwr1[1] + 0 + 2048); fq[0][1] = lds128(far[1] + 0 + 256);
    facc[0][2] = MFMA16(cat8(fml[1][2], fmh[1][2]), fq[1][0], facc[0][2]); PIN(facc[0][2]);
    facc[1][2] = MFMA16(cat8(fml[1][2], fmh[1][2]), fq[1][1], facc[1][2]); PIN(facc[1][2]);
    dmab<0>(favo, fasrd, fla + 32768);
    SBAR();
    fml[0][2] = ldstr(fwr0[1] + 0 + 512); fmh[0][2] = ldstr(fwr0[1] + 0 + 2560);
    facc[2][0] = MFMA16(cat8(fml[1][0], fmh[1][0]), fq[1][2], facc[2][0]); PIN(facc[2][0]);
    facc[2][1] = MFMA16(cat8(fml[1][1], fmh[1][1]), fq[1][2], facc[2][1]); PIN(facc[2][1]);
    dmab<1024>(favo, fasrd, fla + 32768); favo += 4096u;
    SBAR();
    fq[0][2] = lds128(far[1] + 0 + 4096); fml[0][3] = ldstr(fwr1[1] + 0 + 512);
    facc[2][2] = MFMA16(cat8(fml[1][2], fmh[1][2]), fq[1][2], facc[2][2]); PIN(facc[2][2]);
    facc[0][3] = MFMA16(cat8(fml[1][3], fmh[1][3]), fq[1][0], facc[0][3]); PIN(facc[0][3]);
    SBAR();
    fmh[0][3] = ldstr(fwr1[1] + 0 + 2560); fq[0][3] = lds128(far[1] + 0 + 4352);
    facc[1][3] = MFMA16(cat8(fml[1][3], fmh[1][3]), fq[1][1], facc[1][3]); PIN(facc[1][3]);
    facc[2][3] = MFMA16(cat8(fml[1][3], fmh[1][3]), fq[1][2], facc[2][3]); PIN(facc[2][3]);
    SBAR();
    facc[3][0] = MFMA16(cat8(fml[1][0], fmh[1][0]), fq[1][3], facc[3][0]); PIN(facc[3][0]);
    facc[3][1] = MFMA16(cat8(fml[1][1], fmh[1][1]), fq[1][3], facc[3][1]); PIN(facc[3][1]);
    SBAR();
    facc[3][2] = MFMA16(cat8(fml[1][2], fmh[1][2]), fq[1][3], facc[3][2]); PIN(facc[3][2]);
    facc[3][3] = MFMA16(cat8(fml[1][3], fmh[1][3]), fq[1][3], facc[3][3]); PIN(facc[3][3]);
    SBAR();
    wait_vm_bar<8>();
    fml[1][0] = ldstr(fwr0[1] + 32768 + 0); fmh[1][0] = ldstr(fwr0[1] + 32768 + 2048);
    facc[0][0] = MFMA16(cat8(fml[0][0], fmh[0][0]), fq[0][0], facc[0][0]); PIN(facc[0][0]);
    facc[0][1] = MFMA16(cat8(fml[0][1], fmh[0][1]), fq[0][0], facc[0][1]); PIN(facc[0][1]);
    dmaw<0>(fwvo, fwsrd, flw + 65536);
    SBAR();
    fq[1][0] = lds128(far[1] + 32768 + 0); fml[1][1] = ldstr(fwr1[1] + 32768 + 0);
    facc[1][0] = MFMA16(cat8(fml[0][0], fmh[0][0]), fq[0][1], facc[1][0]); PIN(facc[1][0]);
    facc[1][1] = MFMA16(cat8(fml[0][1], fmh[0][1]), fq[0][1], facc[1][1]); PIN(facc[1][1]);
    dmaw<0>(fwvo + 128u, fwsrd, flw + 66560); fwvo += 65536u;
    SBAR();
    fmh[1][1] = ldstr(fwr1[1] + 32768 + 2048); fq[1][1] = lds128(far[1] + 32768 + 256);
    facc[0][2] = MFMA16(cat8(fml[0][2], fmh[0][2]), fq[0][0], facc[0][2]); PIN(facc[0][2]);
    facc[1][2] = MFMA16(cat8(fml[0][2], fmh[0][2]), fq[0][1], facc[1][2]); PIN(facc[1][2]);
    dmab<0>(favo, fasrd, fla + 65536);
    SBAR();
    fml[1][2] = ldstr(fwr0[1] + 32768 + 512); fmh[1][2] = ldstr(fwr0[1] + 32768 + 2560);
    facc[2][0] = MFMA16(cat8(fml[0][0], fmh[0][0]), fq[0][2], facc[2][0]); PIN(facc[2][0]);
    facc[2][1] = MFMA16(cat8(fml[0][1], fmh[0][1]), fq[0][2], facc[2][1]); PIN(facc[2][1]);
    dmab<1024>(favo, fasrd, fla + 65536); favo += 4096u;
    SBAR();
    fq[1][2] = lds128(far[1] + 32768 + 4096); fml[1][3] = ldstr(fwr1[1] + 32768 + 512);
    facc[2][2] = MFMA16(cat8(fml[0][2], fmh[0][2]), fq[0][2], facc[2][2]); PIN(facc[2][2]);
    facc[0][3] = MFMA16(cat8(fml[0][3], fmh[0][3]), fq[0][0], facc[0][3]); PIN(facc[0][3]);
    SBAR();
    fmh[1][3] = ldstr(fwr1[1] + 32768 + 2560); fq[1][3] = lds128(far[1] + 32768 + 4352);
    facc[1][3] = MFMA16(cat8(fml[0][3], fmh[0][3]), fq[0][1], facc[1][3]); PIN(facc[1][3]);
    facc[2][3] = MFMA16(cat8(fml[0][3], fmh[0][3]), fq[0][2], facc[2][3]); PIN(facc[2][3]);
    SBAR();
    facc[3][0] = MFMA16(cat8(fml[0][0], fmh[0][0]), fq[0][3], facc[3][0]); PIN(facc[3][0]);
    facc[3][1] = MFMA16(cat8(fml[0][1], fmh[0][1]), fq[0][3], facc[3][1]); PIN(facc[3][1]);
    SBAR();
    facc[3][2] = MFMA16(cat8(fml[0][2], fmh[0][2]), fq[0][3], facc[3][2]); PIN(facc[3][2]);
    facc[3][3] = MFMA16(cat8(fml[0][3], fmh[0][3]), fq[0][3], facc[3][3]); PIN(facc[3][3]);
    SBAR();
    wait_vm_bar<8>();
    fml[0][0] = ldstr(fwr0[0] + 0 + 0); fmh[0][0] = ldstr(fwr0[0] + 0 + 2048);
    facc[0][0] = MFMA16(cat8(fml[1][0], fmh[1][0]), fq[1][0], facc[0][0]); PIN(facc[0][0]);
    facc[0][1] = MFMA16(cat8(fml[1][1], fmh[1][1]), fq[1][0], facc[0][1]); PIN(facc[0][1]);
    dmaw<0>(fwvo, fwsrd, flw + 98304);
    SBAR();
    fq[0][0] = lds128(far[0] + 0 + 0); fml[0][1] = ldstr(fwr1[0] + 0 + 0);
    facc[1][0] = MFMA16(cat8(fml[1][0], fmh[1][0]), fq[1][1], facc[1][0]); PIN(facc[1][0]);
    facc[1][1] = MFMA16(cat8(fml[1][1], fmh[1][1]), fq[1][1], facc[1][1]); PIN(facc[1][1]);
    dmaw<0>(fwvo + 128u, fwsrd, flw + 99328); fwvo += 65536u;
    SBAR();
    fmh[0][1] = ldstr(fwr1[0] + 0 + 2048); fq[0][1] = lds128(far[0] + 0 + 256);
    facc[0][2] = MFMA16(cat8(fml[1][2], fmh[1][2]), fq[1][0], facc[0][2]); PIN(facc[0][2]);
    facc[1][2] = MFMA16(cat8(fml[1][2], fmh[1][2]), fq[1][1], facc[1][2]); PIN(facc[1][2]);
    dmab<0>(favo, fasrd, fla + 98304);
    SBAR();
    fml[0][2] = ldstr(fwr0[0] + 0 + 512); fmh[0][2] = ldstr(fwr0[0] + 0 + 2560);
    facc[2][0] = MFMA16(cat8(fml[1][0], fmh[1][0]), fq[1][2], facc[2][0]); PIN(facc[2][0]);
    facc[2][1] = MFMA16(cat8(fml[1][1], fmh[1][1]), fq[1][2], facc[2][1]); PIN(facc[2][1]);
    dmab<1024>(favo, fasrd, fla + 98304); favo += 4096u;
    SBAR();
    fq[0][2] = lds128(far[0] + 0 + 4096); fml[0][3] = ldstr(fwr1[0] + 0 + 512);
    facc[2][2] = MFMA16(cat8(fml[1][2], fmh[1][2]), fq[1][2], facc[2][2]); PIN(facc[2][2]);
    facc[0][3] = MFMA16(cat8(fml[1][3], fmh[1][3]), fq[1][0], facc[0][3]); PIN(facc[0][3]);
    SBAR();
    fmh[0][3] = ldstr(fwr1[0] + 0 + 2560); fq[0][3] = lds128(far[0] + 0 + 4352);
    facc[1][3] = MFMA16(cat8(fml[1][3], fmh[1][3]), fq[1][1], facc[1][3]); PIN(facc[1][3]);
    facc[2][3] = MFMA16(cat8(fml[1][3], fmh[1][3]), fq[1][2], facc[2][3]); PIN(facc[2][3]);
    SBAR();
    facc[3][0] = MFMA16(cat8(fml[1][0], fmh[1][0]), fq[1][3], facc[3][0]); PIN(facc[3][0]);
    facc[3][1] = MFMA16(cat8(fml[1][1], fmh[1][1]), fq[1][3], facc[3][1]); PIN(facc[3][1]);
    SBAR();
    facc[3][2] = MFMA16(cat8(fml[1][2], fmh[1][2]), fq[1][3], facc[3][2]); PIN(facc[3][2]);
    facc[3][3] = MFMA16(cat8(fml[1][3], fmh[1][3]), fq[1][3], facc[3][3]); PIN(facc[3][3]);
    SBAR();
    wait_vm_bar<8>();
    fml[1][0] = ldstr(fwr0[0] + 32768 + 0); fmh[1][0] = ldstr(fwr0[0] + 32768 + 2048);
    facc[0][0] = MFMA16(cat8(fml[0][0], fmh[0][0]), fq[0][0], facc[0][0]); PIN(facc[0][0]);
    facc[0][1] = MFMA16(cat8(fml[0][1], fmh[0][1]), fq[0][0], facc[0][1]); PIN(facc[0][1]);
    dmaw<0>(fwvo, fwsrd, flw + 0);
    SBAR();
    fq[1][0] = lds128(far[0] + 32768 + 0); fml[1][1] = ldstr(fwr1[0] + 32768 + 0);
    facc[1][0] = MFMA16(cat8(fml[0][0], fmh[0][0]), fq[0][1], facc[1][0]); PIN(facc[1][0]);
    facc[1][1] = MFMA16(cat8(fml[0][1], fmh[0][1]), fq[0][1], facc[1][1]); PIN(facc[1][1]);
    dmaw<0>(fwvo + 128u, fwsrd, flw + 1024); fwvo += 65536u;
    SBAR();
    fmh[1][1] = ldstr(fwr1[0] + 32768 + 2048); fq[1][1] = lds128(far[0] + 32768 + 256);
    facc[0][2] = MFMA16(cat8(fml[0][2], fmh[0][2]), fq[0][0], facc[0][2]); PIN(facc[0][2]);
    facc[1][2] = MFMA16(cat8(fml[0][2], fmh[0][2]), fq[0][1], facc[1][2]); PIN(facc[1][2]);
    dmab<0>(favo, fasrd, fla + 0);
    SBAR();
    fml[1][2] = ldstr(fwr0[0] + 32768 + 512); fmh[1][2] = ldstr(fwr0[0] + 32768 + 2560);
    facc[2][0] = MFMA16(cat8(fml[0][0], fmh[0][0]), fq[0][2], facc[2][0]); PIN(facc[2][0]);
    facc[2][1] = MFMA16(cat8(fml[0][1], fmh[0][1]), fq[0][2], facc[2][1]); PIN(facc[2][1]);
    dmab<1024>(favo, fasrd, fla + 0); favo += 4096u;
    SBAR();
    fq[1][2] = lds128(far[0] + 32768 + 4096); fml[1][3] = ldstr(fwr1[0] + 32768 + 512);
    facc[2][2] = MFMA16(cat8(fml[0][2], fmh[0][2]), fq[0][2], facc[2][2]); PIN(facc[2][2]);
    facc[0][3] = MFMA16(cat8(fml[0][3], fmh[0][3]), fq[0][0], facc[0][3]); PIN(facc[0][3]);
    SBAR();
    fmh[1][3] = ldstr(fwr1[0] + 32768 + 2560); fq[1][3] = lds128(far[0] + 32768 + 4352);
    facc[1][3] = MFMA16(cat8(fml[0][3], fmh[0][3]), fq[0][1], facc[1][3]); PIN(facc[1][3]);
    facc[2][3] = MFMA16(cat8(fml[0][3], fmh[0][3]), fq[0][2], facc[2][3]); PIN(facc[2][3]);
    SBAR();
    facc[3][0] = MFMA16(cat8(fml[0][0], fmh[0][0]), fq[0][3], facc[3][0]); PIN(facc[3][0]);
    facc[3][1] = MFMA16(cat8(fml[0][1], fmh[0][1]), fq[0][3], facc[3][1]); PIN(facc[3][1]);
    SBAR();
    facc[3][2] = MFMA16(cat8(fml[0][2], fmh[0][2]), fq[0][3], facc[3][2]); PIN(facc[3][2]);
    facc[3][3] = MFMA16(cat8(fml[0][3], fmh[0][3]), fq[0][3], facc[3][3]); PIN(facc[3][3]);
    SBAR();
    wait_vm_bar<8>();
    fml[0][0] = ldstr(fwr0[1] + 0 + 0); fmh[0][0] = ldstr(fwr0[1] + 0 + 2048);
    facc[0][0] = MFMA16(cat8(fml[1][0], fmh[1][0]), fq[1][0], facc[0][0]); PIN(facc[0][0]);
    facc[0][1] = MFMA16(cat8(fml[1][1], fmh[1][1]), fq[1][0], facc[0][1]); PIN(facc[0][1]);
    dmaw<0>(fwvo, fwsrd, flw + 32768);
    SBAR();
    fq[0][0] = lds128(far[1] + 0 + 0); fml[0][1] = ldstr(fwr1[1] + 0 + 0);
    facc[1][0] = MFMA16(cat8(fml[1][0], fmh[1][0]), fq[1][1], facc[1][0]); PIN(facc[1][0]);
    facc[1][1] = MFMA16(cat8(fml[1][1], fmh[1][1]), fq[1][1], facc[1][1]); PIN(facc[1][1]);
    dmaw<0>(fwvo + 128u, fwsrd, flw + 33792); fwvo += 65536u;
    SBAR();
    fmh[0][1] = ldstr(fwr1[1] + 0 + 2048); fq[0][1] = lds128(far[1] + 0 + 256);
    facc[0][2] = MFMA16(cat8(fml[1][2], fmh[1][2]), fq[1][0], facc[0][2]); PIN(facc[0][2]);
    facc[1][2] = MFMA16(cat8(fml[1][2], fmh[1][2]), fq[1][1], facc[1][2]); PIN(facc[1][2]);
    dmab<0>(favo, fasrd, fla + 32768);
    SBAR();
    fml[0][2] = ldstr(fwr0[1] + 0 + 512); fmh[0][2] = ldstr(fwr0[1] + 0 + 2560);
    facc[2][0] = MFMA16(cat8(fml[1][0], fmh[1][0]), fq[1][2], facc[2][0]); PIN(facc[2][0]);
    facc[2][1] = MFMA16(cat8(fml[1][1], fmh[1][1]), fq[1][2], facc[2][1]); PIN(facc[2][1]);
    dmab<1024>(favo, fasrd, fla + 32768); favo += 4096u;
    SBAR();
    fq[0][2] = lds128(far[1] + 0 + 4096); fml[0][3] = ldstr(fwr1[1] + 0 + 512);
    facc[2][2] = MFMA16(cat8(fml[1][2], fmh[1][2]), fq[1][2], facc[2][2]); PIN(facc[2][2]);
    facc[0][3] = MFMA16(cat8(fml[1][3], fmh[1][3]), fq[1][0], facc[0][3]); PIN(facc[0][3]);
    SBAR();
    fmh[0][3] = ldstr(fwr1[1] + 0 + 2560); fq[0][3] = lds128(far[1] + 0 + 4352);
    facc[1][3] = MFMA16(cat8(fml[1][3], fmh[1][3]), fq[1][1], facc[1][3]); PIN(facc[1][3]);
    facc[2][3] = MFMA16(cat8(fml[1][3], fmh[1][3]), fq[1][2], facc[2][3]); PIN(facc[2][3]);
    SBAR();
    facc[3][0] = MFMA16(cat8(fml[1][0], fmh[1][0]), fq[1][3], facc[3][0]); PIN(facc[3][0]);
    facc[3][1] = MFMA16(cat8(fml[1][1], fmh[1][1]), fq[1][3], facc[3][1]); PIN(facc[3][1]);
    SBAR();
    facc[3][2] = MFMA16(cat8(fml[1][2], fmh[1][2]), fq[1][3], facc[3][2]); PIN(facc[3][2]);
    facc[3][3] = MFMA16(cat8(fml[1][3], fmh[1][3]), fq[1][3], facc[3][3]); PIN(facc[3][3]);
    SBAR();
    wait_vm_bar<8>();
    fml[1][0] = ldstr(fwr0[1] + 32768 + 0); fmh[1][0] = ldstr(fwr0[1] + 32768 + 2048);
    facc[0][0] = MFMA16(cat8(fml[0][0], fmh[0][0]), fq[0][0], facc[0][0]); PIN(facc[0][0]);
    facc[0][1] = MFMA16(cat8(fml[0][1], fmh[0][1]), fq[0][0], facc[0][1]); PIN(facc[0][1]);
    dmaw<0>(fwvo, fwsrd, flw + 65536);
    SBAR();
    fq[1][0] = lds128(far[1] + 32768 + 0); fml[1][1] = ldstr(fwr1[1] + 32768 + 0);
    facc[1][0] = MFMA16(cat8(fml[0][0], fmh[0][0]), fq[0][1], facc[1][0]); PIN(facc[1][0]);
    facc[1][1] = MFMA16(cat8(fml[0][1], fmh[0][1]), fq[0][1], facc[1][1]); PIN(facc[1][1]);
    dmaw<0>(fwvo + 128u, fwsrd, flw + 66560); fwvo += 65536u;
    SBAR();
    fmh[1][1] = ldstr(fwr1[1] + 32768 + 2048); fq[1][1] = lds128(far[1] + 32768 + 256);
    facc[0][2] = MFMA16(cat8(fml[0][2], fmh[0][2]), fq[0][0], facc[0][2]); PIN(facc[0][2]);
    facc[1][2] = MFMA16(cat8(fml[0][2], fmh[0][2]), fq[0][1], facc[1][2]); PIN(facc[1][2]);
    dmab<0>(favo, fasrd, fla + 65536);
    SBAR();
    fml[1][2] = ldstr(fwr0[1] + 32768 + 512); fmh[1][2] = ldstr(fwr0[1] + 32768 + 2560);
    facc[2][0] = MFMA16(cat8(fml[0][0], fmh[0][0]), fq[0][2], facc[2][0]); PIN(facc[2][0]);
    facc[2][1] = MFMA16(cat8(fml[0][1], fmh[0][1]), fq[0][2], facc[2][1]); PIN(facc[2][1]);
    dmab<1024>(favo, fasrd, fla + 65536); favo += 4096u;
    SBAR();
    fq[1][2] = lds128(far[1] + 32768 + 4096); fml[1][3] = ldstr(fwr1[1] + 32768 + 512);
    facc[2][2] = MFMA16(cat8(fml[0][2], fmh[0][2]), fq[0][2], facc[2][2]); PIN(facc[2][2]);
    facc[0][3] = MFMA16(cat8(fml[0][3], fmh[0][3]), fq[0][0], facc[0][3]); PIN(facc[0][3]);
    SBAR();
    fmh[1][3] = ldstr(fwr1[1] + 32768 + 2560); fq[1][3] = lds128(far[1] + 32768 + 4352);
    facc[1][3] = MFMA16(cat8(fml[0][3], fmh[0][3]), fq[0][1], facc[1][3]); PIN(facc[1][3]);
    facc[2][3] = MFMA16(cat8(fml[0][3], fmh[0][3]), fq[0][2], facc[2][3]); PIN(facc[2][3]);
    SBAR();
    facc[3][0] = MFMA16(cat8(fml[0][0], fmh[0][0]), fq[0][3], facc[3][0]); PIN(facc[3][0]);
    facc[3][1] = MFMA16(cat8(fml[0][1], fmh[0][1]), fq[0][3], facc[3][1]); PIN(facc[3][1]);
    SBAR();
    facc[3][2] = MFMA16(cat8(fml[0][2], fmh[0][2]), fq[0][3], facc[3][2]); PIN(facc[3][2]);
    facc[3][3] = MFMA16(cat8(fml[0][3], fmh[0][3]), fq[0][3], facc[3][3]); PIN(facc[3][3]);
    SBAR();
    wait_vm_bar<8>();
    fml[0][0] = ldstr(fwr0[0] + 0 + 0); fmh[0][0] = ldstr(fwr0[0] + 0 + 2048);
    facc[0][0] = MFMA16(cat8(fml[1][0], fmh[1][0]), fq[1][0], facc[0][0]); PIN(facc[0][0]);
    facc[0][1] = MFMA16(cat8(fml[1][1], fmh[1][1]), fq[1][0], facc[0][1]); PIN(facc[0][1]);
    dmaw<0>(fwvo, fwsrd, flw + 98304);
    SBAR();
    fq[0][0] = lds128(far[0] + 0 + 0); fml[0][1] = ldstr(fwr1[0] + 0 + 0);
    facc[1][0] = MFMA16(cat8(fml[1][0], fmh[1][0]), fq[1][1], facc[1][0]); PIN(facc[1][0]);
    facc[1][1] = MFMA16(cat8(fml[1][1], fmh[1][1]), fq[1][1], facc[1][1]); PIN(facc[1][1]);
    dmaw<0>(fwvo + 128u, fwsrd, flw + 99328); fwvo += 65536u;
    SBAR();
    fmh[0][1] = ldstr(fwr1[0] + 0 + 2048); fq[0][1] = lds128(far[0] + 0 + 256);
    facc[0][2] = MFMA16(cat8(fml[1][2], fmh[1][2]), fq[1][0], facc[0][2]); PIN(facc[0][2]);
    facc[1][2] = MFMA16(cat8(fml[1][2], fmh[1][2]), fq[1][1], facc[1][2]); PIN(facc[1][2]);
    dmab<0>(favo, fasrd, fla + 98304);
    SBAR();
    fml[0][2] = ldstr(fwr0[0] + 0 + 512); fmh[0][2] = ldstr(fwr0[0] + 0 + 2560);
    facc[2][0] = MFMA16(cat8(fml[1][0], fmh[1][0]), fq[1][2], facc[2][0]); PIN(facc[2][0]);
    facc[2][1] = MFMA16(cat8(fml[1][1], fmh[1][1]), fq[1][2], facc[2][1]); PIN(facc[2][1]);
    dmab<1024>(favo, fasrd, fla + 98304); favo += 4096u;
    SBAR();
    fq[0][2] = lds128(far[0] + 0 + 4096); fml[0][3] = ldstr(fwr1[0] + 0 + 512);
    facc[2][2] = MFMA16(cat8(fml[1][2], fmh[1][2]), fq[1][2], facc[2][2]); PIN(facc[2][2]);
    facc[0][3] = MFMA16(cat8(fml[1][3], fmh[1][3]), fq[1][0], facc[0][3]); PIN(facc[0][3]);
    SBAR();
    fmh[0][3] = ldstr(fwr1[0] + 0 + 2560); fq[0][3] = lds128(far[0] + 0 + 4352);
    facc[1][3] = MFMA16(cat8(fml[1][3], fmh[1][3]), fq[1][1], facc[1][3]); PIN(facc[1][3]);
    facc[2][3] = MFMA16(cat8(fml[1][3], fmh[1][3]), fq[1][2], facc[2][3]); PIN(facc[2][3]);
    SBAR();
    facc[3][0] = MFMA16(cat8(fml[1][0], fmh[1][0]), fq[1][3], facc[3][0]); PIN(facc[3][0]);
    facc[3][1] = MFMA16(cat8(fml[1][1], fmh[1][1]), fq[1][3], facc[3][1]); PIN(facc[3][1]);
    SBAR();
    facc[3][2] = MFMA16(cat8(fml[1][2], fmh[1][2]), fq[1][3], facc[3][2]); PIN(facc[3][2]);
    facc[3][3] = MFMA16(cat8(fml[1][3], fmh[1][3]), fq[1][3], facc[3][3]); PIN(facc[3][3]);
    SBAR();
    wait_vm_bar<8>();
    fml[1][0] = ldstr(fwr0[0] + 32768 + 0); fmh[1][0] = ldstr(fwr0[0] + 32768 + 2048);
    facc[0][0] = MFMA16(cat8(fml[0][0], fmh[0][0]), fq[0][0], facc[0][0]); PIN(facc[0][0]);
    facc[0][1] = MFMA16(cat8(fml[0][1], fmh[0][1]), fq[0][0], facc[0][1]); PIN(facc[0][1]);
    dmaw<0>(fwvo, fwsrd, flw + 0);
    SBAR();
    fq[1][0] = lds128(far[0] + 32768 + 0); fml[1][1] = ldstr(fwr1[0] + 32768 + 0);
    facc[1][0] = MFMA16(cat8(fml[0][0], fmh[0][0]), fq[0][1], facc[1][0]); PIN(facc[1][0]);
    facc[1][1] = MFMA16(cat8(fml[0][1], fmh[0][1]), fq[0][1], facc[1][1]); PIN(facc[1][1]);
    dmaw<0>(fwvo + 128u, fwsrd, flw + 1024); fwvo += 65536u;
    SBAR();
    fmh[1][1] = ldstr(fwr1[0] + 32768 + 2048); fq[1][1] = lds128(far[0] + 32768 + 256);
    facc[0][2] = MFMA16(cat8(fml[0][2], fmh[0][2]), fq[0][0], facc[0][2]); PIN(facc[0][2]);
    facc[1][2] = MFMA16(cat8(fml[0][2], fmh[0][2]), fq[0][1], facc[1][2]); PIN(facc[1][2]);
    dmab<0>(favo, fasrd, fla + 0);
    SBAR();
    fml[1][2] = ldstr(fwr0[0] + 32768 + 512); fmh[1][2] = ldstr(fwr0[0] + 32768 + 2560);
    facc[2][0] = MFMA16(cat8(fml[0][0], fmh[0][0]), fq[0][2], facc[2][0]); PIN(facc[2][0]);
    facc[2][1] = MFMA16(cat8(fml[0][1], fmh[0][1]), fq[0][2], facc[2][1]); PIN(facc[2][1]);
    dmab<1024>(favo, fasrd, fla + 0); favo += 4096u;
    SBAR();
    fq[1][2] = lds128(far[0] + 32768 + 4096); fml[1][3] = ldstr(fwr1[0] + 32768 + 512);
    facc[2][2] = MFMA16(cat8(fml[0][2], fmh[0][2]), fq[0][2], facc[2][2]); PIN(facc[2][2]);
    facc[0][3] = MFMA16(cat8(fml[0][3], fmh[0][3]), fq[0][0], facc[0][3]); PIN(facc[0][3]);
    SBAR();
    fmh[1][3] = ldstr(fwr1[0] + 32768 + 2560); fq[1][3] = lds128(far[0] + 32768 + 4352);
    facc[1][3] = MFMA16(cat8(fml[0][3], fmh[0][3]), fq[0][1], facc[1][3]); PIN(facc[1][3]);
    facc[2][3] = MFMA16(cat8(fml[0][3], fmh[0][3]), fq[0][2], facc[2][3]); PIN(facc[2][3]);
    SBAR();
    facc[3][0] = MFMA16(cat8(fml[0][0], fmh[0][0]), fq[0][3], facc[3][0]); PIN(facc[3][0]);
    facc[3][1] = MFMA16(cat8(fml[0][1], fmh[0][1]), fq[0][3], facc[3][1]); PIN(facc[3][1]);
    SBAR();
    facc[3][2] = MFMA16(cat8(fml[0][2], fmh[0][2]), fq[0][3], facc[3][2]); PIN(facc[3][2]);
    facc[3][3] = MFMA16(cat8(fml[0][3], fmh[0][3]), fq[0][3], facc[3][3]); PIN(facc[3][3]);
    SBAR();
    wait_vm_bar<8>();
    fml[0][0] = ldstr(fwr0[1] + 0 + 0); fmh[0][0] = ldstr(fwr0[1] + 0 + 2048);
    facc[0][0] = MFMA16(cat8(fml[1][0], fmh[1][0]), fq[1][0], facc[0][0]); PIN(facc[0][0]);
    facc[0][1] = MFMA16(cat8(fml[1][1], fmh[1][1]), fq[1][0], facc[0][1]); PIN(facc[0][1]);
    dmaw<0>(fwvo, fwsrd, flw + 32768);
    SBAR();
    fq[0][0] = lds128(far[1] + 0 + 0); fml[0][1] = ldstr(fwr1[1] + 0 + 0);
    facc[1][0] = MFMA16(cat8(fml[1][0], fmh[1][0]), fq[1][1], facc[1][0]); PIN(facc[1][0]);
    facc[1][1] = MFMA16(cat8(fml[1][1], fmh[1][1]), fq[1][1], facc[1][1]); PIN(facc[1][1]);
    dmaw<0>(fwvo + 128u, fwsrd, flw + 33792); fwvo += 65536u;
    SBAR();
    fmh[0][1] = ldstr(fwr1[1] + 0 + 2048); fq[0][1] = lds128(far[1] + 0 + 256);
    facc[0][2] = MFMA16(cat8(fml[1][2], fmh[1][2]), fq[1][0], facc[0][2]); PIN(facc[0][2]);
    facc[1][2] = MFMA16(cat8(fml[1][2], fmh[1][2]), fq[1][1], facc[1][2]); PIN(facc[1][2]);
    dmab<0>(favo, fasrd, fla + 32768);
    SBAR();
    fml[0][2] = ldstr(fwr0[1] + 0 + 512); fmh[0][2] = ldstr(fwr0[1] + 0 + 2560);
    facc[2][0] = MFMA16(cat8(fml[1][0], fmh[1][0]), fq[1][2], facc[2][0]); PIN(facc[2][0]);
    facc[2][1] = MFMA16(cat8(fml[1][1], fmh[1][1]), fq[1][2], facc[2][1]); PIN(facc[2][1]);
    dmab<1024>(favo, fasrd, fla + 32768); favo += 4096u;
    SBAR();
    fq[0][2] = lds128(far[1] + 0 + 4096); fml[0][3] = ldstr(fwr1[1] + 0 + 512);
    facc[2][2] = MFMA16(cat8(fml[1][2], fmh[1][2]), fq[1][2], facc[2][2]); PIN(facc[2][2]);
    facc[0][3] = MFMA16(cat8(fml[1][3], fmh[1][3]), fq[1][0], facc[0][3]); PIN(facc[0][3]);
    SBAR();
    fmh[0][3] = ldstr(fwr1[1] + 0 + 2560); fq[0][3] = lds128(far[1] + 0 + 4352);
    facc[1][3] = MFMA16(cat8(fml[1][3], fmh[1][3]), fq[1][1], facc[1][3]); PIN(facc[1][3]);
    facc[2][3] = MFMA16(cat8(fml[1][3], fmh[1][3]), fq[1][2], facc[2][3]); PIN(facc[2][3]);
    SBAR();
    facc[3][0] = MFMA16(cat8(fml[1][0], fmh[1][0]), fq[1][3], facc[3][0]); PIN(facc[3][0]);
    facc[3][1] = MFMA16(cat8(fml[1][1], fmh[1][1]), fq[1][3], facc[3][1]); PIN(facc[3][1]);
    SBAR();
    facc[3][2] = MFMA16(cat8(fml[1][2], fmh[1][2]), fq[1][3], facc[3][2]); PIN(facc[3][2]);
    facc[3][3] = MFMA16(cat8(fml[1][3], fmh[1][3]), fq[1][3], facc[3][3]); PIN(facc[3][3]);
    SBAR();
    wait_vm_bar<8>();
    fml[1][0] = ldstr(fwr0[1] + 32768 + 0); fmh[1][0] = ldstr(fwr0[1] + 32768 + 2048);
    facc[0][0] = MFMA16(cat8(fml[0][0], fmh[0][0]), fq[0][0], facc[0][0]); PIN(facc[0][0]);
    facc[0][1] = MFMA16(cat8(fml[0][1], fmh[0][1]), fq[0][0], facc[0][1]); PIN(facc[0][1]);
    dmaw<0>(fwvo, fwsrd, flw + 65536);
    SBAR();
    fq[1][0] = lds128(far[1] + 32768 + 0); fml[1][1] = ldstr(fwr1[1] + 32768 + 0);
    facc[1][0] = MFMA16(cat8(fml[0][0], fmh[0][0]), fq[0][1], facc[1][0]); PIN(facc[1][0]);
    facc[1][1] = MFMA16(cat8(fml[0][1], fmh[0][1]), fq[0][1], facc[1][1]); PIN(facc[1][1]);
    dmaw<0>(fwvo + 128u, fwsrd, flw + 66560); fwvo += 65536u;
    SBAR();
    fmh[1][1] = ldstr(fwr1[1] + 32768 + 2048); fq[1][1] = lds128(far[1] + 32768 + 256);
    facc[0][2] = MFMA16(cat8(fml[0][2], fmh[0][2]), fq[0][0], facc[0][2]); PIN(facc[0][2]);
    facc[1][2] = MFMA16(cat8(fml[0][2], fmh[0][2]), fq[0][1], facc[1][2]); PIN(facc[1][2]);
    dmab<0>(favo, fasrd, fla + 65536);
    SBAR();
    fml[1][2] = ldstr(fwr0[1] + 32768 + 512); fmh[1][2] = ldstr(fwr0[1] + 32768 + 2560);
    facc[2][0] = MFMA16(cat8(fml[0][0], fmh[0][0]), fq[0][2], facc[2][0]); PIN(facc[2][0]);
    facc[2][1] = MFMA16(cat8(fml[0][1], fmh[0][1]), fq[0][2], facc[2][1]); PIN(facc[2][1]);
    dmab<1024>(favo, fasrd, fla + 65536); favo += 4096u;
    SBAR();
    fq[1][2] = lds128(far[1] + 32768 + 4096); fml[1][3] = ldstr(fwr1[1] + 32768 + 512);
    facc[2][2] = MFMA16(cat8(fml[0][2], fmh[0][2]), fq[0][2], facc[2][2]); PIN(facc[2][2]);
    facc[0][3] = MFMA16(cat8(fml[0][3], fmh[0][3]), fq[0][0], facc[0][3]); PIN(facc[0][3]);
    SBAR();
    fmh[1][3] = ldstr(fwr1[1] + 32768 + 2560); fq[1][3] = lds128(far[1] + 32768 + 4352);
    facc[1][3] = MFMA16(cat8(fml[0][3], fmh[0][3]), fq[0][1], facc[1][3]); PIN(facc[1][3]);
    facc[2][3] = MFMA16(cat8(fml[0][3], fmh[0][3]), fq[0][2], facc[2][3]); PIN(facc[2][3]);
    SBAR();
    facc[3][0] = MFMA16(cat8(fml[0][0], fmh[0][0]), fq[0][3], facc[3][0]); PIN(facc[3][0]);
    facc[3][1] = MFMA16(cat8(fml[0][1], fmh[0][1]), fq[0][3], facc[3][1]); PIN(facc[3][1]);
    SBAR();
    facc[3][2] = MFMA16(cat8(fml[0][2], fmh[0][2]), fq[0][3], facc[3][2]); PIN(facc[3][2]);
    facc[3][3] = MFMA16(cat8(fml[0][3], fmh[0][3]), fq[0][3], facc[3][3]); PIN(facc[3][3]);
    SBAR();
    wait_vm_bar<8>();
    fml[0][0] = ldstr(fwr0[0] + 0 + 0); fmh[0][0] = ldstr(fwr0[0] + 0 + 2048);
    facc[0][0] = MFMA16(cat8(fml[1][0], fmh[1][0]), fq[1][0], facc[0][0]); PIN(facc[0][0]);
    facc[0][1] = MFMA16(cat8(fml[1][1], fmh[1][1]), fq[1][0], facc[0][1]); PIN(facc[0][1]);
    dmaw<0>(fwvo, fwsrd, flw + 98304);
    SBAR();
    fq[0][0] = lds128(far[0] + 0 + 0); fml[0][1] = ldstr(fwr1[0] + 0 + 0);
    facc[1][0] = MFMA16(cat8(fml[1][0], fmh[1][0]), fq[1][1], facc[1][0]); PIN(facc[1][0]);
    facc[1][1] = MFMA16(cat8(fml[1][1], fmh[1][1]), fq[1][1], facc[1][1]); PIN(facc[1][1]);
    dmaw<0>(fwvo + 128u, fwsrd, flw + 99328); fwvo += 65536u;
    SBAR();
    fmh[0][1] = ldstr(fwr1[0] + 0 + 2048); fq[0][1] = lds128(far[0] + 0 + 256);
    facc[0][2] = MFMA16(cat8(fml[1][2], fmh[1][2]), fq[1][0], facc[0][2]); PIN(facc[0][2]);
    facc[1][2] = MFMA16(cat8(fml[1][2], fmh[1][2]), fq[1][1], facc[1][2]); PIN(facc[1][2]);
    dmab<0>(favo, fasrd, fla + 98304);
    SBAR();
    fml[0][2] = ldstr(fwr0[0] + 0 + 512); fmh[0][2] = ldstr(fwr0[0] + 0 + 2560);
    facc[2][0] = MFMA16(cat8(fml[1][0], fmh[1][0]), fq[1][2], facc[2][0]); PIN(facc[2][0]);
    facc[2][1] = MFMA16(cat8(fml[1][1], fmh[1][1]), fq[1][2], facc[2][1]); PIN(facc[2][1]);
    dmab<1024>(favo, fasrd, fla + 98304); favo += 4096u;
    SBAR();
    fq[0][2] = lds128(far[0] + 0 + 4096); fml[0][3] = ldstr(fwr1[0] + 0 + 512);
    facc[2][2] = MFMA16(cat8(fml[1][2], fmh[1][2]), fq[1][2], facc[2][2]); PIN(facc[2][2]);
    facc[0][3] = MFMA16(cat8(fml[1][3], fmh[1][3]), fq[1][0], facc[0][3]); PIN(facc[0][3]);
    SBAR();
    fmh[0][3] = ldstr(fwr1[0] + 0 + 2560); fq[0][3] = lds128(far[0] + 0 + 4352);
    facc[1][3] = MFMA16(cat8(fml[1][3], fmh[1][3]), fq[1][1], facc[1][3]); PIN(facc[1][3]);
    facc[2][3] = MFMA16(cat8(fml[1][3], fmh[1][3]), fq[1][2], facc[2][3]); PIN(facc[2][3]);
    SBAR();
    facc[3][0] = MFMA16(cat8(fml[1][0], fmh[1][0]), fq[1][3], facc[3][0]); PIN(facc[3][0]);
    facc[3][1] = MFMA16(cat8(fml[1][1], fmh[1][1]), fq[1][3], facc[3][1]); PIN(facc[3][1]);
    SBAR();
    facc[3][2] = MFMA16(cat8(fml[1][2], fmh[1][2]), fq[1][3], facc[3][2]); PIN(facc[3][2]);
    facc[3][3] = MFMA16(cat8(fml[1][3], fmh[1][3]), fq[1][3], facc[3][3]); PIN(facc[3][3]);
    SBAR();
    wait_vm_bar<8>();
    fml[1][0] = ldstr(fwr0[0] + 32768 + 0); fmh[1][0] = ldstr(fwr0[0] + 32768 + 2048);
    facc[0][0] = MFMA16(cat8(fml[0][0], fmh[0][0]), fq[0][0], facc[0][0]); PIN(facc[0][0]);
    facc[0][1] = MFMA16(cat8(fml[0][1], fmh[0][1]), fq[0][0], facc[0][1]); PIN(facc[0][1]);
    dmaw<0>(fwvo, fwsrd, flw + 0);
    SBAR();
    fq[1][0] = lds128(far[0] + 32768 + 0); fml[1][1] = ldstr(fwr1[0] + 32768 + 0);
    facc[1][0] = MFMA16(cat8(fml[0][0], fmh[0][0]), fq[0][1], facc[1][0]); PIN(facc[1][0]);
    facc[1][1] = MFMA16(cat8(fml[0][1], fmh[0][1]), fq[0][1], facc[1][1]); PIN(facc[1][1]);
    dmaw<0>(fwvo + 128u, fwsrd, flw + 1024); fwvo += 65536u;
    SBAR();
    fmh[1][1] = ldstr(fwr1[0] + 32768 + 2048); fq[1][1] = lds128(far[0] + 32768 + 256);
    facc[0][2] = MFMA16(cat8(fml[0][2], fmh[0][2]), fq[0][0], facc[0][2]); PIN(facc[0][2]);
    facc[1][2] = MFMA16(cat8(fml[0][2], fmh[0][2]), fq[0][1], facc[1][2]); PIN(facc[1][2]);
    dmab<0>(favo, fasrd, fla + 0);
    SBAR();
    fml[1][2] = ldstr(fwr0[0] + 32768 + 512); fmh[1][2] = ldstr(fwr0[0] + 32768 + 2560);
    facc[2][0] = MFMA16(cat8(fml[0][0], fmh[0][0]), fq[0][2], facc[2][0]); PIN(facc[2][0]);
    facc[2][1] = MFMA16(cat8(fml[0][1], fmh[0][1]), fq[0][2], facc[2][1]); PIN(facc[2][1]);
    dmab<1024>(favo, fasrd, fla + 0); favo += 4096u;
    SBAR();
    fq[1][2] = lds128(far[0] + 32768 + 4096); fml[1][3] = ldstr(fwr1[0] + 32768 + 512);
    facc[2][2] = MFMA16(cat8(fml[0][2], fmh[0][2]), fq[0][2], facc[2][2]); PIN(facc[2][2]);
    facc[0][3] = MFMA16(cat8(fml[0][3], fmh[0][3]), fq[0][0], facc[0][3]); PIN(facc[0][3]);
    SBAR();
    fmh[1][3] = ldstr(fwr1[0] + 32768 + 2560); fq[1][3] = lds128(far[0] + 32768 + 4352);
    facc[1][3] = MFMA16(cat8(fml[0][3], fmh[0][3]), fq[0][1], facc[1][3]); PIN(facc[1][3]);
    facc[2][3] = MFMA16(cat8(fml[0][3], fmh[0][3]), fq[0][2], facc[2][3]); PIN(facc[2][3]);
    SBAR();
    facc[3][0] = MFMA16(cat8(fml[0][0], fmh[0][0]), fq[0][3], facc[3][0]); PIN(facc[3][0]);
    facc[3][1] = MFMA16(cat8(fml[0][1], fmh[0][1]), fq[0][3], facc[3][1]); PIN(facc[3][1]);
    SBAR();
    facc[3][2] = MFMA16(cat8(fml[0][2], fmh[0][2]), fq[0][3], facc[3][2]); PIN(facc[3][2]);
    facc[3][3] = MFMA16(cat8(fml[0][3], fmh[0][3]), fq[0][3], facc[3][3]); PIN(facc[3][3]);
    SBAR();
    wait_vm_bar<8>();
    fml[0][0] = ldstr(fwr0[1] + 0 + 0); fmh[0][0] = ldstr(fwr0[1] + 0 + 2048);
    facc[0][0] = MFMA16(cat8(fml[1][0], fmh[1][0]), fq[1][0], facc[0][0]); PIN(facc[0][0]);
    facc[0][1] = MFMA16(cat8(fml[1][1], fmh[1][1]), fq[1][0], facc[0][1]); PIN(facc[0][1]);
    dmaw<0>(fwvo, fwsrd, flw + 32768);
    SBAR();
    fq[0][0] = lds128(far[1] + 0 + 0); fml[0][1] = ldstr(fwr1[1] + 0 + 0);
    facc[1][0] = MFMA16(cat8(fml[1][0], fmh[1][0]), fq[1][1], facc[1][0]); PIN(facc[1][0]);
    facc[1][1] = MFMA16(cat8(fml[1][1], fmh[1][1]), fq[1][1], facc[1][1]); PIN(facc[1][1]);
    dmaw<0>(fwvo + 128u, fwsrd, flw + 33792); fwvo += 65536u;
    SBAR();
    fmh[0][1] = ldstr(fwr1[1] + 0 + 2048); fq[0][1] = lds128(far[1] + 0 + 256);
    facc[0][2] = MFMA16(cat8(fml[1][2], fmh[1][2]), fq[1][0], facc[0][2]); PIN(facc[0][2]);
    facc[1][2] = MFMA16(cat8(fml[1][2], fmh[1][2]), fq[1][1], facc[1][2]); PIN(facc[1][2]);
    dmab<0>(favo, fasrd, fla + 32768);
    SBAR();
    fml[0][2] = ldstr(fwr0[1] + 0 + 512); fmh[0][2] = ldstr(fwr0[1] + 0 + 2560);
    facc[2][0] = MFMA16(cat8(fml[1][0], fmh[1][0]), fq[1][2], facc[2][0]); PIN(facc[2][0]);
    facc[2][1] = MFMA16(cat8(fml[1][1], fmh[1][1]), fq[1][2], facc[2][1]); PIN(facc[2][1]);
    dmab<1024>(favo, fasrd, fla + 32768); favo += 4096u;
    SBAR();
    fq[0][2] = lds128(far[1] + 0 + 4096); fml[0][3] = ldstr(fwr1[1] + 0 + 512);
    facc[2][2] = MFMA16(cat8(fml[1][2], fmh[1][2]), fq[1][2], facc[2][2]); PIN(facc[2][2]);
    facc[0][3] = MFMA16(cat8(fml[1][3], fmh[1][3]), fq[1][0], facc[0][3]); PIN(facc[0][3]);
    SBAR();
    fmh[0][3] = ldstr(fwr1[1] + 0 + 2560); fq[0][3] = lds128(far[1] + 0 + 4352);
    facc[1][3] = MFMA16(cat8(fml[1][3], fmh[1][3]), fq[1][1], facc[1][3]); PIN(facc[1][3]);
    facc[2][3] = MFMA16(cat8(fml[1][3], fmh[1][3]), fq[1][2], facc[2][3]); PIN(facc[2][3]);
    SBAR();
    facc[3][0] = MFMA16(cat8(fml[1][0], fmh[1][0]), fq[1][3], facc[3][0]); PIN(facc[3][0]);
    facc[3][1] = MFMA16(cat8(fml[1][1], fmh[1][1]), fq[1][3], facc[3][1]); PIN(facc[3][1]);
    SBAR();
    facc[3][2] = MFMA16(cat8(fml[1][2], fmh[1][2]), fq[1][3], facc[3][2]); PIN(facc[3][2]);
    facc[3][3] = MFMA16(cat8(fml[1][3], fmh[1][3]), fq[1][3], facc[3][3]); PIN(facc[3][3]);
    SBAR();
    wait_vm_bar<8>();
    fml[1][0] = ldstr(fwr0[1] + 32768 + 0); fmh[1][0] = ldstr(fwr0[1] + 32768 + 2048);
    facc[0][0] = MFMA16(cat8(fml[0][0], fmh[0][0]), fq[0][0], facc[0][0]); PIN(facc[0][0]);
    facc[0][1] = MFMA16(cat8(fml[0][1], fmh[0][1]), fq[0][0], facc[0][1]); PIN(facc[0][1]);
    dmaw<0>(fwvo, fwsrd, flw + 65536);
    SBAR();
    fq[1][0] = lds128(far[1] + 32768 + 0); fml[1][1] = ldstr(fwr1[1] + 32768 + 0);
    facc[1][0] = MFMA16(cat8(fml[0][0], fmh[0][0]), fq[0][1], facc[1][0]); PIN(facc[1][0]);
    facc[1][1] = MFMA16(cat8(fml[0][1], fmh[0][1]), fq[0][1], facc[1][1]); PIN(facc[1][1]);
    dmaw<0>(fwvo + 128u, fwsrd, flw + 66560); fwvo += 65536u;
    SBAR();
    fmh[1][1] = ldstr(fwr1[1] + 32768 + 2048); fq[1][1] = lds128(far[1] + 32768 + 256);
    facc[0][2] = MFMA16(cat8(fml[0][2], fmh[0][2]), fq[0][0], facc[0][2]); PIN(facc[0][2]);
    facc[1][2] = MFMA16(cat8(fml[0][2], fmh[0][2]), fq[0][1], facc[1][2]); PIN(facc[1][2]);
    dmab<0>(favo, fasrd, fla + 65536);
    SBAR();
    fml[1][2] = ldstr(fwr0[1] + 32768 + 512); fmh[1][2] = ldstr(fwr0[1] + 32768 + 2560);
    facc[2][0] = MFMA16(cat8(fml[0][0], fmh[0][0]), fq[0][2], facc[2][0]); PIN(facc[2][0]);
    facc[2][1] = MFMA16(cat8(fml[0][1], fmh[0][1]), fq[0][2], facc[2][1]); PIN(facc[2][1]);
    dmab<1024>(favo, fasrd, fla + 65536); favo += 4096u;
    SBAR();
    fq[1][2] = lds128(far[1] + 32768 + 4096); fml[1][3] = ldstr(fwr1[1] + 32768 + 512);
    facc[2][2] = MFMA16(cat8(fml[0][2], fmh[0][2]), fq[0][2], facc[2][2]); PIN(facc[2][2]);
    facc[0][3] = MFMA16(cat8(fml[0][3], fmh[0][3]), fq[0][0], facc[0][3]); PIN(facc[0][3]);
    SBAR();
    fmh[1][3] = ldstr(fwr1[1] + 32768 + 2560); fq[1][3] = lds128(far[1] + 32768 + 4352);
    facc[1][3] = MFMA16(cat8(fml[0][3], fmh[0][3]), fq[0][1], facc[1][3]); PIN(facc[1][3]);
    facc[2][3] = MFMA16(cat8(fml[0][3], fmh[0][3]), fq[0][2], facc[2][3]); PIN(facc[2][3]);
    SBAR();
    facc[3][0] = MFMA16(cat8(fml[0][0], fmh[0][0]), fq[0][3], facc[3][0]); PIN(facc[3][0]);
    facc[3][1] = MFMA16(cat8(fml[0][1], fmh[0][1]), fq[0][3], facc[3][1]); PIN(facc[3][1]);
    SBAR();
    facc[3][2] = MFMA16(cat8(fml[0][2], fmh[0][2]), fq[0][3], facc[3][2]); PIN(facc[3][2]);
    facc[3][3] = MFMA16(cat8(fml[0][3], fmh[0][3]), fq[0][3], facc[3][3]); PIN(facc[3][3]);
    SBAR();
    wait_vm_bar<8>();
    fml[0][0] = ldstr(fwr0[0] + 0 + 0); fmh[0][0] = ldstr(fwr0[0] + 0 + 2048);
    facc[0][0] = MFMA16(cat8(fml[1][0], fmh[1][0]), fq[1][0], facc[0][0]); PIN(facc[0][0]);
    facc[0][1] = MFMA16(cat8(fml[1][1], fmh[1][1]), fq[1][0], facc[0][1]); PIN(facc[0][1]);
    dmaw<0>(fwvo, fwsrd, flw + 98304);
    SBAR();
    fq[0][0] = lds128(far[0] + 0 + 0); fml[0][1] = ldstr(fwr1[0] + 0 + 0);
    facc[1][0] = MFMA16(cat8(fml[1][0], fmh[1][0]), fq[1][1], facc[1][0]); PIN(facc[1][0]);
    facc[1][1] = MFMA16(cat8(fml[1][1], fmh[1][1]), fq[1][1], facc[1][1]); PIN(facc[1][1]);
    dmaw<0>(fwvo + 128u, fwsrd, flw + 99328); fwvo += 65536u;
    SBAR();
    fmh[0][1] = ldstr(fwr1[0] + 0 + 2048); fq[0][1] = lds128(far[0] + 0 + 256);
    facc[0][2] = MFMA16(cat8(fml[1][2], fmh[1][2]), fq[1][0], facc[0][2]); PIN(facc[0][2]);
    facc[1][2] = MFMA16(cat8(fml[1][2], fmh[1][2]), fq[1][1], facc[1][2]); PIN(facc[1][2]);
    dmab<0>(favo, fasrd, fla + 98304);
    SBAR();
    fml[0][2] = ldstr(fwr0[0] + 0 + 512); fmh[0][2] = ldstr(fwr0[0] + 0 + 2560);
    facc[2][0] = MFMA16(cat8(fml[1][0], fmh[1][0]), fq[1][2], facc[2][0]); PIN(facc[2][0]);
    facc[2][1] = MFMA16(cat8(fml[1][1], fmh[1][1]), fq[1][2], facc[2][1]); PIN(facc[2][1]);
    dmab<1024>(favo, fasrd, fla + 98304); favo += 4096u;
    SBAR();
    fq[0][2] = lds128(far[0] + 0 + 4096); fml[0][3] = ldstr(fwr1[0] + 0 + 512);
    facc[2][2] = MFMA16(cat8(fml[1][2], fmh[1][2]), fq[1][2], facc[2][2]); PIN(facc[2][2]);
    facc[0][3] = MFMA16(cat8(fml[1][3], fmh[1][3]), fq[1][0], facc[0][3]); PIN(facc[0][3]);
    SBAR();
    fmh[0][3] = ldstr(fwr1[0] + 0 + 2560); fq[0][3] = lds128(far[0] + 0 + 4352);
    facc[1][3] = MFMA16(cat8(fml[1][3], fmh[1][3]), fq[1][1], facc[1][3]); PIN(facc[1][3]);
    facc[2][3] = MFMA16(cat8(fml[1][3], fmh[1][3]), fq[1][2], facc[2][3]); PIN(facc[2][3]);
    SBAR();
    facc[3][0] = MFMA16(cat8(fml[1][0], fmh[1][0]), fq[1][3], facc[3][0]); PIN(facc[3][0]);
    facc[3][1] = MFMA16(cat8(fml[1][1], fmh[1][1]), fq[1][3], facc[3][1]); PIN(facc[3][1]);
    SBAR();
    facc[3][2] = MFMA16(cat8(fml[1][2], fmh[1][2]), fq[1][3], facc[3][2]); PIN(facc[3][2]);
    facc[3][3] = MFMA16(cat8(fml[1][3], fmh[1][3]), fq[1][3], facc[3][3]); PIN(facc[3][3]);
    SBAR();
    wait_vm_bar<8>();
    fml[1][0] = ldstr(fwr0[0] + 32768 + 0); fmh[1][0] = ldstr(fwr0[0] + 32768 + 2048);
    facc[0][0] = MFMA16(cat8(fml[0][0], fmh[0][0]), fq[0][0], facc[0][0]); PIN(facc[0][0]);
    facc[0][1] = MFMA16(cat8(fml[0][1], fmh[0][1]), fq[0][0], facc[0][1]); PIN(facc[0][1]);
    dmaw<0>(fwvo, fwsrd, flw + 0);
    SBAR();
    fq[1][0] = lds128(far[0] + 32768 + 0); fml[1][1] = ldstr(fwr1[0] + 32768 + 0);
    facc[1][0] = MFMA16(cat8(fml[0][0], fmh[0][0]), fq[0][1], facc[1][0]); PIN(facc[1][0]);
    facc[1][1] = MFMA16(cat8(fml[0][1], fmh[0][1]), fq[0][1], facc[1][1]); PIN(facc[1][1]);
    dmaw<0>(fwvo + 128u, fwsrd, flw + 1024); fwvo += 65536u;
    SBAR();
    fmh[1][1] = ldstr(fwr1[0] + 32768 + 2048); fq[1][1] = lds128(far[0] + 32768 + 256);
    facc[0][2] = MFMA16(cat8(fml[0][2], fmh[0][2]), fq[0][0], facc[0][2]); PIN(facc[0][2]);
    facc[1][2] = MFMA16(cat8(fml[0][2], fmh[0][2]), fq[0][1], facc[1][2]); PIN(facc[1][2]);
    dmab<0>(favo, fasrd, fla + 0);
    SBAR();
    fml[1][2] = ldstr(fwr0[0] + 32768 + 512); fmh[1][2] = ldstr(fwr0[0] + 32768 + 2560);
    facc[2][0] = MFMA16(cat8(fml[0][0], fmh[0][0]), fq[0][2], facc[2][0]); PIN(facc[2][0]);
    facc[2][1] = MFMA16(cat8(fml[0][1], fmh[0][1]), fq[0][2], facc[2][1]); PIN(facc[2][1]);
    dmab<1024>(favo, fasrd, fla + 0); favo += 4096u;
    SBAR();
    fq[1][2] = lds128(far[0] + 32768 + 4096); fml[1][3] = ldstr(fwr1[0] + 32768 + 512);
    facc[2][2] = MFMA16(cat8(fml[0][2], fmh[0][2]), fq[0][2], facc[2][2]); PIN(facc[2][2]);
    facc[0][3] = MFMA16(cat8(fml[0][3], fmh[0][3]), fq[0][0], facc[0][3]); PIN(facc[0][3]);
    SBAR();
    fmh[1][3] = ldstr(fwr1[0] + 32768 + 2560); fq[1][3] = lds128(far[0] + 32768 + 4352);
    facc[1][3] = MFMA16(cat8(fml[0][3], fmh[0][3]), fq[0][1], facc[1][3]); PIN(facc[1][3]);
    facc[2][3] = MFMA16(cat8(fml[0][3], fmh[0][3]), fq[0][2], facc[2][3]); PIN(facc[2][3]);
    SBAR();
    facc[3][0] = MFMA16(cat8(fml[0][0], fmh[0][0]), fq[0][3], facc[3][0]); PIN(facc[3][0]);
    facc[3][1] = MFMA16(cat8(fml[0][1], fmh[0][1]), fq[0][3], facc[3][1]); PIN(facc[3][1]);
    SBAR();
    facc[3][2] = MFMA16(cat8(fml[0][2], fmh[0][2]), fq[0][3], facc[3][2]); PIN(facc[3][2]);
    facc[3][3] = MFMA16(cat8(fml[0][3], fmh[0][3]), fq[0][3], facc[3][3]); PIN(facc[3][3]);
    SBAR();
    wait_vm_bar<8>();
    fml[0][0] = ldstr(fwr0[1] + 0 + 0); fmh[0][0] = ldstr(fwr0[1] + 0 + 2048);
    facc[0][0] = MFMA16(cat8(fml[1][0], fmh[1][0]), fq[1][0], facc[0][0]); PIN(facc[0][0]);
    facc[0][1] = MFMA16(cat8(fml[1][1], fmh[1][1]), fq[1][0], facc[0][1]); PIN(facc[0][1]);
    dmaw<0>(fwvo, fwsrd, flw + 32768);
    SBAR();
    fq[0][0] = lds128(far[1] + 0 + 0); fml[0][1] = ldstr(fwr1[1] + 0 + 0);
    facc[1][0] = MFMA16(cat8(fml[1][0], fmh[1][0]), fq[1][1], facc[1][0]); PIN(facc[1][0]);
    facc[1][1] = MFMA16(cat8(fml[1][1], fmh[1][1]), fq[1][1], facc[1][1]); PIN(facc[1][1]);
    dmaw<0>(fwvo + 128u, fwsrd, flw + 33792); fwvo += 65536u;
    SBAR();
    fmh[0][1] = ldstr(fwr1[1] + 0 + 2048); fq[0][1] = lds128(far[1] + 0 + 256);
    facc[0][2] = MFMA16(cat8(fml[1][2], fmh[1][2]), fq[1][0], facc[0][2]); PIN(facc[0][2]);
    facc[1][2] = MFMA16(cat8(fml[1][2], fmh[1][2]), fq[1][1], facc[1][2]); PIN(facc[1][2]);
    dmab<0>(favo, fasrd, fla + 32768);
    SBAR();
    fml[0][2] = ldstr(fwr0[1] + 0 + 512); fmh[0][2] = ldstr(fwr0[1] + 0 + 2560);
    facc[2][0] = MFMA16(cat8(fml[1][0], fmh[1][0]), fq[1][2], facc[2][0]); PIN(facc[2][0]);
    facc[2][1] = MFMA16(cat8(fml[1][1], fmh[1][1]), fq[1][2], facc[2][1]); PIN(facc[2][1]);
    dmab<1024>(favo, fasrd, fla + 32768); favo += 4096u;
    SBAR();
    fq[0][2] = lds128(far[1] + 0 + 4096); fml[0][3] = ldstr(fwr1[1] + 0 + 512);
    facc[2][2] = MFMA16(cat8(fml[1][2], fmh[1][2]), fq[1][2], facc[2][2]); PIN(facc[2][2]);
    facc[0][3] = MFMA16(cat8(fml[1][3], fmh[1][3]), fq[1][0], facc[0][3]); PIN(facc[0][3]);
    SBAR();
    fmh[0][3] = ldstr(fwr1[1] + 0 + 2560); fq[0][3] = lds128(far[1] + 0 + 4352);
    facc[1][3] = MFMA16(cat8(fml[1][3], fmh[1][3]), fq[1][1], facc[1][3]); PIN(facc[1][3]);
    facc[2][3] = MFMA16(cat8(fml[1][3], fmh[1][3]), fq[1][2], facc[2][3]); PIN(facc[2][3]);
    SBAR();
    facc[3][0] = MFMA16(cat8(fml[1][0], fmh[1][0]), fq[1][3], facc[3][0]); PIN(facc[3][0]);
    facc[3][1] = MFMA16(cat8(fml[1][1], fmh[1][1]), fq[1][3], facc[3][1]); PIN(facc[3][1]);
    SBAR();
    facc[3][2] = MFMA16(cat8(fml[1][2], fmh[1][2]), fq[1][3], facc[3][2]); PIN(facc[3][2]);
    facc[3][3] = MFMA16(cat8(fml[1][3], fmh[1][3]), fq[1][3], facc[3][3]); PIN(facc[3][3]);
    SBAR();
    wait_vm_bar<8>();
    fml[1][0] = ldstr(fwr0[1] + 32768 + 0); fmh[1][0] = ldstr(fwr0[1] + 32768 + 2048);
    facc[0][0] = MFMA16(cat8(fml[0][0], fmh[0][0]), fq[0][0], facc[0][0]); PIN(facc[0][0]);
    facc[0][1] = MFMA16(cat8(fml[0][1], fmh[0][1]), fq[0][0], facc[0][1]); PIN(facc[0][1]);
    SBAR();
    fq[1][0] = lds128(far[1] + 32768 + 0); fml[1][1] = ldstr(fwr1[1] + 32768 + 0);
    facc[1][0] = MFMA16(cat8(fml[0][0], fmh[0][0]), fq[0][1], facc[1][0]); PIN(facc[1][0]);
    facc[1][1] = MFMA16(cat8(fml[0][1], fmh[0][1]), fq[0][1], facc[1][1]); PIN(facc[1][1]);
    SBAR();
    fmh[1][1] = ldstr(fwr1[1] + 32768 + 2048); fq[1][1] = lds128(far[1] + 32768 + 256);
    facc[0][2] = MFMA16(cat8(fml[0][2], fmh[0][2]), fq[0][0], facc[0][2]); PIN(facc[0][2]);
    facc[1][2] = MFMA16(cat8(fml[0][2], fmh[0][2]), fq[0][1], facc[1][2]); PIN(facc[1][2]);
    SBAR();
    fml[1][2] = ldstr(fwr0[1] + 32768 + 512); fmh[1][2] = ldstr(fwr0[1] + 32768 + 2560);
    facc[2][0] = MFMA16(cat8(fml[0][0], fmh[0][0]), fq[0][2], facc[2][0]); PIN(facc[2][0]);
    facc[2][1] = MFMA16(cat8(fml[0][1], fmh[0][1]), fq[0][2], facc[2][1]); PIN(facc[2][1]);
    SBAR();
    fq[1][2] = lds128(far[1] + 32768 + 4096); fml[1][3] = ldstr(fwr1[1] + 32768 + 512);
    facc[2][2] = MFMA16(cat8(fml[0][2], fmh[0][2]), fq[0][2], facc[2][2]); PIN(facc[2][2]);
    facc[0][3] = MFMA16(cat8(fml[0][3], fmh[0][3]), fq[0][0], facc[0][3]); PIN(facc[0][3]);
    SBAR();
    fmh[1][3] = ldstr(fwr1[1] + 32768 + 2560); fq[1][3] = lds128(far[1] + 32768 + 4352);
    facc[1][3] = MFMA16(cat8(fml[0][3], fmh[0][3]), fq[0][1], facc[1][3]); PIN(facc[1][3]);
    facc[2][3] = MFMA16(cat8(fml[0][3], fmh[0][3]), fq[0][2], facc[2][3]); PIN(facc[2][3]);
    SBAR();
    facc[3][0] = MFMA16(cat8(fml[0][0], fmh[0][0]), fq[0][3], facc[3][0]); PIN(facc[3][0]);
    facc[3][1] = MFMA16(cat8(fml[0][1], fmh[0][1]), fq[0][3], facc[3][1]); PIN(facc[3][1]);
    SBAR();
    facc[3][2] = MFMA16(cat8(fml[0][2], fmh[0][2]), fq[0][3], facc[3][2]); PIN(facc[3][2]);
    facc[3][3] = MFMA16(cat8(fml[0][3], fmh[0][3]), fq[0][3], facc[3][3]); PIN(facc[3][3]);
    SBAR();
    wait_vm_bar<4>();
    fml[0][0] = ldstr(fwr0[0] + 0 + 0); fmh[0][0] = ldstr(fwr0[0] + 0 + 2048);
    facc[0][0] = MFMA16(cat8(fml[1][0], fmh[1][0]), fq[1][0], facc[0][0]); PIN(facc[0][0]);
    facc[0][1] = MFMA16(cat8(fml[1][1], fmh[1][1]), fq[1][0], facc[0][1]); PIN(facc[0][1]);
    SBAR();
    fq[0][0] = lds128(far[0] + 0 + 0); fml[0][1] = ldstr(fwr1[0] + 0 + 0);
    facc[1][0] = MFMA16(cat8(fml[1][0], fmh[1][0]), fq[1][1], facc[1][0]); PIN(facc[1][0]);
    facc[1][1] = MFMA16(cat8(fml[1][1], fmh[1][1]), fq[1][1], facc[1][1]); PIN(facc[1][1]);
    SBAR();
    fmh[0][1] = ldstr(fwr1[0] + 0 + 2048); fq[0][1] = lds128(far[0] + 0 + 256);
    facc[0][2] = MFMA16(cat8(fml[1][2], fmh[1][2]), fq[1][0], facc[0][2]); PIN(facc[0][2]);
    facc[1][2] = MFMA16(cat8(fml[1][2], fmh[1][2]), fq[1][1], facc[1][2]); PIN(facc[1][2]);
    SBAR();
    fml[0][2] = ldstr(fwr0[0] + 0 + 512); fmh[0][2] = ldstr(fwr0[0] + 0 + 2560);
    facc[2][0] = MFMA16(cat8(fml[1][0], fmh[1][0]), fq[1][2], facc[2][0]); PIN(facc[2][0]);
    facc[2][1] = MFMA16(cat8(fml[1][1], fmh[1][1]), fq[1][2], facc[2][1]); PIN(facc[2][1]);
    SBAR();
    fq[0][2] = lds128(far[0] + 0 + 4096); fml[0][3] = ldstr(fwr1[0] + 0 + 512);
    facc[2][2] = MFMA16(cat8(fml[1][2], fmh[1][2]), fq[1][2], facc[2][2]); PIN(facc[2][2]);
    facc[0][3] = MFMA16(cat8(fml[1][3], fmh[1][3]), fq[1][0], facc[0][3]); PIN(facc[0][3]);
    SBAR();
    fmh[0][3] = ldstr(fwr1[0] + 0 + 2560); fq[0][3] = lds128(far[0] + 0 + 4352);
    facc[1][3] = MFMA16(cat8(fml[1][3], fmh[1][3]), fq[1][1], facc[1][3]); PIN(facc[1][3]);
    facc[2][3] = MFMA16(cat8(fml[1][3], fmh[1][3]), fq[1][2], facc[2][3]); PIN(facc[2][3]);
    SBAR();
    facc[3][0] = MFMA16(cat8(fml[1][0], fmh[1][0]), fq[1][3], facc[3][0]); PIN(facc[3][0]);
    facc[3][1] = MFMA16(cat8(fml[1][1], fmh[1][1]), fq[1][3], facc[3][1]); PIN(facc[3][1]);
    SBAR();
    facc[3][2] = MFMA16(cat8(fml[1][2], fmh[1][2]), fq[1][3], facc[3][2]); PIN(facc[3][2]);
    facc[3][3] = MFMA16(cat8(fml[1][3], fmh[1][3]), fq[1][3], facc[3][3]); PIN(facc[3][3]);
    SBAR();
    wait_vm_bar<0>();
    fml[1][0] = ldstr(fwr0[0] + 32768 + 0); fmh[1][0] = ldstr(fwr0[0] + 32768 + 2048);
    facc[0][0] = MFMA16(cat8(fml[0][0], fmh[0][0]), fq[0][0], facc[0][0]); PIN(facc[0][0]);
    facc[0][1] = MFMA16(cat8(fml[0][1], fmh[0][1]), fq[0][0], facc[0][1]); PIN(facc[0][1]);
    SBAR();
    fq[1][0] = lds128(far[0] + 32768 + 0); fml[1][1] = ldstr(fwr1[0] + 32768 + 0);
    facc[1][0] = MFMA16(cat8(fml[0][0], fmh[0][0]), fq[0][1], facc[1][0]); PIN(facc[1][0]);
    facc[1][1] = MFMA16(cat8(fml[0][1], fmh[0][1]), fq[0][1], facc[1][1]); PIN(facc[1][1]);
    SBAR();
    fmh[1][1] = ldstr(fwr1[0] + 32768 + 2048); fq[1][1] = lds128(far[0] + 32768 + 256);
    facc[0][2] = MFMA16(cat8(fml[0][2], fmh[0][2]), fq[0][0], facc[0][2]); PIN(facc[0][2]);
    facc[1][2] = MFMA16(cat8(fml[0][2], fmh[0][2]), fq[0][1], facc[1][2]); PIN(facc[1][2]);
    SBAR();
    fml[1][2] = ldstr(fwr0[0] + 32768 + 512); fmh[1][2] = ldstr(fwr0[0] + 32768 + 2560);
    facc[2][0] = MFMA16(cat8(fml[0][0], fmh[0][0]), fq[0][2], facc[2][0]); PIN(facc[2][0]);
    facc[2][1] = MFMA16(cat8(fml[0][1], fmh[0][1]), fq[0][2], facc[2][1]); PIN(facc[2][1]);
    SBAR();
    fq[1][2] = lds128(far[0] + 32768 + 4096); fml[1][3] = ldstr(fwr1[0] + 32768 + 512);
    facc[2][2] = MFMA16(cat8(fml[0][2], fmh[0][2]), fq[0][2], facc[2][2]); PIN(facc[2][2]);
    facc[0][3] = MFMA16(cat8(fml[0][3], fmh[0][3]), fq[0][0], facc[0][3]); PIN(facc[0][3]);
    SBAR();
    fmh[1][3] = ldstr(fwr1[0] + 32768 + 2560); fq[1][3] = lds128(far[0] + 32768 + 4352);
    facc[1][3] = MFMA16(cat8(fml[0][3], fmh[0][3]), fq[0][1], facc[1][3]); PIN(facc[1][3]);
    facc[2][3] = MFMA16(cat8(fml[0][3], fmh[0][3]), fq[0][2], facc[2][3]); PIN(facc[2][3]);
    SBAR();
    facc[3][0] = MFMA16(cat8(fml[0][0], fmh[0][0]), fq[0][3], facc[3][0]); PIN(facc[3][0]);
    facc[3][1] = MFMA16(cat8(fml[0][1], fmh[0][1]), fq[0][3], facc[3][1]); PIN(facc[3][1]);
    SBAR();
    facc[3][2] = MFMA16(cat8(fml[0][2], fmh[0][2]), fq[0][3], facc[3][2]); PIN(facc[3][2]);
    facc[3][3] = MFMA16(cat8(fml[0][3], fmh[0][3]), fq[0][3], facc[3][3]); PIN(facc[3][3]);
    SBAR();
    facc[0][0] = MFMA16(cat8(fml[1][0], fmh[1][0]), fq[1][0], facc[0][0]); PIN(facc[0][0]);
    facc[0][1] = MFMA16(cat8(fml[1][1], fmh[1][1]), fq[1][0], facc[0][1]); PIN(facc[0][1]);
    SBAR();
    facc[1][0] = MFMA16(cat8(fml[1][0], fmh[1][0]), fq[1][1], facc[1][0]); PIN(facc[1][0]);
    facc[1][1] = MFMA16(cat8(fml[1][1], fmh[1][1]), fq[1][1], facc[1][1]); PIN(facc[1][1]);
    SBAR();
    facc[0][2] = MFMA16(cat8(fml[1][2], fmh[1][2]), fq[1][0], facc[0][2]); PIN(facc[0][2]);
    facc[1][2] = MFMA16(cat8(fml[1][2], fmh[1][2]), fq[1][1], facc[1][2]); PIN(facc[1][2]);
    SBAR();
    facc[2][0] = MFMA16(cat8(fml[1][0], fmh[1][0]), fq[1][2], facc[2][0]); PIN(facc[2][0]);
    facc[2][1] = MFMA16(cat8(fml[1][1], fmh[1][1]), fq[1][2], facc[2][1]); PIN(facc[2][1]);
    SBAR();
    facc[2][2] = MFMA16(cat8(fml[1][2], fmh[1][2]), fq[1][2], facc[2][2]); PIN(facc[2][2]);
    facc[0][3] = MFMA16(cat8(fml[1][3], fmh[1][3]), fq[1][0], facc[0][3]); PIN(facc[0][3]);
    SBAR();
    facc[1][3] = MFMA16(cat8(fml[1][3], fmh[1][3]), fq[1][1], facc[1][3]); PIN(facc[1][3]);
    facc[2][3] = MFMA16(cat8(fml[1][3], fmh[1][3]), fq[1][2], facc[2][3]); PIN(facc[2][3]);
    SBAR();
    facc[3][0] = MFMA16(cat8(fml[1][0], fmh[1][0]), fq[1][3], facc[3][0]); PIN(facc[3][0]);
    facc[3][1] = MFMA16(cat8(fml[1][1], fmh[1][1]), fq[1][3], facc[3][1]); PIN(facc[3][1]);
    SBAR();
    facc[3][2] = MFMA16(cat8(fml[1][2], fmh[1][2]), fq[1][3], facc[3][2]); PIN(facc[3][2]);
    facc[3][3] = MFMA16(cat8(fml[1][3], fmh[1][3]), fq[1][3], facc[3][3]); PIN(facc[3][3]);
    SBAR();
    LAS char* xo = (LAS char*)lds + wid * 8192 + l16;
    asm volatile("s_barrier" ::: "memory");
#pragma unroll
    for (int i = 0; i < 2; ++i)
#pragma unroll
        for (int q = 0; q < 4; ++q) *(LAS f32x4*)(xo + (i * 4 + q) * 1024) = fwk ? facc[2 * i + (q >> 1)][q & 1] : facc[2 * i + (q >> 1)][2 + (q & 1)];
    asm volatile("s_waitcnt lgkmcnt(0)\n\ts_barrier" ::: "memory");
    f32x16 part[2];
    {
        LAS const char* po = (LAS const char*)lds + (wid ^ 4) * 8192 + l16;
#pragma unroll
        for (int i = 0; i < 2; ++i) {
#pragma unroll
            for (int q = 0; q < 4; ++q) { const f32x4 v = *(LAS const f32x4*)(po + (i * 4 + q) * 1024) + (fwk ? facc[2 * i + (q >> 1)][2 + (q & 1)] : facc[2 * i + (q >> 1)][q & 1]);
#pragma unroll
                for (int e = 0; e < 4; ++e) part[i][4 * q + e] = v[e]; }
        }
    }
    const int cb = 2 * fwc + fwk;
    const unsigned exb = WS_EX + (g128 * 4 + fcg) * 262144 + cb * 4096;
#pragma unroll
    for (int i = 0; i < 2; ++i) {
        const int to = 2 * fwr_ + i;
        if (to != fq4) {
            st16(wsr, l16, exb + to * 65536 + fq4 * 16384, pack8(part[i], 0)); st16(wsr, l16, exb + to * 65536 + fq4 * 16384 + 1024, pack8(part[i], 1));
        }
    }
    wait_vm<0>();
    { unsigned old = 0;
      if (lane == 0) old = __hip_atomic_fetch_add(cnt, 1u, __ATOMIC_RELAXED, __HIP_MEMORY_SCOPE_WORKGROUP);
      if (__builtin_amdgcn_readfirstlane(old) == 15u) st_flag(wsr, WS_FLAG + F_EX * 32768 + b * 128, tag); }
    if (fwr_ == (fq4 >> 1)) {
        f32x16 mine = (fq4 & 1) ? part[1] : part[0];
        const unsigned ooff = (unsigned)(((g128 * 4 + fq4) * 32 + (lane & 15)) * DM + fcg * 128 + cb * 32 + 4 * g4) * 2u;
        u32x2 xv[4];
#pragma unroll
        for (int q = 0; q < 4; ++q) xv[q] = *(const u32x2*)((const char*)x + ooff + (q >> 1) * 16384 + (q & 1) * 32);
        const unsigned pvo_e = (unsigned)((b & ~6) + 2 * (lane & 3)) * 128u;
        pv = poll_issue(fbase + F_EX * 32768, pvo_e);
        wait_vm<0>();
        poll_check(pv, fbase + F_EX * 32768, pvo_e, tag);
        bf16x8 in[3][2];
#pragma unroll
        for (int f = 0; f < 3; ++f) { const int from = f + (f >= fq4 ? 1 : 0);
#pragma unroll
            for (int q = 0; q < 2; ++q) in[f][q] = gload(ws + exb + fq4 * 65536 + from * 16384 + q * 1024, l16); }
        asm volatile("s_waitcnt vmcnt(0)" : "+v"(in[0][0]), "+v"(in[0][1]), "+v"(in[1][0]), "+v"(in[1][1]), "+v"(in[2][0]), "+v"(in[2][1]) :: "memory");
#pragma unroll
        for (int q = 0; q < 4; ++q) {
            f32x4 a, bb, c;
#pragma unroll
            for (int e = 0; e < 4; ++e) {
                const unsigned w0 = __builtin_bit_cast(u32x4, in[0][q >> 1])[2 * (q & 1) + (e >> 1)], w1 = __builtin_bit_cast(u32x4, in[1][q >> 1])[2 * (q & 1) + (e >> 1)], w2 = __builtin_bit_cast(u32x4, in[2][q >> 1])[2 * (q & 1) + (e >> 1)];
                a[e] = (e & 1) ? bf_hi(w0) : bf_lo(w0); bb[e] = (e & 1) ? bf_hi(w1) : bf_lo(w1); c[e] = (e & 1) ? bf_hi(w2) : bf_lo(w2);
            }
            float ov[4];
#pragma unroll
            for (int e = 0; e < 4; ++e) {
                const float mv = mine[4 * q + e];
                const float s0 = fq4 == 0 ? mv : a[e], s1 = fq4 == 0 ? a[e] : (fq4 == 1 ? mv : bb[e]), s2 = fq4 <= 1 ? bb[e] : (fq4 == 2 ? mv : c[e]);
                const float gg = fq4 == 3 ? mv : c[e], ss = (s0 + s1) + s2;
                const float sl = gg * __builtin_amdgcn_rcpf(1.0f + EX2(gg * -1.4426950408889634f));
                const float xx = (e & 1) ? bf_hi(xv[q][e >> 1]) : bf_lo(xv[q][e >> 1]);
                ov[e] = xx + ss * sl;
            }
            *(u32x2*)((char*)out + ooff + (q >> 1) * 16384 + (q & 1) * 32) = (u32x2){cvtpk(ov[0], ov[1]), cvtpk(ov[2], ov[3])};
        }
    }
    }
    if (b == 0 && wid == 1) {
        u32x2 v[4];
#pragma unroll
        for (int i = 0; i < 4; ++i) v[i] = poll_issue(fbase + F_CB * 32768, (unsigned)(i * 64 + lane) * 128u);
        asm volatile("s_waitcnt vmcnt(0)" : "+v"(v[0]), "+v"(v[1]), "+v"(v[2]), "+v"(v[3]) :: "memory");
#pragma unroll
        for (int i = 0; i < 4; ++i) poll_check(v[i], fbase + F_CB * 32768, (unsigned)(i * 64 + lane) * 128u, tag);
        st_flag(wsr, WS_GEN, tag);
    }
}

extern "C" void kernel_launch(void* const* d_in, const int* in_sizes, int n_in, void* d_out, int out_size, void* d_ws, size_t ws_size, hipStream_t stream) {
    const size_t WS_OFF = 0;
    (void)hipFuncSetAttribute((const void*)fused_kernel, hipFuncAttributeMaxDynamicSharedMemorySize, LDS_BYTES);
    fused_kernel<<<256, 512, LDS_BYTES, stream>>>((const u16*)d_in[0], (const u16*)d_in[1], (const u16*)d_in[2], (const u16*)d_in[3], (const u16*)d_in[4], (const u16*)d_in[5],
                                                   (const u16*)d_in[7], (const u16*)d_in[8], (const u16*)d_in[9], (char*)d_ws + WS_OFF, (u16*)d_out);
}
```
